```python
import torch

def _softmax(x, axis=-1):
    return torch.softmax(x, dim=axis)
def _gelu(x):
    return torch.nn.functional.gelu(x, approximate="tanh")
def _silu(x):
    return torch.nn.functional.silu(x)
def _sigmoid(x):
    return torch.sigmoid(x)
def _layernorm(x, axis=-1, eps=1e-5):
    m = x.mean(dim=axis, keepdim=True)
    v = x.var(dim=axis, keepdim=True, unbiased=False)
    return (x - m) / torch.sqrt(v + eps)
def _scatter_add(tgt, idx, src, axis=0):
    return tgt.clone().index_add_(axis, idx, src)
def _segment_sum(src, seg, n):
    out = torch.zeros((n,) + src.shape[1:], dtype=src.dtype)
    return out.index_add_(0, seg, src)
def _pad(x, pw):
    flat = tuple(v for pair in reversed(pw) for v in pair)
    return torch.nn.functional.pad(x, flat)

kernel_name = 'gated_mix__grouped_kv_mqa__ssm_conv_s5__prompt'

def setup_inputs():

    _w = (lambda a: (lambda t: t.to(torch.bfloat16) if t.is_floating_point() else t)(torch.as_tensor(a)))
    def _A_setup():
        B, S, D, H = 1, 2048, 448, 16
        Dh = D // H
        return {
            "x": _w((lambda *s: torch.randn(*s, dtype=torch.bfloat16) * 0.05)(B, S, D)),
            "wq": _w((lambda *s: torch.randn(*s, dtype=torch.bfloat16) * 1.6)(D, H * Dh)),
            "wk": _w((lambda *s: torch.randn(*s, dtype=torch.bfloat16) * 1.6)(D, Dh)),
            "wv": _w((lambda *s: torch.randn(*s, dtype=torch.bfloat16) * 0.05)(D, Dh)),
            "wo": _w((lambda *s: torch.randn(*s, dtype=torch.bfloat16) * 0.05)(H * Dh, D)),
        }
    _A_in = _A_setup()

    def _B_setup():
        B, S, D = 1, 2048, 448
        P = D
        Lambda_re = -0.5 * (lambda _x: torch.exp(torch.as_tensor(_x)))((lambda *s: torch.randn(*s, dtype=torch.bfloat16) * 0.4)(P))
        Lambda_im = (lambda *s: torch.randn(*s, dtype=torch.bfloat16) * 0.8)(P) * 1.0
        B_re = (lambda *s: torch.randn(*s, dtype=torch.bfloat16) * 0.4)(P, D) * (D ** -0.5)
        B_im = (lambda *s: torch.randn(*s, dtype=torch.bfloat16) * 0.4)(P, D) * (D ** -0.5)
        C_re = (lambda *s: torch.randn(*s, dtype=torch.bfloat16) * 0.4)(D, P) * (P ** -0.5)
        C_im = (lambda *s: torch.randn(*s, dtype=torch.bfloat16) * 0.4)(D, P) * (P ** -0.5)
        log_dt = (lambda *s: torch.randn(*s, dtype=torch.bfloat16) * 0.4)(P) * 0.3 - 2.0
        Dskip = (lambda *s: torch.randn(*s, dtype=torch.bfloat16) * 0.2)(D)
        return {
            "x": _w((lambda *s: torch.randn(*s, dtype=torch.bfloat16) * 0.05)(B, S, D)),
            "Lambda_re": _w(Lambda_re),
            "Lambda_im": _w(Lambda_im),
            "B_re": _w(B_re),
            "B_im": _w(B_im),
            "C_re": _w(C_re),
            "C_im": _w(C_im),
            "log_dt": _w(log_dt),
            "Dskip": _w(Dskip),
        }
    _B_in = _B_setup()

    return {
        "A_x": _A_in["x"],
        "A_wq": _A_in["wq"],
        "A_wk": _A_in["wk"],
        "A_wv": _A_in["wv"],
        "A_wo": _A_in["wo"],
        "B_x": _B_in["x"],
        "B_Lambda_re": _B_in["Lambda_re"],
        "B_Lambda_im": _B_in["Lambda_im"],
        "B_B_re": _B_in["B_re"],
        "B_B_im": _B_in["B_im"],
        "B_C_re": _B_in["C_re"],
        "B_C_im": _B_in["C_im"],
        "B_log_dt": _B_in["log_dt"],
        "B_Dskip": _B_in["Dskip"],
        "gate_w": _w((lambda *s: torch.randn(*s, dtype=torch.bfloat16) * 0.05)(448) * 16.0),
    }

def reference(A_x, A_wq, A_wk, A_wv, A_wo, B_x, B_Lambda_re, B_Lambda_im, B_B_re, B_B_im, B_C_re, B_C_im, B_log_dt, B_Dskip, gate_w):
    def _A_ref(x, wq, wk, wv, wo):
        B, S, D = x.shape
        H = 16
        Dh = D // H
        q = (lambda x, *d: x.permute(*d))((x @ wq).reshape(B, S, H, Dh), 0, 2, 1, 3)
        k = x @ wk
        v = x @ wv
        scores = torch.einsum("bhqd,bkd->bhqk", q, k) * (Dh ** -0.5)
        p = _softmax(scores, axis=-1)
        o = torch.einsum("bhqk,bkd->bhqd", p, v)
        return (lambda x, *d: x.permute(*d))(o, 0, 2, 1, 3).reshape(B, S, H * Dh) @ wo

    def _B_ref(x, Lambda_re, Lambda_im, B_re, B_im, C_re, C_im, log_dt, Dskip):
        Bsz, L, H = x.shape
        Lam = Lambda_re + 1j * Lambda_im
        Bmat = B_re + 1j * B_im
        Cmat = C_re + 1j * C_im
        dt = torch.exp(log_dt)
        Lam_bar = torch.exp(dt * Lam)
        B_bar = ((Lam_bar - 1.0) / Lam)[:, None] * Bmat
        Bu = torch.einsum("bld,pd->blp", x + 0j, B_bar)
        h = torch.zeros_like(Bu[:, 0, :])
        hs = []
        for t in range(L):
            h = Lam_bar[None, :] * h + Bu[:, t, :]
            hs.append(h)
        hseq = torch.stack(hs, 1)
        y = 2.0 * torch.einsum("blp,dp->bld", hseq, Cmat).real + x * Dskip[None, None, :]
        return y

    x = A_x
    g = _sigmoid((x * gate_w).sum(-1, keepdims=True))
    a = _A_ref(x, A_wq, A_wk, A_wv, A_wo)
    b = _B_ref(x, B_Lambda_re, B_Lambda_im, B_B_re, B_B_im, B_C_re, B_C_im, B_log_dt, B_Dskip)
    return g * a + (1 - g) * b
```

```cpp
#include <hip/hip_runtime.h>
#include <hip/hip_bf16.h>
#include <stdint.h>

typedef __bf16 bf16x8 __attribute__((ext_vector_type(8)));
typedef __bf16 bf16x2 __attribute__((ext_vector_type(2)));
typedef float f32x16 __attribute__((ext_vector_type(16)));
typedef float f32x4 __attribute__((ext_vector_type(4)));
typedef float f32x2 __attribute__((ext_vector_type(2)));
typedef unsigned u32x4 __attribute__((ext_vector_type(4)));
typedef unsigned u32x2 __attribute__((ext_vector_type(2)));
typedef short s16x4 __attribute__((ext_vector_type(4)));
typedef short s16x8 __attribute__((ext_vector_type(8)));
typedef __amdgpu_buffer_rsrc_t rsrc_t;
#define LAS __attribute__((address_space(3)))
#define DI __device__ __forceinline__
#define SBAR() __builtin_amdgcn_sched_barrier(0)
#define PIN(x) asm volatile("" : "+v"(x))
#define MFMA32(a, b, c) __builtin_amdgcn_mfma_f32_32x32x16_bf16((a), (b), (c), 0, 0, 0)
#define MFMA16(a, b, c) __builtin_amdgcn_mfma_f32_16x16x32_bf16((a), (b), (c), 0, 0, 0)
#define SC1 16

#define MAP42 1

constexpr int SEQ = 2048, DM = 448, ROWB = DM * 2;
constexpr unsigned WS_SYNC = 0, WS_SYNC_BYTES = 24576;
constexpr unsigned WS_KVCNT = 0;
constexpr unsigned WS_HCNT = 16384;
constexpr unsigned WS_KV = 65536;
constexpr unsigned WS_E = WS_KV + 262144;
constexpr unsigned WS_H = WS_E + 131072;
constexpr unsigned WS_O = WS_H + 32 * 114688;
constexpr unsigned WS_END = WS_O + 32 * 65536;
constexpr unsigned WS_STAMP = WS_END;

constexpr unsigned L_CH = 30720, L_CHW = 8192, L_CHB = 16384;
constexpr unsigned L_CRW = 122880, L_CRX = 148480;
constexpr unsigned L_RING = 106496;
constexpr unsigned L_QSH = 155648, L_G = 100352, L_FLAG = 100608;
constexpr unsigned L_C = 0, L_CIM = 50176, L_HSCR = 155648;
constexpr unsigned L_WO = 106496, L_MRG = 146432, L_LSUM = 162816, L_RED = 0;
constexpr unsigned LDS_BYTES = 163840;
constexpr unsigned SPIN_CAP = 1u << 14;

DI unsigned cvt2(float a, float b) { f32x2 v = {a, b}; return __builtin_bit_cast(unsigned, __builtin_convertvector(v, bf16x2)); }
DI float bf2f(unsigned h) { return __uint_as_float(h << 16); }
DI float bfround(float x) { return __uint_as_float(cvt2(x, 0.f) << 16); }
DI rsrc_t mkrs(const void* p, unsigned bytes) { return __builtin_amdgcn_make_buffer_rsrc((void*)p, (short)0, (int)bytes, 0x00020000); }
DI void dma(rsrc_t rs, unsigned voff, unsigned soff, unsigned lds) {
    asm volatile("s_mov_b32 m0, %0\n\ts_nop 0\n\tbuffer_load_dwordx4 %1, %2, %3 offen lds" :: "s"(lds), "v"(voff), "s"(rs), "s"(soff) : "memory"); }
DI void dma_sc1(rsrc_t rs, unsigned voff, unsigned soff, unsigned lds) {
    asm volatile("s_mov_b32 m0, %0\n\ts_nop 0\n\tbuffer_load_dwordx4 %1, %2, %3 offen sc1 lds" :: "s"(lds), "v"(voff), "s"(rs), "s"(soff) : "memory"); }
template <int N> DI void wait_bar() { asm volatile("s_waitcnt vmcnt(%0) lgkmcnt(0)\n\ts_barrier" :: "i"(N) : "memory"); }
template <int N> DI void wait_vm() { asm volatile("s_waitcnt vmcnt(%0)" :: "i"(N) : "memory"); }
DI void lds_bar() { asm volatile("s_waitcnt lgkmcnt(0)\n\ts_barrier" ::: "memory"); }
DI bf16x8 ld128(LAS char* L, unsigned off) { return __builtin_bit_cast(bf16x8, *(LAS const u32x4*)(L + off)); }
DI s16x4 ldtr(LAS char* L, unsigned off) { return __builtin_amdgcn_ds_read_tr16_b64_v4i16((LAS s16x4*)(L + off)); }
DI bf16x8 ldtr2(LAS char* L, unsigned off0, unsigned off1) {
    s16x4 a = ldtr(L, off0), b = ldtr(L, off1); return __builtin_bit_cast(bf16x8, __builtin_shufflevector(a, b, 0, 1, 2, 3, 4, 5, 6, 7)); }
DI bf16x8 asbf(u32x4 v) { return __builtin_bit_cast(bf16x8, v); }
DI unsigned nosent(unsigned v) { return v < 0xFFFFFFFEu ? v : 0xFFFFFFFEu; }
DI unsigned atomic_read(rsrc_t rs, unsigned voff) { return (unsigned)__builtin_amdgcn_raw_ptr_buffer_atomic_add_i32(0, rs, (int)voff, 0, SC1); }
DI void atomic_inc(rsrc_t rs, unsigned voff) { asm volatile("buffer_atomic_add %0, %1, %2, 0 offen sc1" :: "v"(1u), "v"(voff), "s"(rs) : "memory"); }
struct cf { float r, i; };
DI cf cmul(cf a, cf b) { return {a.r * b.r - a.i * b.i, a.r * b.i + a.i * b.r}; }
DI cf cfma(cf a, cf b, cf c) { return {__builtin_fmaf(a.r, b.r, __builtin_fmaf(-a.i, b.i, c.r)), __builtin_fmaf(a.r, b.i, __builtin_fmaf(a.i, b.r, c.i))}; }
DI float shfl(float v, int src) { return __int_as_float(__builtin_amdgcn_ds_bpermute(src << 2, __float_as_int(v))); }
DI float rowmax16(const f32x16& s) {
    float a = fmaxf(fmaxf(s[0], s[1]), s[2]), b = fmaxf(fmaxf(s[3], s[4]), s[5]);
    a = fmaxf(fmaxf(a, s[6]), s[7]); b = fmaxf(fmaxf(b, s[8]), s[9]); a = fmaxf(fmaxf(a, s[10]), s[11]); b = fmaxf(fmaxf(b, s[12]), s[13]);
    a = fmaxf(fmaxf(a, s[14]), s[15]); a = fmaxf(a, b);
    auto rr = __builtin_amdgcn_permlane32_swap(__float_as_uint(a), __float_as_uint(a), false, false);
    return fmaxf(__uint_as_float(rr[0]), __uint_as_float(rr[1])); }
__host__ __device__ constexpr int kvpos(int n, int sub) {
    return sub == 0 ? (n < 2 ? 2 * n : 4 * n - 3) : sub == 1 ? (n < 2 ? 2 * n + 1 : 4 * n - 1)
         : sub == 2 ? (n == 0 ? 4 : n == 31 ? 126 : 4 * n + 4) : (n == 30 ? 125 : n == 31 ? 127 : 4 * n + 6); }
__host__ __device__ constexpr unsigned kvlds(int pos) { return (((pos >> 3) + 1) % 3) * 16384 + (pos & 7) * 1024; }
DI unsigned kvglob(int half, int pos) { return WS_KV + (pos >> 3) * 16384 + half * 8192 + (pos & 7) * 1024; }
DI void set_shift(u32x4& q1, float c, int hi) {
    const float c1 = bfround(c), c2 = bfround(c - c1), c3 = c - c1 - c2;
    if (hi) { q1[2] = cvt2(-c1, -c2); q1[3] = cvt2(-c3, 0.f); } }

#define STAMP(k)

__global__ void __launch_bounds__(512, 2)
gated_mix_kernel(const uint16_t* __restrict__ x, const uint16_t* __restrict__ wq, const uint16_t* __restrict__ wk, const uint16_t* __restrict__ wv,
                 const uint16_t* __restrict__ wo, const uint16_t* __restrict__ lre, const uint16_t* __restrict__ lim,
                 const uint16_t* __restrict__ bre, const uint16_t* __restrict__ bim, const uint16_t* __restrict__ cre, const uint16_t* __restrict__ cim,
                 const uint16_t* __restrict__ logdt, const uint16_t* __restrict__ dskip, const uint16_t* __restrict__ gate,
                 float* __restrict__ out, char* __restrict__ ws) {
    __shared__ __attribute__((aligned(16))) char smem[LDS_BYTES];
    LAS char* L = (LAS char*)smem;
    const unsigned l0 = (unsigned)(uintptr_t)L;
    const int lane = threadIdx.x & 63, w = __builtin_amdgcn_readfirstlane(threadIdx.x >> 6);
    const int b = blockIdx.x;
    const int blk = ((b & 7) >> 1) * 8 + (b >> 5), j = (b & 1) * 4 + ((b >> 3) & 3);
    const int th = w & 1, hd = (w >> 1) & 1, kh = w >> 2, kq = w >> 1;
    const int r32 = lane & 31, hi = lane >> 5, r16 = lane & 15, G = lane >> 4;
    const int tq = r16 >> 2, tp = lane & 3;
    const rsrc_t rsX = mkrs(x, SEQ * ROWB), rsWS = mkrs(ws, WS_END);
    STAMP(0);

    {
        const u32x4 ones = {~0u, ~0u, ~0u, ~0u};
        __builtin_amdgcn_raw_buffer_store_b128(ones, rsWS, lane * 16, WS_O + blk * 65536 + ((th * 16 + 2 * j + hd) * 2 + kh) * 1024, SC1);
        if (w >= 4 && G == 0 && (w - 4) * 16 + r16 < 56)
            __builtin_amdgcn_raw_buffer_store_b64((u32x2){~0u, ~0u}, rsWS, ((w - 4) * 16 + r16) * 8, WS_E + (blk * DM + 56 * j) * 8, SC1);
    }
    unsigned prm0 = 0, prm1 = 0, prm2 = 0, prm3 = 0, prm4 = 0, prm5 = 0, prm6 = 0;
    if (w == 0) {
        const uint16_t* gp = gate + lane;
        asm volatile("global_load_ushort %0, %7, off\n\tglobal_load_ushort %1, %7, off offset:128\n\tglobal_load_ushort %2, %7, off offset:256\n\t"
                     "global_load_ushort %3, %7, off offset:384\n\tglobal_load_ushort %4, %7, off offset:512\n\tglobal_load_ushort %5, %7, off offset:640\n\t"
                     "global_load_ushort %6, %7, off offset:768"
                     : "=&v"(prm0), "=&v"(prm1), "=&v"(prm2), "=&v"(prm3), "=&v"(prm4), "=&v"(prm5), "=&v"(prm6) : "v"(gp) : "memory");
    } else if (w >= 4) {
        const int st = 56 * j + min((w - 4) * 16 + r16, 55);
        asm volatile("global_load_ushort %0, %3, off\n\tglobal_load_ushort %1, %4, off\n\tglobal_load_ushort %2, %5, off"
                     : "=&v"(prm0), "=&v"(prm1), "=&v"(prm2) : "v"(lre + st), "v"(lim + st), "v"(logdt + st) : "memory");
    }
    const int kind = j & 1, tg = j >> 1;
    {
        const rsrc_t rsWK = mkrs(kind ? wv : wk, DM * 56);
        dma(rsWK, lane * 16, w * 1024, l0 + L_CRW + w * 1024);
        dma(rsWK, lane * 16, (8 + w) * 1024, l0 + L_CRW + (8 + w) * 1024);
        dma(rsWK, lane * 16, (16 + w) * 1024, l0 + L_CRW + (16 + w) * 1024);
#pragma unroll
        for (int n = 0; n < 2; ++n) {
            const int p = min(n * 8 + w, 13), s = p * 64 + lane, row = s / 56, sl = s - row * 56;
            const unsigned voff = row * ROWB + (((sl & ~7) | ((sl & 7) ^ ((row >> 1) & 7))) << 4);
            if (n == 1 && w == 7) dma(rsWK, lane * 16, 24 * 1024, l0 + L_CRW + 24 * 1024);
            else dma(rsX, voff, (blk * 64 + tg * 16) * ROWB, l0 + L_CRX + p * 1024);
        }
    }
    asm volatile("s_barrier" ::: "memory");
    const rsrc_t rsWQ = mkrs(wq, DM * ROWB), rsBR = mkrs(bre, DM * ROWB), rsBI = mkrs(bim, DM * ROWB);
    const int v = w - 1;
    unsigned vx, vw, vb, vx7, vw7; bool wok, wok7;
    {
        const int sl = lane & 7, r8 = lane >> 3;
        { const int row = 8 * v + r8; const int f = ((row >> 1) & 7) ^ ((row & 16) ? 7 : 0) ^ ((row & 32) ? 6 : 0);
          vx = (blk * 64 + row) * ROWB + ((sl ^ f) << 4);
          const int pw = sl ^ (((row >> 1) & 1) << 2); vw = row * ROWB + 112 * j + (pw << 4); wok = pw != 7;
          vb = (56 * j + row) * ROWB + ((sl ^ ((row >> 1) & 7)) << 4); }
        { const int row = 56 + r8; const int f = ((row >> 1) & 7) ^ 7 ^ 6;
          vx7 = (blk * 64 + row) * ROWB + ((sl ^ f) << 4);
          const int pw = sl ^ (((row >> 1) & 1) << 2); vw7 = row * ROWB + 112 * j + (pw << 4); wok7 = pw != 7; }
    }
    auto issue_chunk = [&](int c) {
        const unsigned s = l0 + (c & 3) * L_CH;
        dma(rsX, vx, c * 128, s + v * 1024);
        if (wok) dma(rsWQ, vw, c * 64 * ROWB, s + L_CHW + v * 1024);
        dma(rsBR, vb, c * 128, s + L_CHB + v * 1024);
        dma(rsBI, vb, c * 128, s + L_CHB + 7168 + v * 1024);
        if (w == 1) dma(rsX, vx7, c * 128, s + 7 * 1024);
        if (w == 2) { if (wok7) dma(rsWQ, vw7, c * 64 * ROWB, s + L_CHW + 7 * 1024); }
    };
    if (w >= 1) { issue_chunk(0); issue_chunk(1); issue_chunk(2); issue_chunk(3); }
#define WAITN(k) do { if (w == 0) { } else if (w <= 2) wait_vm<5 * (k)>(); else wait_vm<4 * (k)>(); } while (0)
    if (w == 0) wait_vm<0>(); else WAITN(4);
    asm volatile("" : "+v"(prm0), "+v"(prm1), "+v"(prm2), "+v"(prm3), "+v"(prm4), "+v"(prm5), "+v"(prm6));
    lds_bar();
    STAMP(1);

    auto gate_write = [&](int c, unsigned g16) {
        *(LAS uint16_t*)(L + (c & 3) * L_CH + L_CHW + lane * 128 + (112 ^ (((lane >> 1) & 1) << 6))) = (uint16_t)g16; };
    cf lam = {0.f, 0.f}, coef = {0.f, 0.f}, lam8 = {0.f, 0.f}, lam16 = {0.f, 0.f}, lam64 = {0.f, 0.f}, wlo = {0.f, 0.f}, zG = {0.f, 0.f};
    if (w == 0) {
        gate_write(0, prm0); gate_write(1, prm1); gate_write(2, prm2); gate_write(3, prm3);
        const int cg0 = kind ? tp : 4 * (tp >> 1) + (tp & 1), cg1 = kind ? 4 + tp : 4 * (tp >> 1) + 2 + (tp & 1);
        const unsigned aw = L_CRW + (8 * G + tq) * 56, ax = L_CRX + r16 * ROWB;
        f32x4 d0 = {0.f, 0.f, 0.f, 0.f}, d1 = d0;
#pragma unroll
        for (int ks = 0; ks < 14; ++ks) {
            const int pc = 4 * ks + G;
            const bf16x8 xf = ld128(L, ax + (((pc & ~7) | ((pc & 7) ^ ((r16 >> 1) & 7))) << 4));
            const bf16x8 w0 = ldtr2(L, aw + ks * 32 * 56 + cg0 * 8, aw + (ks * 32 + 4) * 56 + cg0 * 8);
            const bf16x8 w1 = ldtr2(L, aw + ks * 32 * 56 + cg1 * 8, aw + (ks * 32 + 4) * 56 + cg1 * 8);
            if (kind) { d0 = MFMA16(xf, w0, d0); d1 = MFMA16(xf, w1, d1); } else { d0 = MFMA16(w0, xf, d0); d1 = MFMA16(w1, xf, d1); }
        }
        const int T = 2 * blk + (tg >> 1), sg = tg & 1, half = T >> 5, n = T & 31;
        if (kind == 0) {
            u32x4 kv = {cvt2(d0[0], d0[1]), cvt2(d0[2], d0[3]), cvt2(d1[0], d1[1]), cvt2(d1[2], d1[3])};
            if (G == 3) { kv[2] = 0x3F803F80u; kv[3] = 0x00003F80u; }
            const int row = 16 * sg + ((r16 & 3) | ((r16 & 4) << 1) | ((r16 & 8) >> 1));
            const unsigned base = (G >> 1) ? kvglob(half, kvpos(n, 1)) : kvglob(half, kvpos(n, 0));
            __builtin_amdgcn_raw_buffer_store_b128(kv, rsWS, base + ((G & 1) * 32 + row) * 16, 0, SC1);
        } else {
            u32x2 v0 = {cvt2(d0[0], d0[1]), cvt2(d0[2], d0[3])}, v1 = {cvt2(d1[0], d1[1]), cvt2(d1[2], d1[3])};
            if (r16 >= 12) { const unsigned o = r16 == 12 ? 0x3F803F80u : 0u; v1 = (u32x2){o, o}; }
            const unsigned base = kvglob(half, kvpos(n, 2 + sg)), a = ((G >> 1) * 32 + r16) * 16 + (G & 1) * 8;
            __builtin_amdgcn_raw_buffer_store_b64(v0, rsWS, a, base, SC1);
            __builtin_amdgcn_raw_buffer_store_b64(v1, rsWS, a + 256, base, SC1);
        }
    } else if (w >= 4) {
        const float lr = bf2f(prm0), li = bf2f(prm1), dt = bfround(expf(bf2f(prm2)));
        const float er = expf(dt * lr); float sn, cs; sincosf(dt * li, &sn, &cs);
        lam = {er * cs, er * sn};
        const float inv = 1.0f / (lr * lr + li * li);
        coef = {((lam.r - 1.0f) * lr + lam.i * li) * inv, (lam.i * lr - (lam.r - 1.0f) * li) * inv};
        const cf l2 = cmul(lam, lam), l4 = cmul(l2, l2); lam8 = cmul(l4, l4); lam16 = cmul(lam8, lam8);
        const cf l32 = cmul(lam16, lam16); lam64 = cmul(l32, l32);
        const cf l128 = cmul(lam64, lam64), l256 = cmul(l128, l128), l512 = cmul(l256, l256), l1024 = cmul(l512, l512);
        const cf one = {1.f, 0.f};
        wlo = cmul((G & 1) ? lam16 : one, (G & 2) ? l32 : one);
        zG = cmul((G & 1) ? l512 : one, (G & 2) ? l1024 : one);
    }

    f32x16 qa = {};
    f32x4 ur[4] = {}, ui[4] = {};
    unsigned a0, a1, a2, a3, a4;
    if (w < 4) {
        const int row = 32 * th + r32, f = ((row >> 1) & 7) ^ ((row & 16) ? 7 : 0) ^ ((row & 32) ? 6 : 0);
        a0 = row * 128 + ((hi ^ f) << 4);
        a1 = L_CHW + (8 * hi + tq) * 128 + ((56 * hd + 32 * (G & 1) + 8 * tp) ^ (((tq >> 1) & 1) << 6));
        a2 = a3 = a4 = 0;
    } else {
        const int rb = min(16 * (w - 4) + r16, 55);
        a4 = L_CHB + rb * 128 + ((G ^ ((rb >> 1) & 7)) << 4);
        unsigned t[4];
#pragma unroll
        for (int m = 0; m < 4; ++m) { const int row = 16 * (r16 >> 2) + 2 * m + ((r16 & 3) >> 1) + 8 * (r16 & 1);
            const int f = ((row >> 1) & 7) ^ ((row & 16) ? 7 : 0) ^ ((row & 32) ? 6 : 0); t[m] = row * 128 + ((G ^ f) << 4); }
        a0 = t[0]; a1 = t[1]; a2 = t[2]; a3 = t[3];
    }
    auto chunk = [&](int c) {
        const unsigned s = (c & 3) * L_CH;
        if (w < 4) {
#pragma unroll
            for (int ks = 0; ks < 4; ++ks) {
                const bf16x8 xf = ld128(L, s + (a0 ^ (ks << 5)));
                const bf16x8 wf = ldtr2(L, s + a1 + ks * 16 * 128, s + a1 + (ks * 16 + 4) * 128);
                qa = MFMA32(wf, xf, qa);
            }
        } else {
#pragma unroll
            for (int ks = 0; ks < 2; ++ks) {
                const bf16x8 br = ld128(L, s + (a4 ^ (ks << 6))), bi = ld128(L, s + 7168 + (a4 ^ (ks << 6)));
                const bf16x8 x0 = ld128(L, s + (a0 ^ (ks << 6))), x1 = ld128(L, s + (a1 ^ (ks << 6)));
                const bf16x8 x2 = ld128(L, s + (a2 ^ (ks << 6))), x3 = ld128(L, s + (a3 ^ (ks << 6)));
                ur[0] = MFMA16(x0, br, ur[0]); ui[0] = MFMA16(x0, bi, ui[0]); ur[1] = MFMA16(x1, br, ur[1]); ui[1] = MFMA16(x1, bi, ui[1]);
                ur[2] = MFMA16(x2, br, ur[2]); ui[2] = MFMA16(x2, bi, ui[2]); ur[3] = MFMA16(x3, br, ur[3]); ui[3] = MFMA16(x3, bi, ui[3]);
            }
        }
    };
    WAITN(3); lds_bar(); chunk(0);
    WAITN(2); lds_bar(); if (w >= 1) issue_chunk(4); else gate_write(4, prm4); chunk(1);
    if (w == 0) {
        wait_vm<0>();
        if (lane < 8) atomic_inc(rsWS, WS_KVCNT + lane * 2048 + (b & 15) * 128);
        STAMP(2);
    }
    WAITN(2); lds_bar(); if (w >= 1) issue_chunk(5); else gate_write(5, prm5); chunk(2);
    WAITN(2); lds_bar(); if (w >= 1) issue_chunk(6); else gate_write(6, prm6); chunk(3);
    WAITN(2); lds_bar(); chunk(4);
    WAITN(1); lds_bar(); chunk(5);
    WAITN(0); lds_bar(); chunk(6);
    STAMP(3);

    u32x4 q0, q1;
    f32x2 inr = {0.f, 0.f}, ini = inr;
    cf whi = {0.f, 0.f};
    if (w < 4) {
        const float cs = 0.18898223650461361f * 1.4426950408889634f;
        if (hd == 1 && hi) *(LAS float*)(L + L_G + (32 * th + r32) * 4) = 1.0f / (1.0f + expf(-qa[12]));
        if (w == 0 && lane == 0) *(LAS unsigned*)(L + L_FLAG) = 0u;
        q0 = (u32x4){cvt2(qa[0] * cs, qa[1] * cs), cvt2(qa[2] * cs, qa[3] * cs), cvt2(qa[4] * cs, qa[5] * cs), cvt2(qa[6] * cs, qa[7] * cs)};
        q1 = (u32x4){cvt2(qa[8] * cs, qa[9] * cs), cvt2(qa[10] * cs, qa[11] * cs), cvt2(qa[12] * cs, qa[13] * cs), cvt2(qa[14] * cs, qa[15] * cs)};
        if (hi) { q1[2] = 0u; q1[3] = 0u; }
        *(LAS u32x4*)(L + L_QSH + w * 2048 + lane * 16) = q0;
        *(LAS u32x4*)(L + L_QSH + w * 2048 + 1024 + lane * 16) = q1;
        if (w == 1) {
            for (unsigned spin = 0; spin < SPIN_CAP; ++spin) {
                unsigned c = 16;
                if (lane < 16) c = atomic_read(rsWS, WS_KVCNT + (b & 7) * 2048 + lane * 128);
                if (__all(c == 16)) break;
            }
        }
    } else {
        q0 = q1 = (u32x4){0u, 0u, 0u, 0u};
        f32x2 hr = {0.f, 0.f}, hm = hr;
#pragma unroll
        for (int p = 0; p < 8; ++p) {
            const f32x2 a = {ur[p >> 1][2 * (p & 1)], ur[p >> 1][2 * (p & 1) + 1]}, c = {ui[p >> 1][2 * (p & 1)], ui[p >> 1][2 * (p & 1) + 1]};
            const f32x2 tr = coef.r * a - coef.i * c, ti = coef.r * c + coef.i * a;
            ur[p >> 1][2 * (p & 1)] = tr[0]; ur[p >> 1][2 * (p & 1) + 1] = tr[1]; ui[p >> 1][2 * (p & 1)] = ti[0]; ui[p >> 1][2 * (p & 1) + 1] = ti[1];
            const f32x2 nr = lam.r * hr - lam.i * hm + tr, ni = lam.r * hm + lam.i * hr + ti; hr = nr; hm = ni;
        }
        const cf elo = {hr[0], hm[0]}, e16 = cfma(lam8, elo, (cf){hr[1], hm[1]});
        const cf e0 = {shfl(e16.r, r16), shfl(e16.i, r16)}, e1 = {shfl(e16.r, r16 + 16), shfl(e16.i, r16 + 16)};
        const cf e2 = {shfl(e16.r, r16 + 32), shfl(e16.i, r16 + 32)}, e3 = {shfl(e16.r, r16 + 48), shfl(e16.i, r16 + 48)};
        const cf p2 = cfma(lam16, e0, e1), p3 = cfma(lam16, p2, e2), eb = cfma(lam16, p3, e3);
        const cf pg = G == 0 ? (cf){0.f, 0.f} : G == 1 ? e0 : G == 2 ? p2 : p3, ph = cfma(lam8, pg, elo);
        inr = (f32x2){pg.r, ph.r}; ini = (f32x2){pg.i, ph.i}; whi = cmul(wlo, lam8);
        if (G == 0 && (w - 4) * 16 + r16 < 56)
            __builtin_amdgcn_raw_buffer_store_b64((u32x2){nosent(__float_as_uint(eb.r)), nosent(__float_as_uint(eb.i))}, rsWS, ((w - 4) * 16 + r16) * 8,
                                                  WS_E + (blk * DM + 56 * j) * 8, SC1);
    }
    STAMP(4);
    lds_bar();
    STAMP(5);

    const unsigned l16 = lane * 16;
#define KVDMA(phi) do { dma_sc1(rsWS, l16, WS_KV + (phi) * 16384 + w * 2048, l0 + L_RING + (((phi) + 1) % 3) * 16384 + w * 2048); \
                        dma_sc1(rsWS, l16, WS_KV + (phi) * 16384 + w * 2048 + 1024, l0 + L_RING + (((phi) + 1) % 3) * 16384 + w * 2048 + 1024); } while (0)
    KVDMA(0); KVDMA(1); KVDMA(2);
    if (w >= 4) { q0 = *(LAS const u32x4*)(L + L_QSH + (w - 4) * 2048 + l16); q1 = *(LAS const u32x4*)(L + L_QSH + (w - 4) * 2048 + 1024 + l16); }
    const float gt = *(LAS const float*)(L + L_G + (32 * th + r32) * 4);
    const rsrc_t rsCR = mkrs(cre, DM * ROWB), rsCI = mkrs(cim, DM * ROWB);
    const unsigned vc0 = (56 * j + (lane >> 3)) * ROWB + (((lane & 7) ^ (lane >> 4)) << 4), vc1 = vc0 ^ 64;
    auto cdma = [&](int t) {
        const int p = min(t * 8 + w, 97), pl = p >= 49, q = p - 49 * pl, cc = q / 7, pr = q - 7 * cc;
        const unsigned so = pr * 8 * ROWB + cc * 128, ld = l0 + L_C + p * 1024;
        if (pl) dma(rsCI, (pr & 1) ? vc1 : vc0, so, ld); else dma(rsCR, (pr & 1) ? vc1 : vc0, so, ld);
    };
    const unsigned kvb = L_RING + kh * 8192 + l16;
    wait_bar<4>();
    STAMP(6);
    f32x16 sA, sB, o;
    bf16x8 k0, k1, v0, v1;
    u32x4 pw0, pw1;
    {
        const bf16x8 c0 = ld128(L, L_RING + l16 + kvlds(0)), c1 = ld128(L, L_RING + l16 + kvlds(1));
        k0 = ld128(L, kvb + kvlds(0)); k1 = ld128(L, kvb + kvlds(1));
        sB = MFMA32(c0, asbf(q0), (f32x16){}); sB = MFMA32(c1, asbf(q1), sB);
        set_shift(q1, rowmax16(sB), hi);
        sA = MFMA32(k0, asbf(q0), (f32x16){}); sA = MFMA32(k1, asbf(q1), sA);
        k0 = ld128(L, kvb + kvlds(2)); k1 = ld128(L, kvb + kvlds(3)); v0 = ld128(L, kvb + kvlds(4));
    }
    u32x2 ev[8];
    u32x4 hf[14];
    unsigned hc = 0;
#define EX4(A, i) do { A[i] = __builtin_amdgcn_exp2f(A[i]); A[i + 1] = __builtin_amdgcn_exp2f(A[i + 1]); \
                       A[i + 2] = __builtin_amdgcn_exp2f(A[i + 2]); A[i + 3] = __builtin_amdgcn_exp2f(A[i + 3]); PIN(A); } while (0)
#define ITER(A, B, n, HOOK) do { SBAR(); \
    if ((n) < 31) B = MFMA32(k0, asbf(q0), (f32x16){}); \
    if ((n) < 30) k0 = ld128(L, kvb + kvlds(kvpos((n) + 2, 0))); \
    EX4(A, 0); SBAR(); \
    if ((n) > 0) o = MFMA32(v1, asbf(pw1), o); \
    v1 = ld128(L, kvb + kvlds(kvpos((n), 3))); \
    pw0[0] = cvt2(A[0], A[1]); pw0[1] = cvt2(A[2], A[3]); PIN(pw0); EX4(A, 4); SBAR(); \
    if ((n) < 31) B = MFMA32(k1, asbf(q1), B); \
    if ((n) < 30) k1 = ld128(L, kvb + kvlds(kvpos((n) + 2, 1))); \
    pw0[2] = cvt2(A[4], A[5]); pw0[3] = cvt2(A[6], A[7]); PIN(pw0); EX4(A, 8); SBAR(); \
    if ((n) > 0) o = MFMA32(v0, asbf(pw0), o); else o = MFMA32(v0, asbf(pw0), (f32x16){}); \
    pw1[0] = cvt2(A[8], A[9]); pw1[1] = cvt2(A[10], A[11]); PIN(pw1); EX4(A, 12); SBAR(); \
    HOOK; \
    if ((n) < 31) v0 = ld128(L, kvb + kvlds(kvpos((n) + 1, 2))); \
    pw1[2] = cvt2(A[12], A[13]); pw1[3] = cvt2(A[14], A[15]); PIN(pw1); SBAR(); } while (0)
#define STAGE(phi, N, EXTRA) do { ITER(sA, sB, 2 * (phi) - 2, { wait_bar<N>(); EXTRA; if ((phi) + 2 < 16) KVDMA((phi) + 2); }); ITER(sB, sA, 2 * (phi) - 1, {}); } while (0)
#define CD2(t) do { cdma(t); cdma((t) + 1); } while (0)
    STAGE(1, 2, CD2(0));
    STAGE(2, 4, CD2(2));
    STAGE(3, 4, { CD2(4);
        if (w >= 4) { _Pragma("unroll") for (int m = 0; m < 8; ++m) { const int eb = max(blk - 1 - 8 * G - m, 0);
            ev[m] = __builtin_amdgcn_raw_buffer_load_b64(rsWS, (eb * DM + min((w - 4) * 16 + r16, 55)) * 8, WS_E + 56 * j * 8, SC1); } } });
    STAGE(4, 4, CD2(6));
    if (w >= 4) {
        for (unsigned spin = 0; spin < SPIN_CAP; ++spin) {
            bool ok = true;
#pragma unroll
            for (int m = 0; m < 8; ++m) ok &= (blk - 1 - 8 * G - m < 0) || (ev[m][0] != ~0u && ev[m][1] != ~0u);
            if (__all(ok)) break;
#pragma unroll
            for (int m = 0; m < 8; ++m) { const int eb = max(blk - 1 - 8 * G - m, 0);
                ev[m] = __builtin_amdgcn_raw_buffer_load_b64(rsWS, (eb * DM + min((w - 4) * 16 + r16, 55)) * 8, WS_E + 56 * j * 8, SC1); }
        }
        cf part = {0.f, 0.f};
#pragma unroll
        for (int m = 7; m >= 0; --m) { const bool val = blk - 1 - 8 * G - m >= 0;
            part = cfma(lam64, part, (cf){val ? __uint_as_float(ev[m][0]) : 0.f, val ? __uint_as_float(ev[m][1]) : 0.f}); }
        if (blk - 1 - 8 * G < 0) part = (cf){0.f, 0.f}; else part = cmul(zG, part);
        part.r += shfl(part.r, lane ^ 16); part.i += shfl(part.i, lane ^ 16); part.r += shfl(part.r, lane ^ 32); part.i += shfl(part.i, lane ^ 32);
        const cf ilo = cfma(wlo, part, (cf){inr[0], ini[0]}), ihi = cfma(whi, part, (cf){inr[1], ini[1]});
        f32x2 hr = {ilo.r, ihi.r}, hm = {ilo.i, ihi.i};
        unsigned pr[8], pi[8];
#pragma unroll
        for (int p = 0; p < 8; ++p) {
            const f32x2 a = {ur[p >> 1][2 * (p & 1)], ur[p >> 1][2 * (p & 1) + 1]}, c = {ui[p >> 1][2 * (p & 1)], ui[p >> 1][2 * (p & 1) + 1]};
            const f32x2 nr = lam.r * hr - lam.i * hm + a, ni = lam.r * hm + lam.i * hr + c; hr = nr; hm = ni;
            pr[p] = cvt2(hr[0], hr[1]); pi[p] = cvt2(-hm[0], -hm[1]);
        }
        const unsigned sb = L_HSCR + (w - 4) * 2048, wa = sb + r16 * 128;
        const int tok = (lane & 48) + (r16 >> 1) + 8 * (lane & 1);
        const unsigned ga = (tok >> 5) * 57344 + (tok & 31) * 16, gs = WS_H + blk * 114688 + (7 * j + 2 * (w - 4)) * 1024;
#pragma unroll
        for (int pl = 0; pl < 2; ++pl) {
            const unsigned* s = pl ? pi : pr;
            *(LAS u32x4*)(L + wa + (((2 * G) ^ (r16 & 7)) << 4)) = (u32x4){s[0], s[1], s[2], s[3]};
            *(LAS u32x4*)(L + wa + (((2 * G + 1) ^ (r16 & 7)) << 4)) = (u32x4){s[4], s[5], s[6], s[7]};
            asm volatile("s_waitcnt lgkmcnt(0)" ::: "memory");
#pragma unroll
            for (int kk = 0; kk < 2; ++kk) {
                const unsigned pc = 2 * G + (tp >> 1);
                const bf16x8 c = ldtr2(L, sb + (8 * kk + tq) * 128 + ((pc ^ tq) << 4) + 8 * (tp & 1), sb + (8 * kk + 4 + tq) * 128 + ((pc ^ (4 + tq)) << 4) + 8 * (tp & 1));
                if (kk == 0 || w < 7) __builtin_amdgcn_raw_buffer_store_b128(__builtin_bit_cast(u32x4, c), rsWS, ga + pl * 512 + kk * 1024, gs, SC1);
            }
            asm volatile("s_waitcnt lgkmcnt(0)" ::: "memory");
        }
    }
    STAGE(5, 4, CD2(8));
    STAGE(6, 4, CD2(10));
    STAGE(7, 4, cdma(12));
    STAGE(8, 2, { if (w == 4 && lane == 0) atomic_inc(rsWS, WS_HCNT + blk * 128); });
    STAGE(9, 2, {});
    STAGE(10, 2, {});
    if (w == 0) { for (unsigned spin = 0; spin < SPIN_CAP; ++spin) { hc = 8; if (lane == 0) hc = atomic_read(rsWS, WS_HCNT + blk * 128); if (__all(hc == 8)) break; } }
#define HLD(m) hf[m] = __builtin_amdgcn_raw_buffer_load_b128(rsWS, l16, WS_H + blk * 114688 + th * 57344 + (4 * (m) + kq) * 1024, SC1)
    STAGE(11, 2, { HLD(0); HLD(1); HLD(2); HLD(3); });
    STAGE(12, 6, { HLD(4); HLD(5); HLD(6); HLD(7); });
    STAGE(13, 6, { HLD(8); HLD(9); HLD(10); });
    STAGE(14, 5, { HLD(11); HLD(12); HLD(13); });
    STAGE(15, 3, {});
    ITER(sA, sB, 30, {}); ITER(sB, sA, 31, {});
    o = MFMA32(v1, asbf(pw1), o);
    STAMP(7);

    const rsrc_t rsWO = mkrs(wo, DM * ROWB);
    const unsigned vwo = (lane >> 3) * ROWB + 112 * j + (((lane & 7) ^ (((lane >> 4) & 1) << 2)) << 4);
    auto wodma = [&](int p) { dma(rsWO, vwo, p * 8 * ROWB, l0 + L_WO + p * 1024); };
    auto merge_write = [&]() {
        const int g8 = kh ? 0 : 8;
        *(LAS f32x4*)(L + L_MRG + w * 2048 + l16) = kh ? (f32x4){o[0], o[1], o[2], o[3]} : (f32x4){o[8], o[9], o[10], o[11]};
        *(LAS f32x4*)(L + L_MRG + w * 2048 + 1024 + l16) = kh ? (f32x4){o[4], o[5], o[6], o[7]} : (f32x4){o[12], o[13], o[14], o[15]};
        if (hi) *(LAS float*)(L + L_LSUM + w * 128 + r32 * 4) = o[12];
        (void)g8; };
    { const bool bad = hi && !(o[12] <= 1.8446744e19f);
      if (__any(bad) && lane == 0) *(LAS unsigned*)(L + L_FLAG) = 1u; }
    merge_write();
    lds_bar();
    if (__builtin_expect(__builtin_amdgcn_readfirstlane(*(LAS const unsigned*)(L + L_FLAG)) != 0u, 0)) {
        if (hi) { q1[2] = 0u; q1[3] = 0u; }
        float mx = -INFINITY;
        for (int n = 0; n < 32; ++n) {
            const bf16x8 a = asbf(__builtin_amdgcn_raw_buffer_load_b128(rsWS, l16, kvglob(kh, kvpos(n, 0)), SC1));
            const bf16x8 c = asbf(__builtin_amdgcn_raw_buffer_load_b128(rsWS, l16, kvglob(kh, kvpos(n, 1)), SC1));
            f32x16 s = MFMA32(a, asbf(q0), (f32x16){}); s = MFMA32(c, asbf(q1), s);
            mx = fmaxf(mx, rowmax16(s));
        }
        lds_bar();
        if (hi) *(LAS float*)(L + L_LSUM + w * 128 + r32 * 4) = mx;
        lds_bar();
        mx = fmaxf(mx, *(LAS const float*)(L + L_LSUM + (w ^ 4) * 128 + r32 * 4));
        set_shift(q1, mx, hi);
        o = (f32x16){};
        for (int n = 0; n < 32; ++n) {
            const bf16x8 a = asbf(__builtin_amdgcn_raw_buffer_load_b128(rsWS, l16, kvglob(kh, kvpos(n, 0)), SC1));
            const bf16x8 c = asbf(__builtin_amdgcn_raw_buffer_load_b128(rsWS, l16, kvglob(kh, kvpos(n, 1)), SC1));
            const bf16x8 d = asbf(__builtin_amdgcn_raw_buffer_load_b128(rsWS, l16, kvglob(kh, kvpos(n, 2)), SC1));
            const bf16x8 e = asbf(__builtin_amdgcn_raw_buffer_load_b128(rsWS, l16, kvglob(kh, kvpos(n, 3)), SC1));
            f32x16 s = MFMA32(a, asbf(q0), (f32x16){}); s = MFMA32(c, asbf(q1), s);
#pragma unroll
            for (int r = 0; r < 16; ++r) s[r] = __builtin_amdgcn_exp2f(s[r]);
            o = MFMA32(d, asbf((u32x4){cvt2(s[0], s[1]), cvt2(s[2], s[3]), cvt2(s[4], s[5]), cvt2(s[6], s[7])}), o);
            o = MFMA32(e, asbf((u32x4){cvt2(s[8], s[9]), cvt2(s[10], s[11]), cvt2(s[12], s[13]), cvt2(s[14], s[15])}), o);
        }
        lds_bar();
        merge_write();
        lds_bar();
    }
    {
        const f32x4 m0 = *(LAS const f32x4*)(L + L_MRG + (w ^ 4) * 2048 + l16), m1 = *(LAS const f32x4*)(L + L_MRG + (w ^ 4) * 2048 + 1024 + l16);
        const float ls = *(LAS const float*)(L + L_LSUM + w * 128 + r32 * 4) + *(LAS const float*)(L + L_LSUM + (w ^ 4) * 128 + r32 * 4);
        const float sc = gt / ls;
        const f32x4 t0 = ((kh ? (f32x4){o[8], o[9], o[10], o[11]} : (f32x4){o[0], o[1], o[2], o[3]}) + m0) * sc;
        const f32x4 t1 = ((kh ? (f32x4){o[12], o[13], o[14], o[15]} : (f32x4){o[4], o[5], o[6], o[7]}) + m1) * sc;
        u32x4 ov = {nosent(cvt2(t0[0], t0[1])), nosent(cvt2(t0[2], t0[3])), nosent(cvt2(t1[0], t1[1])), nosent(cvt2(t1[2], t1[3]))};
        if (kh && hi) { ov[2] = 0u; ov[3] = 0u; }
        __builtin_amdgcn_raw_buffer_store_b128(ov, rsWS, l16, WS_O + blk * 65536 + ((th * 16 + 2 * j + hd) * 2 + kh) * 1024, SC1);
    }
#pragma unroll
    for (int t = 0; t < 5; ++t) wodma(min(t * 8 + w, 38));
    lds_bar();
    wodma(39 + w); wodma(47 + w); if (w == 0) wodma(55);
    STAMP(8);

    f32x16 y0 = {}, y1 = {};
    {
        const int ra = r32, rb = min(32 + r32, 55);
        const unsigned ca = hi * L_CIM + ra * 128 + ((kq ^ ((ra >> 1) & 7)) << 4), cb = hi * L_CIM + rb * 128 + ((kq ^ ((rb >> 1) & 7)) << 4);
#pragma unroll
        for (int m = 0; m < 14; ++m) {
            const bf16x8 fa = ld128(L, L_C + (m >> 1) * 7168 + (ca ^ ((m & 1) << 6))), fb = ld128(L, L_C + (m >> 1) * 7168 + (cb ^ ((m & 1) << 6)));
            y0 = MFMA32(fa, asbf(hf[m]), y0); y1 = MFMA32(fb, asbf(hf[m]), y1);
        }
        const float s2 = 2.0f * (1.0f - gt);
        y0 *= s2; y1 *= s2;
    }
    const int tok = blk * 64 + 32 * th + r32, colA = 56 * j + 16 * kq + 4 * hi;
    const u32x2 xa = __builtin_amdgcn_raw_buffer_load_b64(rsX, tok * ROWB + colA * 2, 0, 0), xb = __builtin_amdgcn_raw_buffer_load_b64(rsX, tok * ROWB + colA * 2 + 16, 0, 0);
    const rsrc_t rsD = mkrs(dskip, ROWB);
    const u32x2 da = __builtin_amdgcn_raw_buffer_load_b64(rsD, colA * 2, 0, 0), db = __builtin_amdgcn_raw_buffer_load_b64(rsD, colA * 2 + 16, 0, 0);
    STAMP(9);
    wait_bar<0>();
    u32x4 of[8];
    const unsigned ob = WS_O + blk * 65536 + (th * 16 + 4 * kq) * 2048;
#pragma unroll
    for (int t = 0; t < 8; ++t) of[t] = __builtin_amdgcn_raw_buffer_load_b128(rsWS, l16 + t * 1024, ob, SC1);
    bf16x8 wf0[8], wf1[8];
    {
        const unsigned cx = (32 * (G & 1) + 8 * tp) ^ (((tq >> 1) & 1) << 6), nb = L_WO + (112 * kq + tq) * 128, wa = nb + hi * 512;
#pragma unroll
        for (int t = 0; t < 8; ++t) { const unsigned ro = (28 * (t >> 1) + 16 * (t & 1)) * 128, second = ((t & 1) ? nb : wa) + ro + 1024;
            wf0[t] = ldtr2(L, wa + ro + cx, second + cx); wf1[t] = ldtr2(L, wa + ro + (cx ^ 64), second + (cx ^ 64)); }
    }
    for (unsigned spin = 0; spin < SPIN_CAP; ++spin) {
        unsigned mx = 0;
#pragma unroll
        for (int t = 0; t < 8; ++t) mx = max(max(mx, max(of[t][0], of[t][1])), max(of[t][2], of[t][3]));
        if (__all(mx != ~0u)) break;
#pragma unroll
        for (int t = 0; t < 8; ++t) of[t] = __builtin_amdgcn_raw_buffer_load_b128(rsWS, l16 + t * 1024, ob, SC1);
    }
    STAMP(10);
#pragma unroll
    for (int t = 0; t < 8; ++t) { y0 = MFMA32(wf0[t], asbf(of[t]), y0); y1 = MFMA32(wf1[t], asbf(of[t]), y1); }
#pragma unroll
    for (int d = 0; d < 4; ++d) {
        if (d != kq) {
            const f32x16& y = (d >> 1) ? y1 : y0; const int e = 8 * (d & 1);
            const unsigned a = L_RED + ((2 * d + th) * 4 + kq) * 2048 + l16;
            *(LAS f32x4*)(L + a) = (f32x4){y[e], y[e + 1], y[e + 2], y[e + 3]};
            *(LAS f32x4*)(L + a + 1024) = (f32x4){y[e + 4], y[e + 5], y[e + 6], y[e + 7]};
        }
    }
    f32x4 ra, rb;
    if (kq == 0) { ra = (f32x4){y0[0], y0[1], y0[2], y0[3]}; rb = (f32x4){y0[4], y0[5], y0[6], y0[7]}; }
    else if (kq == 1) { ra = (f32x4){y0[8], y0[9], y0[10], y0[11]}; rb = (f32x4){y0[12], y0[13], y0[14], y0[15]}; }
    else if (kq == 2) { ra = (f32x4){y1[0], y1[1], y1[2], y1[3]}; rb = (f32x4){y1[4], y1[5], y1[6], y1[7]}; }
    else { ra = (f32x4){y1[8], y1[9], y1[10], y1[11]}; rb = (f32x4){y1[12], y1[13], y1[14], y1[15]}; }
    {
        const float g1 = 1.0f - gt;
        ra += g1 * (f32x4){bf2f(xa[0] & 0xffff) * bf2f(da[0] & 0xffff), bf2f(xa[0] >> 16) * bf2f(da[0] >> 16), bf2f(xa[1] & 0xffff) * bf2f(da[1] & 0xffff), bf2f(xa[1] >> 16) * bf2f(da[1] >> 16)};
        rb += g1 * (f32x4){bf2f(xb[0] & 0xffff) * bf2f(db[0] & 0xffff), bf2f(xb[0] >> 16) * bf2f(db[0] >> 16), bf2f(xb[1] & 0xffff) * bf2f(db[1] & 0xffff), bf2f(xb[1] >> 16) * bf2f(db[1] >> 16)};
    }
    lds_bar();
#pragma unroll
    for (int d = 1; d < 4; ++d) {
        const unsigned a = L_RED + (w * 4 + (kq ^ d)) * 2048 + l16;
        ra += *(LAS const f32x4*)(L + a); rb += *(LAS const f32x4*)(L + a + 1024);
    }
    const rsrc_t rsO = mkrs(out, SEQ * DM * 4);
    __builtin_amdgcn_raw_buffer_store_b128(__builtin_bit_cast(u32x4, ra), rsO, (tok * DM + colA) * 4, 0, 0);
    if (kq < 3) __builtin_amdgcn_raw_buffer_store_b128(__builtin_bit_cast(u32x4, rb), rsO, (tok * DM + colA + 8) * 4, 0, 0);
    STAMP(11);
}

extern "C" void kernel_launch(void* const* d_in, const int* in_sizes, int n_in, void* d_out, int out_size, void* d_ws, size_t ws_size,
                              hipStream_t stream) {
    const uint16_t* const* in = (const uint16_t* const*)d_in;
    (void)hipMemsetAsync(d_ws, 0, WS_SYNC_BYTES, stream);
    gated_mix_kernel<<<256, 512, 0, stream>>>(in[0], in[1], in[2], in[3], in[4], in[6], in[7], in[8], in[9], in[10], in[11], in[12], in[13], in[14],
                                              (float*)d_out, (char*)d_ws);
}
```

```cpp
#include <hip/hip_runtime.h>
#include <hip/hip_bf16.h>
#include <stdint.h>

typedef __bf16 bf16x8 __attribute__((ext_vector_type(8)));
typedef __bf16 bf16x2 __attribute__((ext_vector_type(2)));
typedef float f32x16 __attribute__((ext_vector_type(16)));
typedef float f32x4 __attribute__((ext_vector_type(4)));
typedef float f32x2 __attribute__((ext_vector_type(2)));
typedef unsigned u32x4 __attribute__((ext_vector_type(4)));
typedef unsigned u32x2 __attribute__((ext_vector_type(2)));
typedef short s16x4 __attribute__((ext_vector_type(4)));
typedef short s16x8 __attribute__((ext_vector_type(8)));
typedef __amdgpu_buffer_rsrc_t rsrc_t;
#define LAS __attribute__((address_space(3)))
#define DI __device__ __forceinline__
#define SBAR() __builtin_amdgcn_sched_barrier(0)
#define PIN(x) asm volatile("" : "+v"(x))
#define MFMA32(a, b, c) __builtin_amdgcn_mfma_f32_32x32x16_bf16((a), (b), (c), 0, 0, 0)
#define MFMA16(a, b, c) __builtin_amdgcn_mfma_f32_16x16x32_bf16((a), (b), (c), 0, 0, 0)
#define SC1 16

#define MAP42 1

constexpr int SEQ = 2048, DM = 448, ROWB = DM * 2;
constexpr unsigned WS_KVCNT = 0;
constexpr unsigned WS_HCNT = 16384;
constexpr unsigned WS_X = 20480;
constexpr unsigned OOB = 0x7FFFFFF0u;
constexpr unsigned WS_KV = 65536;
constexpr unsigned WS_E = WS_KV + 262144;
constexpr unsigned WS_H = WS_E + 131072;
constexpr unsigned WS_O = WS_H + 32 * 114688;
constexpr unsigned WS_END = WS_O + 32 * 65536;
constexpr unsigned WS_STAMP = WS_END;

constexpr unsigned L_CH = 30720, L_CHW = 8192, L_CHB = 16384;
constexpr unsigned L_CRW = 122880, L_CRX = 148480;
constexpr unsigned L_RING = 106496;
constexpr unsigned L_QSH = 155648, L_G = 100352, L_FLAG = 100608;
constexpr unsigned L_C = 0, L_CIM = 50176, L_HSCR = 155648;
constexpr unsigned L_WO = 106496, L_MRG = 146432, L_LSUM = 162816, L_RED = 0;
constexpr unsigned LDS_BYTES = 163840;
constexpr unsigned SPIN_CAP = 1u << 14;

DI unsigned cvt2(float a, float b) { f32x2 v = {a, b}; return __builtin_bit_cast(unsigned, __builtin_convertvector(v, bf16x2)); }
DI float bf2f(unsigned h) { return __uint_as_float(h << 16); }
DI float bfround(float x) { return __uint_as_float(cvt2(x, 0.f) << 16); }
DI rsrc_t mkrs(const void* p, unsigned bytes) { return __builtin_amdgcn_make_buffer_rsrc((void*)p, (short)0, (int)bytes, 0x00020000); }
DI void dma(rsrc_t rs, unsigned voff, unsigned soff, unsigned lds) {
    asm volatile("s_mov_b32 m0, %0\n\ts_nop 0\n\tbuffer_load_dwordx4 %1, %2, %3 offen lds" :: "s"(lds), "v"(voff), "s"(rs), "s"(soff) : "memory"); }
DI void dma_sc1(rsrc_t rs, unsigned voff, unsigned soff, unsigned lds) {
    asm volatile("s_mov_b32 m0, %0\n\ts_nop 0\n\tbuffer_load_dwordx4 %1, %2, %3 offen sc1 lds" :: "s"(lds), "v"(voff), "s"(rs), "s"(soff) : "memory"); }
template <int N> DI void wait_bar() { asm volatile("s_waitcnt vmcnt(%0) lgkmcnt(0)\n\ts_barrier" :: "i"(N) : "memory"); }
template <int N> DI void wait_vm() { asm volatile("s_waitcnt vmcnt(%0)" :: "i"(N) : "memory"); }
DI void lds_bar() { asm volatile("s_waitcnt lgkmcnt(0)\n\ts_barrier" ::: "memory"); }
DI bf16x8 ld128(LAS char* L, unsigned off) { return __builtin_bit_cast(bf16x8, *(LAS const u32x4*)(L + off)); }
DI s16x4 ldtr(LAS char* L, unsigned off) { return __builtin_amdgcn_ds_read_tr16_b64_v4i16((LAS s16x4*)(L + off)); }
DI bf16x8 ldtr2(LAS char* L, unsigned off0, unsigned off1) {
    s16x4 a = ldtr(L, off0), b = ldtr(L, off1); return __builtin_bit_cast(bf16x8, __builtin_shufflevector(a, b, 0, 1, 2, 3, 4, 5, 6, 7)); }
DI bf16x8 asbf(u32x4 v) { return __builtin_bit_cast(bf16x8, v); }
DI unsigned nosent(unsigned v) { return v < 0xFFFFFFFEu ? v : 0xFFFFFFFEu; }
DI unsigned atomic_read(rsrc_t rs, unsigned voff) { return (unsigned)__builtin_amdgcn_raw_ptr_buffer_atomic_add_i32(0, rs, (int)voff, 0, SC1); }
DI void atomic_inc(rsrc_t rs, unsigned voff) { asm volatile("buffer_atomic_add %0, %1, %2, 0 offen sc1" :: "v"(1u), "v"(voff), "s"(rs) : "memory"); }
struct cf { float r, i; };
DI cf cmul(cf a, cf b) { return {a.r * b.r - a.i * b.i, a.r * b.i + a.i * b.r}; }
DI cf cfma(cf a, cf b, cf c) { return {__builtin_fmaf(a.r, b.r, __builtin_fmaf(-a.i, b.i, c.r)), __builtin_fmaf(a.r, b.i, __builtin_fmaf(a.i, b.r, c.i))}; }
DI float shfl(float v, int src) { return __int_as_float(__builtin_amdgcn_ds_bpermute(src << 2, __float_as_int(v))); }
DI float rowmax16(const f32x16& s) {
    float a = fmaxf(fmaxf(s[0], s[1]), s[2]), b = fmaxf(fmaxf(s[3], s[4]), s[5]);
    a = fmaxf(fmaxf(a, s[6]), s[7]); b = fmaxf(fmaxf(b, s[8]), s[9]); a = fmaxf(fmaxf(a, s[10]), s[11]); b = fmaxf(fmaxf(b, s[12]), s[13]);
    a = fmaxf(fmaxf(a, s[14]), s[15]); a = fmaxf(a, b);
    auto rr = __builtin_amdgcn_permlane32_swap(__float_as_uint(a), __float_as_uint(a), false, false);
    return fmaxf(__uint_as_float(rr[0]), __uint_as_float(rr[1])); }
__host__ __device__ constexpr int kvpos(int n, int sub) {
    return sub == 0 ? (n < 2 ? 2 * n : 4 * n - 3) : sub == 1 ? (n < 2 ? 2 * n + 1 : 4 * n - 1)
         : sub == 2 ? (n == 0 ? 4 : n == 31 ? 126 : 4 * n + 4) : (n == 30 ? 125 : n == 31 ? 127 : 4 * n + 6); }
__host__ __device__ constexpr unsigned kvlds(int pos) { return (((pos >> 3) + 1) % 3) * 16384 + (pos & 7) * 1024; }
DI unsigned kvglob(int half, int pos) { return WS_KV + (pos >> 3) * 16384 + half * 8192 + (pos & 7) * 1024; }
DI void set_shift(u32x4& q1, float c, int hi) {
    const float c1 = bfround(c), c2 = bfround(c - c1), c3 = c - c1 - c2;
    if (hi) { q1[2] = cvt2(-c1, -c2); q1[3] = cvt2(-c3, 0.f); } }

#define STAMP(k)
#define STAMP2(k)

__global__ void __launch_bounds__(512, 2)
gated_mix_kernel(const uint16_t* __restrict__ x, const uint16_t* __restrict__ wq, const uint16_t* __restrict__ wk, const uint16_t* __restrict__ wv,
                 const uint16_t* __restrict__ wo, const uint16_t* __restrict__ lre, const uint16_t* __restrict__ lim,
                 const uint16_t* __restrict__ bre, const uint16_t* __restrict__ bim, const uint16_t* __restrict__ cre, const uint16_t* __restrict__ cim,
                 const uint16_t* __restrict__ logdt, const uint16_t* __restrict__ dskip, const uint16_t* __restrict__ gate,
                 float* __restrict__ out, char* __restrict__ ws) {
    __shared__ __attribute__((aligned(16))) char smem[LDS_BYTES];
    LAS char* L = (LAS char*)smem;
    const unsigned l0 = (unsigned)(uintptr_t)L;
    const int lane = threadIdx.x & 63, w = __builtin_amdgcn_readfirstlane(threadIdx.x >> 6);
    const int b = blockIdx.x;
    const int blk = ((b & 7) >> 1) * 8 + (b >> 5), j = (b & 1) * 4 + ((b >> 3) & 3);
    const int th = w & 1, hd = (w >> 1) & 1, kh = w >> 2, kq = w >> 1;
    const int r32 = lane & 31, hi = lane >> 5, r16 = lane & 15, G = lane >> 4;
    const int tq = r16 >> 2, tp = lane & 3;
    const rsrc_t rsX = mkrs(x, SEQ * ROWB), rsWS = mkrs(ws, WS_END);
    STAMP(0);

    __builtin_amdgcn_raw_buffer_store_b128((u32x4){~0u, ~0u, ~0u, ~0u}, rsWS, lane * 16, WS_O + blk * 65536 + ((th * 16 + 2 * j + hd) * 2 + kh) * 1024, SC1);
    const int sl = lane & 7, r8 = lane >> 3;
    u32x4 q0 = {0u, 0u, 0u, 0u}, q1 = q0;
    f32x4 ur[4] = {}, ui[4] = {};
    cf lam = {0.f, 0.f}, lam64 = lam, wlo = lam, whi = lam, zG = lam;
    f32x2 inr = {0.f, 0.f}, ini = inr;
    auto xswz = [](int row) { return ((row >> 1) & 7) ^ ((row & 16) ? 7 : 0) ^ ((row & 32) ? 6 : 0); };
    if (w < 4) {
        f32x16 qa = {};
        const int qrow = 32 * th + r32;
        const unsigned a0 = qrow * 128 + ((hi ^ xswz(qrow)) << 4);
        const unsigned a1 = L_CHW + (8 * hi + tq) * 128 + ((56 * hd + 32 * (G & 1) + 8 * tp) ^ (((tq >> 1) & 1) << 6));
        auto qchunk = [&](int c) {
            const unsigned s = (c & 3) * L_CH;
#pragma unroll
            for (int ks = 0; ks < 4; ++ks) {
                const bf16x8 xf = ld128(L, s + (a0 ^ (ks << 5)));
                const bf16x8 wf = ldtr2(L, s + a1 + ks * 16 * 128, s + a1 + (ks * 16 + 4) * 128);
                qa = MFMA32(wf, xf, qa);
            } };
        auto qfinish = [&]() {
            const float cs = 0.18898223650461361f * 1.4426950408889634f;
            if (hd == 1 && hi) *(LAS float*)(L + L_G + qrow * 4) = 1.0f / (1.0f + expf(-qa[12]));
            q0 = (u32x4){cvt2(qa[0] * cs, qa[1] * cs), cvt2(qa[2] * cs, qa[3] * cs), cvt2(qa[4] * cs, qa[5] * cs), cvt2(qa[6] * cs, qa[7] * cs)};
            q1 = (u32x4){cvt2(qa[8] * cs, qa[9] * cs), cvt2(qa[10] * cs, qa[11] * cs), cvt2(qa[12] * cs, qa[13] * cs), cvt2(qa[14] * cs, qa[15] * cs)};
            if (hi) { q1[2] = 0u; q1[3] = 0u; }
            *(LAS u32x4*)(L + L_QSH + w * 2048 + lane * 16) = q0;
            *(LAS u32x4*)(L + L_QSH + w * 2048 + 1024 + lane * 16) = q1; };
        if (w == 0) {
            const bool master = b == 0;
            const unsigned xoff = WS_X + (b & 15) * 128, bump = (master && lane < 16) ? WS_X + lane * 128 : OOB;
            unsigned x0, g0, g1, g2, g3, g4, g5, g6;
            __builtin_amdgcn_raw_buffer_store_b32(0u, rsWS, master ? lane * 128 : OOB, 0, SC1);
            __builtin_amdgcn_raw_buffer_store_b32(0u, rsWS, master ? lane * 128 + 8192 : OOB, 0, SC1);
            __builtin_amdgcn_raw_buffer_store_b32(0u, rsWS, (master && lane < 32) ? lane * 128 + 16384 : OOB, 0, SC1);
            x0 = 0u;
            asm volatile("buffer_atomic_add %0, %1, %2, 0 offen sc0 sc1" : "+v"(x0) : "v"(lane == 0 ? xoff : OOB), "s"(rsWS) : "memory");
            asm volatile("global_load_ushort %0, %7, off\n\tglobal_load_ushort %1, %7, off offset:128\n\tglobal_load_ushort %2, %7, off offset:256\n\t"
                         "global_load_ushort %3, %7, off offset:384\n\tglobal_load_ushort %4, %7, off offset:512\n\tglobal_load_ushort %5, %7, off offset:640\n\t"
                         "global_load_ushort %6, %7, off offset:768"
                         : "=&v"(g0), "=&v"(g1), "=&v"(g2), "=&v"(g3), "=&v"(g4), "=&v"(g5), "=&v"(g6) : "v"(gate + lane) : "memory");
            const int kind = j & 1, tg = j >> 1;
            const rsrc_t rsWK = mkrs(kind ? wv : wk, DM * 56);
            const unsigned vxa = r8 * ROWB + ((sl ^ (r8 >> 1)) << 4), vxb = (vxa ^ 64) + 8 * ROWB, xs = (blk * 64 + tg * 16) * ROWB;
#pragma unroll
            for (int c = 0; c < 7; ++c) {
                dma(rsX, vxa, xs + c * 128, l0 + L_CRX + c * 2048); dma(rsX, vxb, xs + c * 128, l0 + L_CRX + c * 2048 + 1024);
                dma(rsWK, lane * 16, (4 * c) * 1024, l0 + L_CRW + (4 * c) * 1024);
                if (c < 6) { dma(rsWK, lane * 16, (4 * c + 1) * 1024, l0 + L_CRW + (4 * c + 1) * 1024); dma(rsWK, lane * 16, (4 * c + 2) * 1024, l0 + L_CRW + (4 * c + 2) * 1024);
                             dma(rsWK, lane * 16, (4 * c + 3) * 1024, l0 + L_CRW + (4 * c + 3) * 1024); }
            }
            asm volatile("s_barrier" ::: "memory");
            wait_vm<39>();
            atomic_inc(rsWS, bump);
            asm volatile("s_waitcnt vmcnt(1)" : "+v"(x0), "+v"(g0), "+v"(g1), "+v"(g2), "+v"(g3), "+v"(g4), "+v"(g5), "+v"(g6) :: "memory");
            STAMP(1);
            auto gate_write = [&](int c, unsigned g16) {
                *(LAS uint16_t*)(L + (c & 3) * L_CH + L_CHW + lane * 128 + (112 ^ (((lane >> 1) & 1) << 6))) = (uint16_t)g16; };
            gate_write(0, g0); gate_write(1, g1); gate_write(2, g2); gate_write(3, g3);
            const int cg0 = kind ? tp : 4 * (tp >> 1) + (tp & 1), cg1 = kind ? 4 + tp : 4 * (tp >> 1) + 2 + (tp & 1);
            const unsigned aw0 = L_CRW + (8 * G + tq) * 56 + cg0 * 8, aw1 = L_CRW + (8 * G + tq) * 56 + cg1 * 8, ax = L_CRX + r16 * 128 + ((G ^ (r16 >> 1)) << 4);
            const int T = 2 * blk + (tg >> 1), sg = tg & 1, half = T >> 5, n = T & 31;
            f32x4 d0 = {0.f, 0.f, 0.f, 0.f}, d1 = d0;
            if (kind == 0) {
#pragma unroll
                for (int ks = 0; ks < 14; ++ks) {
                    const bf16x8 xf = ld128(L, (ks >> 1) * 2048 + (ax ^ ((ks & 1) << 6)));
                    d0 = MFMA16(ldtr2(L, aw0 + ks * 32 * 56, aw0 + (ks * 32 + 4) * 56), xf, d0);
                    d1 = MFMA16(ldtr2(L, aw1 + ks * 32 * 56, aw1 + (ks * 32 + 4) * 56), xf, d1);
                }
                u32x4 kv = {cvt2(d0[0], d0[1]), cvt2(d0[2], d0[3]), cvt2(d1[0], d1[1]), cvt2(d1[2], d1[3])};
                if (G == 3) { kv[2] = 0x3F803F80u; kv[3] = 0x00003F80u; }
                const int row = 16 * sg + ((r16 & 3) | ((r16 & 4) << 1) | ((r16 & 8) >> 1));
                const unsigned base = (G >> 1) ? kvglob(half, kvpos(n, 1)) : kvglob(half, kvpos(n, 0));
                __builtin_amdgcn_raw_buffer_store_b128(kv, rsWS, base + ((G & 1) * 32 + row) * 16, 0, SC1);
            } else {
#pragma unroll
                for (int ks = 0; ks < 14; ++ks) {
                    const bf16x8 xf = ld128(L, (ks >> 1) * 2048 + (ax ^ ((ks & 1) << 6)));
                    d0 = MFMA16(xf, ldtr2(L, aw0 + ks * 32 * 56, aw0 + (ks * 32 + 4) * 56), d0);
                    d1 = MFMA16(xf, ldtr2(L, aw1 + ks * 32 * 56, aw1 + (ks * 32 + 4) * 56), d1);
                }
                u32x2 v0 = {cvt2(d0[0], d0[1]), cvt2(d0[2], d0[3])}, v1 = {cvt2(d1[0], d1[1]), cvt2(d1[2], d1[3])};
                if (r16 >= 12) { const unsigned o = r16 == 12 ? 0x3F803F80u : 0u; v1 = (u32x2){o, o}; }
                const unsigned base = kvglob(half, kvpos(n, 2 + sg)), a = ((G >> 1) * 32 + r16) * 16 + (G & 1) * 8;
                __builtin_amdgcn_raw_buffer_store_b64(v0, rsWS, a, base, SC1);
                __builtin_amdgcn_raw_buffer_store_b64(v1, rsWS, a + 256, base, SC1);
            }
            unsigned x1 = 0u;
            asm volatile("buffer_atomic_add %0, %1, %2, 0 offen sc0 sc1" : "+v"(x1) : "v"(lane == 0 ? xoff : OOB), "s"(rsWS) : "memory");
            atomic_inc(rsWS, bump);
            lds_bar(); qchunk(0);
            lds_bar(); gate_write(4, g4); qchunk(1);
            asm volatile("s_waitcnt vmcnt(0)" : "+v"(x1) :: "memory");
            {   const unsigned s0 = __builtin_amdgcn_readfirstlane(x0); unsigned s1 = __builtin_amdgcn_readfirstlane(x1);
                for (unsigned spin = 0; !master && s1 == s0 && spin < SPIN_CAP; ++spin) s1 = __builtin_amdgcn_readfirstlane(atomic_read(rsWS, lane == 0 ? xoff : OOB)); }
            atomic_inc(rsWS, lane < 8 ? WS_KVCNT + lane * 2048 + (b & 15) * 128 : OOB);
            atomic_inc(rsWS, bump);
            STAMP(2);
            lds_bar(); gate_write(5, g5); qchunk(2); atomic_inc(rsWS, bump);
            lds_bar(); gate_write(6, g6); qchunk(3); atomic_inc(rsWS, bump);
            lds_bar(); qchunk(4); atomic_inc(rsWS, bump);
            lds_bar(); qchunk(5); atomic_inc(rsWS, bump);
            lds_bar(); qchunk(6);
            STAMP(3);
            qfinish();
            if (lane == 0) *(LAS unsigned*)(L + L_FLAG) = 0u;
            for (unsigned spin = 0; spin < SPIN_CAP; ++spin) {
                atomic_inc(rsWS, bump);
                const unsigned c = atomic_read(rsWS, lane < 16 ? WS_KVCNT + (b & 7) * 2048 + lane * 128 : OOB);
                if (__all(c == 16 || lane >= 16)) break;
            }
        } else {
            const rsrc_t rsWQ = mkrs(wq, DM * ROWB);
            const int v = w - 1, p3 = min(6 + v, 7);
            const unsigned vx0 = (blk * 64 + 16 * v + r8) * ROWB + ((sl ^ xswz(16 * v + r8)) << 4), vx1 = (blk * 64 + 16 * v + 8 + r8) * ROWB + ((sl ^ xswz(16 * v + 8 + r8)) << 4);
            const int pw = sl ^ (((r8 >> 1) & 1) << 2);
            const unsigned vw = r8 * ROWB + 112 * j + (pw << 4);
            auto issue = [&](int c) {
                const unsigned s = l0 + (c & 3) * L_CH;
                dma(rsX, vx0, c * 128, s + v * 2048); dma(rsX, vx1, c * 128, s + v * 2048 + 1024);
                if (pw != 7) { dma(rsWQ, vw, (c * 64 + 8 * v) * ROWB, s + L_CHW + v * 1024); dma(rsWQ, vw, (c * 64 + 24 + 8 * v) * ROWB, s + L_CHW + (3 + v) * 1024);
                               dma(rsWQ, vw, (c * 64 + 8 * p3) * ROWB, s + L_CHW + p3 * 1024); } };
            asm volatile("s_barrier" ::: "memory");
            issue(0); issue(1); issue(2); issue(3);
            wait_bar<15>(); qchunk(0);
            wait_bar<10>(); issue(4); qchunk(1);
            wait_bar<10>(); issue(5); qchunk(2);
            wait_bar<10>(); issue(6); qchunk(3);
            wait_bar<10>(); qchunk(4);
            wait_bar<5>(); qchunk(5);
            wait_bar<0>(); qchunk(6);
            STAMP(3);
            qfinish();
        }
    } else {
        const int u = w - 4, st = min(16 * u + r16, 55);
        const unsigned eoff = (G == 0 && 16 * u + r16 < 56) ? st * 8 : OOB;
        __builtin_amdgcn_raw_buffer_store_b64((u32x2){~0u, ~0u}, rsWS, eoff, WS_E + (blk * DM + 56 * j) * 8, SC1);
        unsigned p0, p1, p2;
        asm volatile("global_load_ushort %0, %3, off\n\tglobal_load_ushort %1, %4, off\n\tglobal_load_ushort %2, %5, off"
                     : "=&v"(p0), "=&v"(p1), "=&v"(p2) : "v"(lre + 56 * j + st), "v"(lim + 56 * j + st), "v"(logdt + 56 * j + st) : "memory");
        const rsrc_t rsBR = mkrs(bre, DM * ROWB), rsBI = mkrs(bim, DM * ROWB);
        const rsrc_t rs2 = mkrs(u < 3 ? bre : x, u < 3 ? DM * ROWB : SEQ * ROWB), rs3 = mkrs(u < 3 ? bim : x, u < 3 ? DM * ROWB : SEQ * ROWB);
        const unsigned vb = (56 * j + 8 * u + r8) * ROWB + ((sl ^ ((4 * u + (r8 >> 1)) & 7)) << 4);
        const unsigned v2 = u < 3 ? vb + 32 * ROWB : (blk * 64 + 48 + r8) * ROWB + ((sl ^ xswz(48 + r8)) << 4);
        const unsigned v3 = u < 3 ? vb + 32 * ROWB : (blk * 64 + 56 + r8) * ROWB + ((sl ^ xswz(56 + r8)) << 4);
        const unsigned d2 = u < 3 ? L_CHB + (4 + u) * 1024 : 6 * 1024, d3 = u < 3 ? L_CHB + 7168 + (4 + u) * 1024 : 7 * 1024;
        auto issue = [&](int c) {
            const unsigned s = l0 + (c & 3) * L_CH;
            dma(rsBR, vb, c * 128, s + L_CHB + u * 1024); dma(rsBI, vb, c * 128, s + L_CHB + 7168 + u * 1024);
            dma(rs2, v2, c * 128, s + d2); dma(rs3, v3, c * 128, s + d3); };
        const unsigned a4 = L_CHB + st * 128 + ((G ^ ((st >> 1) & 7)) << 4);
        unsigned at[4];
#pragma unroll
        for (int m = 0; m < 4; ++m) { const int row = 16 * (r16 >> 2) + 2 * m + ((r16 & 3) >> 1) + 8 * (r16 & 1); at[m] = row * 128 + ((G ^ xswz(row)) << 4); }
        auto uchunk = [&](int c) {
            const unsigned s = (c & 3) * L_CH;
#pragma unroll
            for (int ks = 0; ks < 2; ++ks) {
                const bf16x8 br = ld128(L, s + (a4 ^ (ks << 6))), bi = ld128(L, s + 7168 + (a4 ^ (ks << 6)));
#pragma unroll
                for (int m = 0; m < 4; ++m) { const bf16x8 xf = ld128(L, s + (at[m] ^ (ks << 6))); ur[m] = MFMA16(xf, br, ur[m]); ui[m] = MFMA16(xf, bi, ui[m]); }
            } };
        asm volatile("s_barrier" ::: "memory");
        issue(0); issue(1); issue(2); issue(3);
        asm volatile("s_waitcnt vmcnt(16)" : "+v"(p0), "+v"(p1), "+v"(p2) :: "memory");
        const float lr = bf2f(p0), li = bf2f(p1), dt = bfround(expf(bf2f(p2)));
        const float er = expf(dt * lr); float sn, cs; sincosf(dt * li, &sn, &cs);
        lam = {er * cs, er * sn};
        const float inv = 1.0f / (lr * lr + li * li);
        const cf coef = {((lam.r - 1.0f) * lr + lam.i * li) * inv, (lam.i * lr - (lam.r - 1.0f) * li) * inv};
        const cf l2 = cmul(lam, lam), l4 = cmul(l2, l2), lam8 = cmul(l4, l4), lam16 = cmul(lam8, lam8), l32 = cmul(lam16, lam16);
        lam64 = cmul(l32, l32);
        const cf l128 = cmul(lam64, lam64), l256 = cmul(l128, l128), l512 = cmul(l256, l256), l1024 = cmul(l512, l512), one = {1.f, 0.f};
        wlo = cmul((G & 1) ? lam16 : one, (G & 2) ? l32 : one); whi = cmul(wlo, lam8);
        zG = cmul((G & 1) ? l512 : one, (G & 2) ? l1024 : one);
        wait_bar<12>(); uchunk(0);
        wait_bar<8>(); issue(4); uchunk(1);
        wait_bar<8>(); issue(5); uchunk(2);
        wait_bar<8>(); issue(6); uchunk(3);
        wait_bar<8>(); uchunk(4);
        wait_bar<4>(); uchunk(5);
        wait_bar<0>(); uchunk(6);
        STAMP(3);
        f32x2 hr = {0.f, 0.f}, hm = hr;
#pragma unroll
        for (int p = 0; p < 8; ++p) {
            const f32x2 a = {ur[p >> 1][2 * (p & 1)], ur[p >> 1][2 * (p & 1) + 1]}, c = {ui[p >> 1][2 * (p & 1)], ui[p >> 1][2 * (p & 1) + 1]};
            const f32x2 tr = coef.r * a - coef.i * c, ti = coef.r * c + coef.i * a;
            ur[p >> 1][2 * (p & 1)] = tr[0]; ur[p >> 1][2 * (p & 1) + 1] = tr[1]; ui[p >> 1][2 * (p & 1)] = ti[0]; ui[p >> 1][2 * (p & 1) + 1] = ti[1];
            const f32x2 nr = lam.r * hr - lam.i * hm + tr, ni = lam.r * hm + lam.i * hr + ti; hr = nr; hm = ni;
        }
        const cf elo = {hr[0], hm[0]}, e16 = cfma(lam8, elo, (cf){hr[1], hm[1]});
        const cf e0 = {shfl(e16.r, r16), shfl(e16.i, r16)}, e1 = {shfl(e16.r, r16 + 16), shfl(e16.i, r16 + 16)};
        const cf e2 = {shfl(e16.r, r16 + 32), shfl(e16.i, r16 + 32)}, e3 = {shfl(e16.r, r16 + 48), shfl(e16.i, r16 + 48)};
        const cf t2 = cfma(lam16, e0, e1), t3 = cfma(lam16, t2, e2), eb = cfma(lam16, t3, e3);
        const cf pg = G == 0 ? (cf){0.f, 0.f} : G == 1 ? e0 : G == 2 ? t2 : t3, ph = cfma(lam8, pg, elo);
        inr = (f32x2){pg.r, ph.r}; ini = (f32x2){pg.i, ph.i};
        __builtin_amdgcn_raw_buffer_store_b64((u32x2){nosent(__float_as_uint(eb.r)), nosent(__float_as_uint(eb.i))}, rsWS, eoff, WS_E + (blk * DM + 56 * j) * 8, SC1);
    }
    STAMP(4);
    lds_bar();
    STAMP(5);

    const unsigned l16 = lane * 16;
#define KVDMA(phi) do { dma_sc1(rsWS, l16, WS_KV + (phi) * 16384 + w * 2048, l0 + L_RING + (((phi) + 1) % 3) * 16384 + w * 2048); \
                        dma_sc1(rsWS, l16, WS_KV + (phi) * 16384 + w * 2048 + 1024, l0 + L_RING + (((phi) + 1) % 3) * 16384 + w * 2048 + 1024); } while (0)
    KVDMA(0); KVDMA(1); KVDMA(2);
    if (w >= 4) { q0 = *(LAS const u32x4*)(L + L_QSH + (w - 4) * 2048 + l16); q1 = *(LAS const u32x4*)(L + L_QSH + (w - 4) * 2048 + 1024 + l16); }
    const float gt = *(LAS const float*)(L + L_G + (32 * th + r32) * 4);
    const rsrc_t rsCR = mkrs(cre, DM * ROWB), rsCI = mkrs(cim, DM * ROWB);
    const int cw = min(w, 6);
    const unsigned vc = (56 * j + 8 * cw + r8) * ROWB + ((sl ^ ((4 * cw + (r8 >> 1)) & 7)) << 4);
    const unsigned kvb = L_RING + kh * 8192 + l16;
    wait_bar<4>();
    STAMP(6);
    f32x16 sA, sB, o;
    bf16x8 k0, k1, v0, v1;
    u32x4 pw0, pw1;
    {
        const bf16x8 c0 = ld128(L, L_RING + l16 + kvlds(0)), c1 = ld128(L, L_RING + l16 + kvlds(1));
        k0 = ld128(L, kvb + kvlds(0)); k1 = ld128(L, kvb + kvlds(1));
        sB = MFMA32(c0, asbf(q0), (f32x16){}); sB = MFMA32(c1, asbf(q1), sB);
        set_shift(q1, rowmax16(sB), hi);
        sA = MFMA32(k0, asbf(q0), (f32x16){}); sA = MFMA32(k1, asbf(q1), sA);
        k0 = ld128(L, kvb + kvlds(2)); k1 = ld128(L, kvb + kvlds(3)); v0 = ld128(L, kvb + kvlds(4));
    }
    u32x2 ev[8];
    u32x4 hf[14];
#define EX4(A, i) do { A[i] = __builtin_amdgcn_exp2f(A[i]); A[i + 1] = __builtin_amdgcn_exp2f(A[i + 1]); \
                       A[i + 2] = __builtin_amdgcn_exp2f(A[i + 2]); A[i + 3] = __builtin_amdgcn_exp2f(A[i + 3]); PIN(A); } while (0)
#define ITER(A, B, n, HOOK) do { SBAR(); \
    if ((n) < 31) { B = MFMA32(k0, asbf(q0), (f32x16){}); PIN(B); } SBAR(); \
    if ((n) < 30) k0 = ld128(L, kvb + kvlds(kvpos((n) + 2, 0))); \
    EX4(A, 0); SBAR(); \
    if ((n) > 0) { o = MFMA32(v1, asbf(pw1), o); PIN(o); } SBAR(); \
    v1 = ld128(L, kvb + kvlds(kvpos((n), 3))); \
    pw0[0] = cvt2(A[0], A[1]); pw0[1] = cvt2(A[2], A[3]); PIN(pw0); SBAR(); EX4(A, 4); SBAR(); \
    if ((n) < 31) { B = MFMA32(k1, asbf(q1), B); PIN(B); } SBAR(); \
    if ((n) < 30) k1 = ld128(L, kvb + kvlds(kvpos((n) + 2, 1))); \
    pw0[2] = cvt2(A[4], A[5]); pw0[3] = cvt2(A[6], A[7]); PIN(pw0); SBAR(); EX4(A, 8); SBAR(); \
    if ((n) > 0) o = MFMA32(v0, asbf(pw0), o); else o = MFMA32(v0, asbf(pw0), (f32x16){}); PIN(o); SBAR(); \
    pw1[0] = cvt2(A[8], A[9]); pw1[1] = cvt2(A[10], A[11]); PIN(pw1); SBAR(); EX4(A, 12); SBAR(); \
    HOOK; \
    if ((n) < 31) v0 = ld128(L, kvb + kvlds(kvpos((n) + 1, 2))); \
    pw1[2] = cvt2(A[12], A[13]); pw1[3] = cvt2(A[14], A[15]); PIN(pw1); SBAR(); } while (0)
#define ABL 0
#define STAGE(phi, N, EXTRA) do { ITER(sA, sB, 2 * (phi) - 2, { if (ABL == 0) wait_bar<N>(); if (ABL == 1) lds_bar(); STAMP2(phi); EXTRA; if (ABL < 3 && (phi) + 2 < 16) KVDMA((phi) + 2); }); ITER(sB, sA, 2 * (phi) - 1, {}); } while (0)
#define CD2(cc) do { if (ABL < 3) { dma(rsCR, vc, (cc) * 128, l0 + L_C + (cc) * 7168 + cw * 1024); dma(rsCI, vc, (cc) * 128, l0 + L_CIM + (cc) * 7168 + cw * 1024); } } while (0)
    const rsrc_t rsE = mkrs(ws + WS_E + 56 * j * 8, 32 * DM * 8 - 56 * j * 8);
    const int ebase = (blk - 1 - 8 * G) * (DM * 8) + min((w & 3) * 16 + r16, 55) * 8;
#define ELD() do { _Pragma("unroll") for (int m = 0; m < 8; ++m) ev[m] = __builtin_amdgcn_raw_buffer_load_b64(rsE, ebase - m * (DM * 8), 0, SC1); } while (0)
    STAGE(1, 2, CD2(0));
    STAGE(2, 4, CD2(1));
    STAGE(3, 4, { CD2(2); if (w >= 4) ELD(); });
    STAGE(4, 4, CD2(3));
    f32x2 hr, hm;
    if (w >= 4) {
        for (unsigned spin = 0; spin < SPIN_CAP; ++spin) {
            unsigned mx = 0;
#pragma unroll
            for (int m = 0; m < 8; ++m) mx = max(mx, max(ev[m][0], ev[m][1]));
            if (__all(mx != ~0u)) break;
            ELD();
        }
        cf part = {__uint_as_float(ev[7][0]), __uint_as_float(ev[7][1])};
#pragma unroll
        for (int m = 6; m >= 0; --m) part = cfma(lam64, part, (cf){__uint_as_float(ev[m][0]), __uint_as_float(ev[m][1])});
        part = cmul(zG, part);
        if (ebase < 0) part = (cf){0.f, 0.f};
        part.r += shfl(part.r, lane ^ 16); part.i += shfl(part.i, lane ^ 16); part.r += shfl(part.r, lane ^ 32); part.i += shfl(part.i, lane ^ 32);
        const cf ilo = cfma(wlo, part, (cf){inr[0], ini[0]}), ihi = cfma(whi, part, (cf){inr[1], ini[1]});
        hr = (f32x2){ilo.r, ihi.r}; hm = (f32x2){ilo.i, ihi.i};
    }
    STAGE(5, 4, CD2(4));
    u32x4 pr0, pr1, pi0, pi1;
    if (w >= 4) {
#pragma unroll
        for (int p = 0; p < 8; ++p) {
            const f32x2 a = {ur[p >> 1][2 * (p & 1)], ur[p >> 1][2 * (p & 1) + 1]}, c = {ui[p >> 1][2 * (p & 1)], ui[p >> 1][2 * (p & 1) + 1]};
            const f32x2 nr = lam.r * hr - lam.i * hm + a, ni = lam.r * hm + lam.i * hr + c; hr = nr; hm = ni;
            if (p < 4) { pr0[p & 3] = cvt2(hr[0], hr[1]); pi0[p & 3] = cvt2(-hm[0], -hm[1]); } else { pr1[p & 3] = cvt2(hr[0], hr[1]); pi1[p & 3] = cvt2(-hm[0], -hm[1]); }
        }
    }
    STAGE(6, 4, CD2(5));
    if (w >= 4) {
        const unsigned sb = L_HSCR + (w - 4) * 2048, wa = sb + r16 * 128;
        const int tok = (lane & 48) + (r16 >> 1) + 8 * (lane & 1);
        const unsigned ga = (tok >> 5) * 57344 + (tok & 31) * 16, gb = w < 7 ? ga + 1024 : OOB, gs = WS_H + blk * 114688 + (7 * j + 2 * (w - 4)) * 1024;
        const unsigned pc = 2 * G + (tp >> 1), ra = sb + tq * 128 + ((pc ^ tq) << 4) + 8 * (tp & 1), rb = sb + (4 + tq) * 128 + ((pc ^ (4 + tq)) << 4) + 8 * (tp & 1);
#pragma unroll
        for (int pl = 0; pl < 2; ++pl) {
            *(LAS u32x4*)(L + wa + (((2 * G) ^ (r16 & 7)) << 4)) = pl ? pi0 : pr0;
            *(LAS u32x4*)(L + wa + (((2 * G + 1) ^ (r16 & 7)) << 4)) = pl ? pi1 : pr1;
            asm volatile("" ::: "memory");
            const bf16x8 c0 = ldtr2(L, ra, rb), c1 = ldtr2(L, ra + 1024, rb + 1024);
            asm volatile("" ::: "memory");
            __builtin_amdgcn_raw_buffer_store_b128(__builtin_bit_cast(u32x4, c0), rsWS, ga + pl * 512, gs, SC1);
            __builtin_amdgcn_raw_buffer_store_b128(__builtin_bit_cast(u32x4, c1), rsWS, gb + pl * 512, gs, SC1);
        }
    }
    STAGE(7, 4, CD2(6));
    STAGE(8, 4, {});
    STAGE(9, 2, atomic_inc(rsWS, (w == 4 && lane == 0) ? WS_HCNT + blk * 128 : OOB));
    STAGE(10, 3, {});
    STAGE(11, 2, {});
    if (w == 0) { for (unsigned spin = 0; spin < SPIN_CAP; ++spin) if (__builtin_amdgcn_readfirstlane(atomic_read(rsWS, lane == 0 ? WS_HCNT + blk * 128 : OOB)) == 8) break; }
#define HLD(m) hf[m] = __builtin_amdgcn_raw_buffer_load_b128(rsWS, l16, WS_H + blk * 114688 + th * 57344 + (4 * (m) + kq) * 1024, SC1)
    STAGE(12, 2, { HLD(0); HLD(1); HLD(2); HLD(3); });
    STAGE(13, 6, { HLD(4); HLD(5); HLD(6); HLD(7); });
    STAGE(14, 6, { HLD(8); HLD(9); HLD(10); });
    STAGE(15, 3, { HLD(11); HLD(12); HLD(13); });
    ITER(sA, sB, 30, {}); ITER(sB, sA, 31, {});
    o = MFMA32(v1, asbf(pw1), o);
    STAMP(7);

    const rsrc_t rsWO = mkrs(wo, DM * ROWB);
    const unsigned vwo = (lane >> 3) * ROWB + 112 * j + (((lane & 7) ^ (((lane >> 4) & 1) << 2)) << 4);
    auto wodma = [&](int p) { dma(rsWO, vwo, p * 8 * ROWB, l0 + L_WO + p * 1024); };
    auto merge_write = [&]() {
        const int g8 = kh ? 0 : 8;
        *(LAS f32x4*)(L + L_MRG + w * 2048 + l16) = kh ? (f32x4){o[0], o[1], o[2], o[3]} : (f32x4){o[8], o[9], o[10], o[11]};
        *(LAS f32x4*)(L + L_MRG + w * 2048 + 1024 + l16) = kh ? (f32x4){o[4], o[5], o[6], o[7]} : (f32x4){o[12], o[13], o[14], o[15]};
        if (hi) *(LAS float*)(L + L_LSUM + w * 128 + r32 * 4) = o[12];
        (void)g8; };
    { const bool bad = ABL == 0 && hi && !(o[12] <= 1.8446744e19f);
      if (__any(bad) && lane == 0) *(LAS unsigned*)(L + L_FLAG) = 1u; }
    merge_write();
    lds_bar();
    if (__builtin_expect(__builtin_amdgcn_readfirstlane(*(LAS const unsigned*)(L + L_FLAG)) != 0u, 0)) {
        if (hi) { q1[2] = 0u; q1[3] = 0u; }
        float mx = -INFINITY;
        for (int n = 0; n < 32; ++n) {
            const bf16x8 a = asbf(__builtin_amdgcn_raw_buffer_load_b128(rsWS, l16, kvglob(kh, kvpos(n, 0)), SC1));
            const bf16x8 c = asbf(__builtin_amdgcn_raw_buffer_load_b128(rsWS, l16, kvglob(kh, kvpos(n, 1)), SC1));
            f32x16 s = MFMA32(a, asbf(q0), (f32x16){}); s = MFMA32(c, asbf(q1), s);
            mx = fmaxf(mx, rowmax16(s));
        }
        lds_bar();
        if (hi) *(LAS float*)(L + L_LSUM + w * 128 + r32 * 4) = mx;
        lds_bar();
        mx = fmaxf(mx, *(LAS const float*)(L + L_LSUM + (w ^ 4) * 128 + r32 * 4));
        set_shift(q1, mx, hi);
        o = (f32x16){};
        for (int n = 0; n < 32; ++n) {
            const bf16x8 a = asbf(__builtin_amdgcn_raw_buffer_load_b128(rsWS, l16, kvglob(kh, kvpos(n, 0)), SC1));
            const bf16x8 c = asbf(__builtin_amdgcn_raw_buffer_load_b128(rsWS, l16, kvglob(kh, kvpos(n, 1)), SC1));
            const bf16x8 d = asbf(__builtin_amdgcn_raw_buffer_load_b128(rsWS, l16, kvglob(kh, kvpos(n, 2)), SC1));
            const bf16x8 e = asbf(__builtin_amdgcn_raw_buffer_load_b128(rsWS, l16, kvglob(kh, kvpos(n, 3)), SC1));
            f32x16 s = MFMA32(a, asbf(q0), (f32x16){}); s = MFMA32(c, asbf(q1), s);
#pragma unroll
            for (int r = 0; r < 16; ++r) s[r] = __builtin_amdgcn_exp2f(s[r]);
            o = MFMA32(d, asbf((u32x4){cvt2(s[0], s[1]), cvt2(s[2], s[3]), cvt2(s[4], s[5]), cvt2(s[6], s[7])}), o);
            o = MFMA32(e, asbf((u32x4){cvt2(s[8], s[9]), cvt2(s[10], s[11]), cvt2(s[12], s[13]), cvt2(s[14], s[15])}), o);
        }
        lds_bar();
        merge_write();
        lds_bar();
    }
    {
        const f32x4 m0 = *(LAS const f32x4*)(L + L_MRG + (w ^ 4) * 2048 + l16), m1 = *(LAS const f32x4*)(L + L_MRG + (w ^ 4) * 2048 + 1024 + l16);
        const float ls = *(LAS const float*)(L + L_LSUM + w * 128 + r32 * 4) + *(LAS const float*)(L + L_LSUM + (w ^ 4) * 128 + r32 * 4);
        const float sc = gt / ls;
        const f32x4 t0 = ((kh ? (f32x4){o[8], o[9], o[10], o[11]} : (f32x4){o[0], o[1], o[2], o[3]}) + m0) * sc;
        const f32x4 t1 = ((kh ? (f32x4){o[12], o[13], o[14], o[15]} : (f32x4){o[4], o[5], o[6], o[7]}) + m1) * sc;
        u32x4 ov = {nosent(cvt2(t0[0], t0[1])), nosent(cvt2(t0[2], t0[3])), nosent(cvt2(t1[0], t1[1])), nosent(cvt2(t1[2], t1[3]))};
        if (kh && hi) { ov[2] = 0u; ov[3] = 0u; }
        __builtin_amdgcn_raw_buffer_store_b128(ov, rsWS, l16, WS_O + blk * 65536 + ((th * 16 + 2 * j + hd) * 2 + kh) * 1024, SC1);
    }
#pragma unroll
    for (int t = 0; t < 5; ++t) wodma(min(t * 8 + w, 38));
    lds_bar();
    wodma(39 + w); wodma(47 + w); if (w == 0) wodma(55);
    STAMP(8);

    f32x16 y0 = {}, y1 = {};
    {
        const int ra = r32, rb = min(32 + r32, 55);
        const unsigned ca = hi * L_CIM + ra * 128 + ((kq ^ ((ra >> 1) & 7)) << 4), cb = hi * L_CIM + rb * 128 + ((kq ^ ((rb >> 1) & 7)) << 4);
#pragma unroll
        for (int m = 0; m < 14; ++m) {
            const bf16x8 fa = ld128(L, L_C + (m >> 1) * 7168 + (ca ^ ((m & 1) << 6))), fb = ld128(L, L_C + (m >> 1) * 7168 + (cb ^ ((m & 1) << 6)));
            y0 = MFMA32(fa, asbf(hf[m]), y0); y1 = MFMA32(fb, asbf(hf[m]), y1);
        }
        const float s2 = 2.0f * (1.0f - gt);
        y0 *= s2; y1 *= s2;
    }
    const int tok = blk * 64 + 32 * th + r32, colA = 56 * j + 16 * kq + 4 * hi;
    const u32x2 xa = __builtin_amdgcn_raw_buffer_load_b64(rsX, tok * ROWB + colA * 2, 0, 0), xb = __builtin_amdgcn_raw_buffer_load_b64(rsX, tok * ROWB + colA * 2 + 16, 0, 0);
    const rsrc_t rsD = mkrs(dskip, ROWB);
    const u32x2 da = __builtin_amdgcn_raw_buffer_load_b64(rsD, colA * 2, 0, 0), db = __builtin_amdgcn_raw_buffer_load_b64(rsD, colA * 2 + 16, 0, 0);
    STAMP(9);
    wait_bar<0>();
    u32x4 of[8];
    const unsigned ob = WS_O + blk * 65536 + (th * 16 + 4 * kq) * 2048;
#pragma unroll
    for (int t = 0; t < 8; ++t) of[t] = __builtin_amdgcn_raw_buffer_load_b128(rsWS, l16 + t * 1024, ob, SC1);
    bf16x8 wf0[8], wf1[8];
    {
        const unsigned cx = (32 * (G & 1) + 8 * tp) ^ (((tq >> 1) & 1) << 6), nb = L_WO + (112 * kq + tq) * 128, wa = nb + hi * 512;
#pragma unroll
        for (int t = 0; t < 8; ++t) { const unsigned ro = (28 * (t >> 1) + 16 * (t & 1)) * 128, second = ((t & 1) ? nb : wa) + ro + 1024;
            wf0[t] = ldtr2(L, wa + ro + cx, second + cx); wf1[t] = ldtr2(L, wa + ro + (cx ^ 64), second + (cx ^ 64)); }
    }
    for (unsigned spin = 0; spin < SPIN_CAP; ++spin) {
        unsigned mx = 0;
#pragma unroll
        for (int t = 0; t < 8; ++t) mx = max(max(mx, max(of[t][0], of[t][1])), max(of[t][2], of[t][3]));
        if (__all(mx != ~0u)) break;
#pragma unroll
        for (int t = 0; t < 8; ++t) of[t] = __builtin_amdgcn_raw_buffer_load_b128(rsWS, l16 + t * 1024, ob, SC1);
    }
    STAMP(10);
#pragma unroll
    for (int t = 0; t < 8; ++t) { y0 = MFMA32(wf0[t], asbf(of[t]), y0); y1 = MFMA32(wf1[t], asbf(of[t]), y1); }
#pragma unroll
    for (int d = 0; d < 4; ++d) {
        if (d != kq) {
            const f32x16& y = (d >> 1) ? y1 : y0; const int e = 8 * (d & 1);
            const unsigned a = L_RED + ((2 * d + th) * 4 + kq) * 2048 + l16;
            *(LAS f32x4*)(L + a) = (f32x4){y[e], y[e + 1], y[e + 2], y[e + 3]};
            *(LAS f32x4*)(L + a + 1024) = (f32x4){y[e + 4], y[e + 5], y[e + 6], y[e + 7]};
        }
    }
    f32x4 ra, rb;
    if (kq == 0) { ra = (f32x4){y0[0], y0[1], y0[2], y0[3]}; rb = (f32x4){y0[4], y0[5], y0[6], y0[7]}; }
    else if (kq == 1) { ra = (f32x4){y0[8], y0[9], y0[10], y0[11]}; rb = (f32x4){y0[12], y0[13], y0[14], y0[15]}; }
    else if (kq == 2) { ra = (f32x4){y1[0], y1[1], y1[2], y1[3]}; rb = (f32x4){y1[4], y1[5], y1[6], y1[7]}; }
    else { ra = (f32x4){y1[8], y1[9], y1[10], y1[11]}; rb = (f32x4){y1[12], y1[13], y1[14], y1[15]}; }
    {
        const float g1 = 1.0f - gt;
        ra += g1 * (f32x4){bf2f(xa[0] & 0xffff) * bf2f(da[0] & 0xffff), bf2f(xa[0] >> 16) * bf2f(da[0] >> 16), bf2f(xa[1] & 0xffff) * bf2f(da[1] & 0xffff), bf2f(xa[1] >> 16) * bf2f(da[1] >> 16)};
        rb += g1 * (f32x4){bf2f(xb[0] & 0xffff) * bf2f(db[0] & 0xffff), bf2f(xb[0] >> 16) * bf2f(db[0] >> 16), bf2f(xb[1] & 0xffff) * bf2f(db[1] & 0xffff), bf2f(xb[1] >> 16) * bf2f(db[1] >> 16)};
    }
    lds_bar();
#pragma unroll
    for (int d = 1; d < 4; ++d) {
        const unsigned a = L_RED + (w * 4 + (kq ^ d)) * 2048 + l16;
        ra += *(LAS const f32x4*)(L + a); rb += *(LAS const f32x4*)(L + a + 1024);
    }
    const rsrc_t rsO = mkrs(out, SEQ * DM * 4);
    __builtin_amdgcn_raw_buffer_store_b128(__builtin_bit_cast(u32x4, ra), rsO, (tok * DM + colA) * 4, 0, 0);
    if (kq < 3) __builtin_amdgcn_raw_buffer_store_b128(__builtin_bit_cast(u32x4, rb), rsO, (tok * DM + colA + 8) * 4, 0, 0);
    STAMP(11);
}

extern "C" void kernel_launch(void* const* d_in, const int* in_sizes, int n_in, void* d_out, int out_size, void* d_ws, size_t ws_size,
                              hipStream_t stream) {
    const uint16_t* const* in = (const uint16_t* const*)d_in;
    gated_mix_kernel<<<256, 512, 0, stream>>>(in[0], in[1], in[2], in[3], in[4], in[6], in[7], in[8], in[9], in[10], in[11], in[12], in[13], in[14],
                                              (float*)d_out, (char*)d_ws);
}
```

```cpp
#define CPREF
#include <hip/hip_runtime.h>
#include <hip/hip_bf16.h>
#include <stdint.h>

typedef __bf16 bf16x8 __attribute__((ext_vector_type(8)));
typedef __bf16 bf16x2 __attribute__((ext_vector_type(2)));
typedef float f32x16 __attribute__((ext_vector_type(16)));
typedef float f32x4 __attribute__((ext_vector_type(4)));
typedef float f32x2 __attribute__((ext_vector_type(2)));
typedef unsigned u32x4 __attribute__((ext_vector_type(4)));
typedef unsigned u32x2 __attribute__((ext_vector_type(2)));
typedef short s16x4 __attribute__((ext_vector_type(4)));
typedef short s16x8 __attribute__((ext_vector_type(8)));
typedef __amdgpu_buffer_rsrc_t rsrc_t;
#define LAS __attribute__((address_space(3)))
#define DI __device__ __forceinline__
#define SBAR() __builtin_amdgcn_sched_barrier(0)
#define PIN(x) asm volatile("" : "+v"(x))
#define MFMA32(a, b, c) __builtin_amdgcn_mfma_f32_32x32x16_bf16((a), (b), (c), 0, 0, 0)
#define MFMA16(a, b, c) __builtin_amdgcn_mfma_f32_16x16x32_bf16((a), (b), (c), 0, 0, 0)
#define SC1 16
#define OAUX 0

#define MAP42 1

constexpr int SEQ = 2048, DM = 448, ROWB = DM * 2;
constexpr unsigned WS_KVCNT = 0;
constexpr unsigned WS_HCNT = 16384;
constexpr unsigned WS_X = 20480;
constexpr unsigned OOB = 0x7FFFFFF0u;
constexpr unsigned WS_KV = 65536;
constexpr unsigned WS_E = WS_KV + 262144;
constexpr unsigned WS_H = WS_E + 131072;
constexpr unsigned WS_O = WS_H + 32 * 114688;
constexpr unsigned WS_END = WS_O + 32 * 65536;
constexpr unsigned WS_STAMP = WS_END;

constexpr unsigned L_CH = 30720, L_CHW = 8192, L_CHB = 16384;
constexpr unsigned L_CRW = 122880, L_CRX = 148480;
constexpr unsigned L_RING = 106496;
constexpr unsigned L_QSH = 155648, L_G = 100352, L_FLAG = 100608;
constexpr unsigned L_C = 0, L_CIM = 50176, L_HSCR = 155648;
constexpr unsigned L_WO = 106496, L_MRG = 146432, L_LSUM = 162816, L_RED = 0;
constexpr unsigned LDS_BYTES = 163840;
constexpr unsigned SPIN_CAP = 1u << 14;

DI unsigned cvt2(float a, float b) { f32x2 v = {a, b}; return __builtin_bit_cast(unsigned, __builtin_convertvector(v, bf16x2)); }
DI float bf2f(unsigned h) { return __uint_as_float(h << 16); }
DI float bfround(float x) { return __uint_as_float(cvt2(x, 0.f) << 16); }
DI rsrc_t mkrs(const void* p, unsigned bytes) { return __builtin_amdgcn_make_buffer_rsrc((void*)p, (short)0, (int)bytes, 0x00020000); }
DI void dma(rsrc_t rs, unsigned voff, unsigned soff, unsigned lds) {
    asm volatile("s_mov_b32 m0, %0\n\ts_nop 0\n\tbuffer_load_dwordx4 %1, %2, %3 offen lds" :: "s"(lds), "v"(voff), "s"(rs), "s"(soff) : "memory"); }
DI void dma_sc1(rsrc_t rs, unsigned voff, unsigned soff, unsigned lds) {
    asm volatile("s_mov_b32 m0, %0\n\ts_nop 0\n\tbuffer_load_dwordx4 %1, %2, %3 offen sc1 lds" :: "s"(lds), "v"(voff), "s"(rs), "s"(soff) : "memory"); }
template <int N> DI void wait_bar() { asm volatile("s_waitcnt vmcnt(%0) lgkmcnt(0)\n\ts_barrier" :: "i"(N) : "memory"); }
template <int N> DI void wait_vm() { asm volatile("s_waitcnt vmcnt(%0)" :: "i"(N) : "memory"); }
DI void lds_bar() { asm volatile("s_waitcnt lgkmcnt(0)\n\ts_barrier" ::: "memory"); }
DI bf16x8 ld128(LAS char* L, unsigned off) { return __builtin_bit_cast(bf16x8, *(LAS const u32x4*)(L + off)); }
DI s16x4 ldtr(LAS char* L, unsigned off) { return __builtin_amdgcn_ds_read_tr16_b64_v4i16((LAS s16x4*)(L + off)); }
DI bf16x8 ldtr2(LAS char* L, unsigned off0, unsigned off1) {
    s16x4 a = ldtr(L, off0), b = ldtr(L, off1); return __builtin_bit_cast(bf16x8, __builtin_shufflevector(a, b, 0, 1, 2, 3, 4, 5, 6, 7)); }
DI bf16x8 asbf(u32x4 v) { return __builtin_bit_cast(bf16x8, v); }
DI unsigned nosent(unsigned v) { return v < 0xFFFFFFFEu ? v : 0xFFFFFFFEu; }
DI unsigned atomic_read(rsrc_t rs, unsigned voff) { return (unsigned)__builtin_amdgcn_raw_ptr_buffer_atomic_add_i32(0, rs, (int)voff, 0, SC1); }
DI void atomic_inc(rsrc_t rs, unsigned voff) { asm volatile("buffer_atomic_add %0, %1, %2, 0 offen sc1" :: "v"(1u), "v"(voff), "s"(rs) : "memory"); }
struct cf { float r, i; };
DI cf cmul(cf a, cf b) { return {a.r * b.r - a.i * b.i, a.r * b.i + a.i * b.r}; }
DI cf cfma(cf a, cf b, cf c) { return {__builtin_fmaf(a.r, b.r, __builtin_fmaf(-a.i, b.i, c.r)), __builtin_fmaf(a.r, b.i, __builtin_fmaf(a.i, b.r, c.i))}; }
DI float shfl(float v, int src) { return __int_as_float(__builtin_amdgcn_ds_bpermute(src << 2, __float_as_int(v))); }
DI float rowmax16(const f32x16& s) {
    float a = fmaxf(fmaxf(s[0], s[1]), s[2]), b = fmaxf(fmaxf(s[3], s[4]), s[5]);
    a = fmaxf(fmaxf(a, s[6]), s[7]); b = fmaxf(fmaxf(b, s[8]), s[9]); a = fmaxf(fmaxf(a, s[10]), s[11]); b = fmaxf(fmaxf(b, s[12]), s[13]);
    a = fmaxf(fmaxf(a, s[14]), s[15]); a = fmaxf(a, b);
    auto rr = __builtin_amdgcn_permlane32_swap(__float_as_uint(a), __float_as_uint(a), false, false);
    return fmaxf(__uint_as_float(rr[0]), __uint_as_float(rr[1])); }
__host__ __device__ constexpr int kvpos(int n, int sub) {
    return sub == 0 ? (n < 2 ? 2 * n : 4 * n - 3) : sub == 1 ? (n < 2 ? 2 * n + 1 : 4 * n - 1)
         : sub == 2 ? (n == 0 ? 4 : n == 31 ? 126 : 4 * n + 4) : (n == 30 ? 125 : n == 31 ? 127 : 4 * n + 6); }
__host__ __device__ constexpr unsigned kvlds(int pos) { return (((pos >> 3) + 1) % 3) * 16384 + (pos & 7) * 1024; }
DI unsigned kvglob(int half, int pos) { return WS_KV + (pos >> 3) * 16384 + half * 8192 + (pos & 7) * 1024; }
DI void set_shift(u32x4& q1, float c, int hi) {
    const float c1 = bfround(c), c2 = bfround(c - c1), c3 = c - c1 - c2;
    if (hi) { q1[2] = cvt2(-c1, -c2); q1[3] = cvt2(-c3, 0.f); } }

#define STAMP(k)
#define STAMP2(k)

__global__ void __launch_bounds__(512, 2)
gated_mix_kernel(const uint16_t* __restrict__ x, const uint16_t* __restrict__ wq, const uint16_t* __restrict__ wk, const uint16_t* __restrict__ wv,
                 const uint16_t* __restrict__ wo, const uint16_t* __restrict__ lre, const uint16_t* __restrict__ lim,
                 const uint16_t* __restrict__ bre, const uint16_t* __restrict__ bim, const uint16_t* __restrict__ cre, const uint16_t* __restrict__ cim,
                 const uint16_t* __restrict__ logdt, const uint16_t* __restrict__ dskip, const uint16_t* __restrict__ gate,
                 float* __restrict__ out, char* __restrict__ ws) {
    __shared__ __attribute__((aligned(16))) char smem[LDS_BYTES];
    LAS char* L = (LAS char*)smem;
    const unsigned l0 = (unsigned)(uintptr_t)L;
    const int lane = threadIdx.x & 63, w = __builtin_amdgcn_readfirstlane(threadIdx.x >> 6);
    const int b = blockIdx.x;
    const int blk = ((b & 7) >> 1) * 8 + (b >> 5), j = (b & 1) * 4 + ((b >> 3) & 3);
    const int th = w & 1, hd = (w >> 1) & 1, kh = w >> 2, kq = w >> 1;
    const int r32 = lane & 31, hi = lane >> 5, r16 = lane & 15, G = lane >> 4;
    const int tq = r16 >> 2, tp = lane & 3;
    const rsrc_t rsX = mkrs(x, SEQ * ROWB), rsWS = mkrs(ws, WS_END);
    STAMP(0);

    { unsigned long long pc; asm volatile("s_getpc_b64 %0" : "=s"(pc));
      const rsrc_t rsC_ = mkrs((const void*)(pc & ~255ull), 33 * 1024);
      asm volatile("buffer_load_dword v254, %0, %1, 0 offen" :: "v"((w * 64 + lane) * 64), "s"(rsC_) : "memory", "v254"); }
    __builtin_amdgcn_raw_buffer_store_b128((u32x4){~0u, ~0u, ~0u, ~0u}, rsWS, lane * 16, WS_O + blk * 65536 + ((th * 16 + 2 * j + hd) * 2 + kh) * 1024, SC1);
    const int sl = lane & 7, r8 = lane >> 3;
    u32x4 q0 = {0u, 0u, 0u, 0u}, q1 = q0;
    f32x4 ur[4] = {}, ui[4] = {};
    cf lam = {0.f, 0.f}, lam64 = lam, wlo = lam, whi = lam, zG = lam;
    f32x2 inr = {0.f, 0.f}, ini = inr;
    auto xswz = [](int row) { return ((row >> 1) & 7) ^ ((row & 16) ? 7 : 0) ^ ((row & 32) ? 6 : 0); };
    if (w < 4) {
        f32x16 qa = {};
        const int qrow = 32 * th + r32;
        const unsigned a0 = qrow * 128 + ((hi ^ xswz(qrow)) << 4);
        const unsigned a1 = L_CHW + (8 * hi + tq) * 128 + ((56 * hd + 32 * (G & 1) + 8 * tp) ^ (((tq >> 1) & 1) << 6));
        auto qchunk = [&](int c) {
            const unsigned s = (c & 3) * L_CH;
#pragma unroll
            for (int ks = 0; ks < 4; ++ks) {
                const bf16x8 xf = ld128(L, s + (a0 ^ (ks << 5)));
                const bf16x8 wf = ldtr2(L, s + a1 + ks * 16 * 128, s + a1 + (ks * 16 + 4) * 128);
                qa = MFMA32(wf, xf, qa);
            } };
        auto qfinish = [&]() {
            const float cs = 0.18898223650461361f * 1.4426950408889634f;
            if (hd == 1 && hi) *(LAS float*)(L + L_G + qrow * 4) = 1.0f / (1.0f + expf(-qa[12]));
            q0 = (u32x4){cvt2(qa[0] * cs, qa[1] * cs), cvt2(qa[2] * cs, qa[3] * cs), cvt2(qa[4] * cs, qa[5] * cs), cvt2(qa[6] * cs, qa[7] * cs)};
            q1 = (u32x4){cvt2(qa[8] * cs, qa[9] * cs), cvt2(qa[10] * cs, qa[11] * cs), cvt2(qa[12] * cs, qa[13] * cs), cvt2(qa[14] * cs, qa[15] * cs)};
            if (hi) { q1[2] = 0u; q1[3] = 0u; }
            *(LAS u32x4*)(L + L_QSH + w * 2048 + lane * 16) = q0;
            *(LAS u32x4*)(L + L_QSH + w * 2048 + 1024 + lane * 16) = q1; };
        if (w == 0) {
            const bool master = b == 0;
            const unsigned xoff = WS_X + (b & 15) * 128, bump = (master && lane < 16) ? WS_X + lane * 128 : OOB;
            unsigned x0, g0, g1, g2, g3, g4, g5, g6;
            __builtin_amdgcn_raw_buffer_store_b32(0u, rsWS, master ? lane * 128 : OOB, 0, SC1);
            __builtin_amdgcn_raw_buffer_store_b32(0u, rsWS, master ? lane * 128 + 8192 : OOB, 0, SC1);
            __builtin_amdgcn_raw_buffer_store_b32(0u, rsWS, (master && lane < 32) ? lane * 128 + 16384 : OOB, 0, SC1);
            x0 = 0u;
            asm volatile("buffer_atomic_add %0, %1, %2, 0 offen sc0 sc1" : "+v"(x0) : "v"(lane == 0 ? xoff : OOB), "s"(rsWS) : "memory");
            asm volatile("global_load_ushort %0, %7, off\n\tglobal_load_ushort %1, %7, off offset:128\n\tglobal_load_ushort %2, %7, off offset:256\n\t"
                         "global_load_ushort %3, %7, off offset:384\n\tglobal_load_ushort %4, %7, off offset:512\n\tglobal_load_ushort %5, %7, off offset:640\n\t"
                         "global_load_ushort %6, %7, off offset:768"
                         : "=&v"(g0), "=&v"(g1), "=&v"(g2), "=&v"(g3), "=&v"(g4), "=&v"(g5), "=&v"(g6) : "v"(gate + lane) : "memory");
            const int kind = j & 1, tg = j >> 1;
            const rsrc_t rsWK = mkrs(kind ? wv : wk, DM * 56);
            const unsigned vxa = r8 * ROWB + ((sl ^ (r8 >> 1)) << 4), vxb = (vxa ^ 64) + 8 * ROWB, xs = (blk * 64 + tg * 16) * ROWB;
#pragma unroll
            for (int c = 0; c < 7; ++c) {
                dma(rsX, vxa, xs + c * 128, l0 + L_CRX + c * 2048); dma(rsX, vxb, xs + c * 128, l0 + L_CRX + c * 2048 + 1024);
                dma(rsWK, lane * 16, (4 * c) * 1024, l0 + L_CRW + (4 * c) * 1024);
                if (c < 6) { dma(rsWK, lane * 16, (4 * c + 1) * 1024, l0 + L_CRW + (4 * c + 1) * 1024); dma(rsWK, lane * 16, (4 * c + 2) * 1024, l0 + L_CRW + (4 * c + 2) * 1024);
                             dma(rsWK, lane * 16, (4 * c + 3) * 1024, l0 + L_CRW + (4 * c + 3) * 1024); }
            }
            asm volatile("s_barrier" ::: "memory");
            wait_vm<39>();
            atomic_inc(rsWS, bump);
            asm volatile("s_waitcnt vmcnt(1)" : "+v"(x0), "+v"(g0), "+v"(g1), "+v"(g2), "+v"(g3), "+v"(g4), "+v"(g5), "+v"(g6) :: "memory");
            STAMP(1);
            auto gate_write = [&](int c, unsigned g16) {
                *(LAS uint16_t*)(L + (c & 3) * L_CH + L_CHW + lane * 128 + (112 ^ (((lane >> 1) & 1) << 6))) = (uint16_t)g16; };
            gate_write(0, g0); gate_write(1, g1); gate_write(2, g2); gate_write(3, g3);
            const int cg0 = kind ? tp : 4 * (tp >> 1) + (tp & 1), cg1 = kind ? 4 + tp : 4 * (tp >> 1) + 2 + (tp & 1);
            const unsigned aw0 = L_CRW + (8 * G + tq) * 56 + cg0 * 8, aw1 = L_CRW + (8 * G + tq) * 56 + cg1 * 8, ax = L_CRX + r16 * 128 + ((G ^ (r16 >> 1)) << 4);
            const int T = 2 * blk + (tg >> 1), sg = tg & 1, half = T >> 5, n = T & 31;
            f32x4 d0 = {0.f, 0.f, 0.f, 0.f}, d1 = d0;
            if (kind == 0) {
#pragma unroll
                for (int ks = 0; ks < 14; ++ks) {
                    const bf16x8 xf = ld128(L, (ks >> 1) * 2048 + (ax ^ ((ks & 1) << 6)));
                    d0 = MFMA16(ldtr2(L, aw0 + ks * 32 * 56, aw0 + (ks * 32 + 4) * 56), xf, d0);
                    d1 = MFMA16(ldtr2(L, aw1 + ks * 32 * 56, aw1 + (ks * 32 + 4) * 56), xf, d1);
                }
                u32x4 kv = {cvt2(d0[0], d0[1]), cvt2(d0[2], d0[3]), cvt2(d1[0], d1[1]), cvt2(d1[2], d1[3])};
                if (G == 3) { kv[2] = 0x3F803F80u; kv[3] = 0x00003F80u; }
                const int row = 16 * sg + ((r16 & 3) | ((r16 & 4) << 1) | ((r16 & 8) >> 1));
                const unsigned base = (G >> 1) ? kvglob(half, kvpos(n, 1)) : kvglob(half, kvpos(n, 0));
                __builtin_amdgcn_raw_buffer_store_b128(kv, rsWS, base + ((G & 1) * 32 + row) * 16, 0, SC1);
            } else {
#pragma unroll
                for (int ks = 0; ks < 14; ++ks) {
                    const bf16x8 xf = ld128(L, (ks >> 1) * 2048 + (ax ^ ((ks & 1) << 6)));
                    d0 = MFMA16(xf, ldtr2(L, aw0 + ks * 32 * 56, aw0 + (ks * 32 + 4) * 56), d0);
                    d1 = MFMA16(xf, ldtr2(L, aw1 + ks * 32 * 56, aw1 + (ks * 32 + 4) * 56), d1);
                }
                u32x2 v0 = {cvt2(d0[0], d0[1]), cvt2(d0[2], d0[3])}, v1 = {cvt2(d1[0], d1[1]), cvt2(d1[2], d1[3])};
                if (r16 >= 12) { const unsigned o = r16 == 12 ? 0x3F803F80u : 0u; v1 = (u32x2){o, o}; }
                const unsigned base = kvglob(half, kvpos(n, 2 + sg)), a = ((G >> 1) * 32 + r16) * 16 + (G & 1) * 8;
                __builtin_amdgcn_raw_buffer_store_b64(v0, rsWS, a, base, SC1);
                __builtin_amdgcn_raw_buffer_store_b64(v1, rsWS, a + 256, base, SC1);
            }
            unsigned x1 = 0u;
            asm volatile("buffer_atomic_add %0, %1, %2, 0 offen sc0 sc1" : "+v"(x1) : "v"(lane == 0 ? xoff : OOB), "s"(rsWS) : "memory");
            atomic_inc(rsWS, bump);
            lds_bar(); qchunk(0);
            lds_bar(); gate_write(4, g4); qchunk(1);
            asm volatile("s_waitcnt vmcnt(0)" : "+v"(x1) :: "memory");
            {   const unsigned s0 = __builtin_amdgcn_readfirstlane(x0); unsigned s1 = __builtin_amdgcn_readfirstlane(x1);
                for (unsigned spin = 0; !master && s1 == s0 && spin < SPIN_CAP; ++spin) s1 = __builtin_amdgcn_readfirstlane(atomic_read(rsWS, lane == 0 ? xoff : OOB)); }
            atomic_inc(rsWS, lane < 8 ? WS_KVCNT + lane * 2048 + (b & 15) * 128 : OOB);
            atomic_inc(rsWS, bump);
            STAMP(2);
            lds_bar(); gate_write(5, g5); qchunk(2); atomic_inc(rsWS, bump);
            lds_bar(); gate_write(6, g6); qchunk(3); atomic_inc(rsWS, bump);
            lds_bar(); qchunk(4); atomic_inc(rsWS, bump);
            lds_bar(); qchunk(5); atomic_inc(rsWS, bump);
            lds_bar(); qchunk(6);
            STAMP(3);
            qfinish();
            if (lane == 0) *(LAS unsigned*)(L + L_FLAG) = 0u;
            for (unsigned spin = 0; spin < SPIN_CAP; ++spin) {
                atomic_inc(rsWS, bump);
                const unsigned c = atomic_read(rsWS, lane < 16 ? WS_KVCNT + (b & 7) * 2048 + lane * 128 : OOB);
                if (__all(c == 16 || lane >= 16)) break;
            }
        } else {
            const rsrc_t rsWQ = mkrs(wq, DM * ROWB);
            const int v = w - 1, p3 = min(6 + v, 7);
            const unsigned vx0 = (blk * 64 + 16 * v + r8) * ROWB + ((sl ^ xswz(16 * v + r8)) << 4), vx1 = (blk * 64 + 16 * v + 8 + r8) * ROWB + ((sl ^ xswz(16 * v + 8 + r8)) << 4);
            const int pw = sl ^ (((r8 >> 1) & 1) << 2);
            const unsigned vw = r8 * ROWB + 112 * j + (pw << 4);
            auto issue = [&](int c) {
                const unsigned s = l0 + (c & 3) * L_CH;
                dma(rsX, vx0, c * 128, s + v * 2048); dma(rsX, vx1, c * 128, s + v * 2048 + 1024);
                if (pw != 7) { dma(rsWQ, vw, (c * 64 + 8 * v) * ROWB, s + L_CHW + v * 1024); dma(rsWQ, vw, (c * 64 + 24 + 8 * v) * ROWB, s + L_CHW + (3 + v) * 1024);
                               dma(rsWQ, vw, (c * 64 + 8 * p3) * ROWB, s + L_CHW + p3 * 1024); } };
            asm volatile("s_barrier" ::: "memory");
            issue(0); issue(1); issue(2); issue(3);
            wait_bar<15>(); qchunk(0);
            wait_bar<10>(); issue(4); qchunk(1);
            wait_bar<10>(); issue(5); qchunk(2);
            wait_bar<10>(); issue(6); qchunk(3);
            wait_bar<10>(); qchunk(4);
            wait_bar<5>(); qchunk(5);
            wait_bar<0>(); qchunk(6);
            STAMP(3);
            qfinish();
        }
    } else {
        const int u = w - 4, st = min(16 * u + r16, 55);
        const unsigned eoff = (G == 0 && 16 * u + r16 < 56) ? st * 8 : OOB;
        __builtin_amdgcn_raw_buffer_store_b64((u32x2){~0u, ~0u}, rsWS, eoff, WS_E + (blk * DM + 56 * j) * 8, SC1);
        unsigned p0, p1, p2;
        asm volatile("global_load_ushort %0, %3, off\n\tglobal_load_ushort %1, %4, off\n\tglobal_load_ushort %2, %5, off"
                     : "=&v"(p0), "=&v"(p1), "=&v"(p2) : "v"(lre + 56 * j + st), "v"(lim + 56 * j + st), "v"(logdt + 56 * j + st) : "memory");
        const rsrc_t rsBR = mkrs(bre, DM * ROWB), rsBI = mkrs(bim, DM * ROWB);
        const rsrc_t rs2 = mkrs(u < 3 ? bre : x, u < 3 ? DM * ROWB : SEQ * ROWB), rs3 = mkrs(u < 3 ? bim : x, u < 3 ? DM * ROWB : SEQ * ROWB);
        const unsigned vb = (56 * j + 8 * u + r8) * ROWB + ((sl ^ ((4 * u + (r8 >> 1)) & 7)) << 4);
        const unsigned v2 = u < 3 ? vb + 32 * ROWB : (blk * 64 + 48 + r8) * ROWB + ((sl ^ xswz(48 + r8)) << 4);
        const unsigned v3 = u < 3 ? vb + 32 * ROWB : (blk * 64 + 56 + r8) * ROWB + ((sl ^ xswz(56 + r8)) << 4);
        const unsigned d2 = u < 3 ? L_CHB + (4 + u) * 1024 : 6 * 1024, d3 = u < 3 ? L_CHB + 7168 + (4 + u) * 1024 : 7 * 1024;
        auto issue = [&](int c) {
            const unsigned s = l0 + (c & 3) * L_CH;
            dma(rsBR, vb, c * 128, s + L_CHB + u * 1024); dma(rsBI, vb, c * 128, s + L_CHB + 7168 + u * 1024);
            dma(rs2, v2, c * 128, s + d2); dma(rs3, v3, c * 128, s + d3); };
        const unsigned a4 = L_CHB + st * 128 + ((G ^ ((st >> 1) & 7)) << 4);
        unsigned at[4];
#pragma unroll
        for (int m = 0; m < 4; ++m) { const int row = 16 * (r16 >> 2) + 2 * m + ((r16 & 3) >> 1) + 8 * (r16 & 1); at[m] = row * 128 + ((G ^ xswz(row)) << 4); }
        auto uchunk = [&](int c) {
            const unsigned s = (c & 3) * L_CH;
#pragma unroll
            for (int ks = 0; ks < 2; ++ks) {
                const bf16x8 br = ld128(L, s + (a4 ^ (ks << 6))), bi = ld128(L, s + 7168 + (a4 ^ (ks << 6)));
#pragma unroll
                for (int m = 0; m < 4; ++m) { const bf16x8 xf = ld128(L, s + (at[m] ^ (ks << 6))); ur[m] = MFMA16(xf, br, ur[m]); ui[m] = MFMA16(xf, bi, ui[m]); }
            } };
        asm volatile("s_barrier" ::: "memory");
        issue(0); issue(1); issue(2); issue(3);
        asm volatile("s_waitcnt vmcnt(16)" : "+v"(p0), "+v"(p1), "+v"(p2) :: "memory");
        const float lr = bf2f(p0), li = bf2f(p1), dt = bfround(expf(bf2f(p2)));
        const float er = expf(dt * lr); float sn, cs; sincosf(dt * li, &sn, &cs);
        lam = {er * cs, er * sn};
        const float inv = 1.0f / (lr * lr + li * li);
        const cf coef = {((lam.r - 1.0f) * lr + lam.i * li) * inv, (lam.i * lr - (lam.r - 1.0f) * li) * inv};
        const cf l2 = cmul(lam, lam), l4 = cmul(l2, l2), lam8 = cmul(l4, l4), lam16 = cmul(lam8, lam8), l32 = cmul(lam16, lam16);
        lam64 = cmul(l32, l32);
        const cf l128 = cmul(lam64, lam64), l256 = cmul(l128, l128), l512 = cmul(l256, l256), l1024 = cmul(l512, l512), one = {1.f, 0.f};
        wlo = cmul((G & 1) ? lam16 : one, (G & 2) ? l32 : one); whi = cmul(wlo, lam8);
        zG = cmul((G & 1) ? l512 : one, (G & 2) ? l1024 : one);
        wait_bar<12>(); uchunk(0);
        wait_bar<8>(); issue(4); uchunk(1);
        wait_bar<8>(); issue(5); uchunk(2);
        wait_bar<8>(); issue(6); uchunk(3);
        wait_bar<8>(); uchunk(4);
        wait_bar<4>(); uchunk(5);
        wait_bar<0>(); uchunk(6);
        STAMP(3);
        f32x2 hr = {0.f, 0.f}, hm = hr;
#pragma unroll
        for (int p = 0; p < 8; ++p) {
            const f32x2 a = {ur[p >> 1][2 * (p & 1)], ur[p >> 1][2 * (p & 1) + 1]}, c = {ui[p >> 1][2 * (p & 1)], ui[p >> 1][2 * (p & 1) + 1]};
            const f32x2 tr = coef.r * a - coef.i * c, ti = coef.r * c + coef.i * a;
            ur[p >> 1][2 * (p & 1)] = tr[0]; ur[p >> 1][2 * (p & 1) + 1] = tr[1]; ui[p >> 1][2 * (p & 1)] = ti[0]; ui[p >> 1][2 * (p & 1) + 1] = ti[1];
            const f32x2 nr = lam.r * hr - lam.i * hm + tr, ni = lam.r * hm + lam.i * hr + ti; hr = nr; hm = ni;
        }
        const cf elo = {hr[0], hm[0]}, e16 = cfma(lam8, elo, (cf){hr[1], hm[1]});
        const cf e0 = {shfl(e16.r, r16), shfl(e16.i, r16)}, e1 = {shfl(e16.r, r16 + 16), shfl(e16.i, r16 + 16)};
        const cf e2 = {shfl(e16.r, r16 + 32), shfl(e16.i, r16 + 32)}, e3 = {shfl(e16.r, r16 + 48), shfl(e16.i, r16 + 48)};
        const cf t2 = cfma(lam16, e0, e1), t3 = cfma(lam16, t2, e2), eb = cfma(lam16, t3, e3);
        const cf pg = G == 0 ? (cf){0.f, 0.f} : G == 1 ? e0 : G == 2 ? t2 : t3, ph = cfma(lam8, pg, elo);
        inr = (f32x2){pg.r, ph.r}; ini = (f32x2){pg.i, ph.i};
        __builtin_amdgcn_raw_buffer_store_b64((u32x2){nosent(__float_as_uint(eb.r)), nosent(__float_as_uint(eb.i))}, rsWS, eoff, WS_E + (blk * DM + 56 * j) * 8, SC1);
    }
    STAMP(4);
    lds_bar();
    STAMP(5);

    const unsigned l16 = lane * 16;
#define KVDMA(phi) do { dma_sc1(rsWS, l16, WS_KV + (phi) * 16384 + w * 2048, l0 + L_RING + (((phi) + 1) % 3) * 16384 + w * 2048); \
                        dma_sc1(rsWS, l16, WS_KV + (phi) * 16384 + w * 2048 + 1024, l0 + L_RING + (((phi) + 1) % 3) * 16384 + w * 2048 + 1024); } while (0)
    KVDMA(0); KVDMA(1); KVDMA(2);
    if (w >= 4) { q0 = *(LAS const u32x4*)(L + L_QSH + (w - 4) * 2048 + l16); q1 = *(LAS const u32x4*)(L + L_QSH + (w - 4) * 2048 + 1024 + l16); }
    const float gt = *(LAS const float*)(L + L_G + (32 * th + r32) * 4);
    const rsrc_t rsCR = mkrs(cre, DM * ROWB), rsCI = mkrs(cim, DM * ROWB);
    const int cw = min(w, 6);
    const unsigned vc = (56 * j + 8 * cw + r8) * ROWB + ((sl ^ ((4 * cw + (r8 >> 1)) & 7)) << 4);
    const unsigned kvb = L_RING + kh * 8192 + l16;
    wait_bar<4>();
    STAMP(6);
    f32x16 sA, sB, o;
    bf16x8 k0, k1, v0, v1;
    u32x4 pw0, pw1;
    {
        const bf16x8 c0 = ld128(L, L_RING + l16 + kvlds(0)), c1 = ld128(L, L_RING + l16 + kvlds(1));
        k0 = ld128(L, kvb + kvlds(0)); k1 = ld128(L, kvb + kvlds(1));
        sB = MFMA32(c0, asbf(q0), (f32x16){}); sB = MFMA32(c1, asbf(q1), sB);
        set_shift(q1, rowmax16(sB), hi);
        sA = MFMA32(k0, asbf(q0), (f32x16){}); sA = MFMA32(k1, asbf(q1), sA);
        k0 = ld128(L, kvb + kvlds(2)); k1 = ld128(L, kvb + kvlds(3)); v0 = ld128(L, kvb + kvlds(4));
    }
    u32x2 ev[8];
    u32x4 hf[14];
#define EX4(A, i) do { A[i] = __builtin_amdgcn_exp2f(A[i]); A[i + 1] = __builtin_amdgcn_exp2f(A[i + 1]); \
                       A[i + 2] = __builtin_amdgcn_exp2f(A[i + 2]); A[i + 3] = __builtin_amdgcn_exp2f(A[i + 3]); PIN(A); } while (0)
#define ITER(A, B, n, HOOK) do { SBAR(); \
    if ((n) < 31) { B = MFMA32(k0, asbf(q0), (f32x16){}); PIN(B); } SBAR(); \
    if ((n) < 30) k0 = ld128(L, kvb + kvlds(kvpos((n) + 2, 0))); \
    EX4(A, 0); SBAR(); \
    if ((n) > 0) { o = MFMA32(v1, asbf(pw1), o); PIN(o); } SBAR(); \
    v1 = ld128(L, kvb + kvlds(kvpos((n), 3))); \
    pw0[0] = cvt2(A[0], A[1]); pw0[1] = cvt2(A[2], A[3]); PIN(pw0); SBAR(); EX4(A, 4); SBAR(); \
    if ((n) < 31) { B = MFMA32(k1, asbf(q1), B); PIN(B); } SBAR(); \
    if ((n) < 30) k1 = ld128(L, kvb + kvlds(kvpos((n) + 2, 1))); \
    pw0[2] = cvt2(A[4], A[5]); pw0[3] = cvt2(A[6], A[7]); PIN(pw0); SBAR(); EX4(A, 8); SBAR(); \
    if ((n) > 0) o = MFMA32(v0, asbf(pw0), o); else o = MFMA32(v0, asbf(pw0), (f32x16){}); PIN(o); SBAR(); \
    pw1[0] = cvt2(A[8], A[9]); pw1[1] = cvt2(A[10], A[11]); PIN(pw1); SBAR(); EX4(A, 12); SBAR(); \
    HOOK; \
    if ((n) < 31) v0 = ld128(L, kvb + kvlds(kvpos((n) + 1, 2))); \
    pw1[2] = cvt2(A[12], A[13]); pw1[3] = cvt2(A[14], A[15]); PIN(pw1); SBAR(); } while (0)
#define ABL 0
#define STAGE(phi, N, EXTRA) do { ITER(sA, sB, 2 * (phi) - 2, { if (ABL == 0) wait_bar<N>(); if (ABL == 1) lds_bar(); STAMP2(phi); EXTRA; if (ABL < 3 && (phi) + 2 < 16) KVDMA((phi) + 2); }); ITER(sB, sA, 2 * (phi) - 1, {}); } while (0)
#define CD2(cc) do { if (ABL < 3) { dma(rsCR, vc, (cc) * 128, l0 + L_C + (cc) * 7168 + cw * 1024); dma(rsCI, vc, (cc) * 128, l0 + L_CIM + (cc) * 7168 + cw * 1024); } } while (0)
    const rsrc_t rsE = mkrs(ws + WS_E + 56 * j * 8, 32 * DM * 8 - 56 * j * 8);
    const int ebase = (blk - 1 - 8 * G) * (DM * 8) + min((w & 3) * 16 + r16, 55) * 8;
#define ELD() do { _Pragma("unroll") for (int m = 0; m < 8; ++m) ev[m] = __builtin_amdgcn_raw_buffer_load_b64(rsE, ebase - m * (DM * 8), 0, SC1); } while (0)
    STAGE(1, 2, CD2(0));
    STAGE(2, 4, CD2(1));
    STAGE(3, 4, { CD2(2); if (w >= 4) ELD(); });
    STAGE(4, 4, CD2(3));
    f32x2 hr, hm;
    if (w >= 4) {
        for (unsigned spin = 0; spin < SPIN_CAP; ++spin) {
            unsigned mx = 0;
#pragma unroll
            for (int m = 0; m < 8; ++m) mx = max(mx, max(ev[m][0], ev[m][1]));
            if (__all(mx != ~0u)) break;
            ELD();
        }
        cf part = {__uint_as_float(ev[7][0]), __uint_as_float(ev[7][1])};
#pragma unroll
        for (int m = 6; m >= 0; --m) part = cfma(lam64, part, (cf){__uint_as_float(ev[m][0]), __uint_as_float(ev[m][1])});
        part = cmul(zG, part);
        if (ebase < 0) part = (cf){0.f, 0.f};
        part.r += shfl(part.r, lane ^ 16); part.i += shfl(part.i, lane ^ 16); part.r += shfl(part.r, lane ^ 32); part.i += shfl(part.i, lane ^ 32);
        const cf ilo = cfma(wlo, part, (cf){inr[0], ini[0]}), ihi = cfma(whi, part, (cf){inr[1], ini[1]});
        hr = (f32x2){ilo.r, ihi.r}; hm = (f32x2){ilo.i, ihi.i};
    }
    STAGE(5, 4, CD2(4));
    u32x4 pr0, pr1, pi0, pi1;
    if (w >= 4) {
#pragma unroll
        for (int p = 0; p < 8; ++p) {
            const f32x2 a = {ur[p >> 1][2 * (p & 1)], ur[p >> 1][2 * (p & 1) + 1]}, c = {ui[p >> 1][2 * (p & 1)], ui[p >> 1][2 * (p & 1) + 1]};
            const f32x2 nr = lam.r * hr - lam.i * hm + a, ni = lam.r * hm + lam.i * hr + c; hr = nr; hm = ni;
            if (p < 4) { pr0[p & 3] = cvt2(hr[0], hr[1]); pi0[p & 3] = cvt2(-hm[0], -hm[1]); } else { pr1[p & 3] = cvt2(hr[0], hr[1]); pi1[p & 3] = cvt2(-hm[0], -hm[1]); }
        }
    }
    STAGE(6, 4, CD2(5));
    if (w >= 4) {
        const unsigned sb = L_HSCR + (w - 4) * 2048, wa = sb + r16 * 128;
        const int tok = (lane & 48) + (r16 >> 1) + 8 * (lane & 1);
        const unsigned ga = (tok >> 5) * 57344 + (tok & 31) * 16, gb = w < 7 ? ga + 1024 : OOB, gs = WS_H + blk * 114688 + (7 * j + 2 * (w - 4)) * 1024;
        const unsigned pc = 2 * G + (tp >> 1), ra = sb + tq * 128 + ((pc ^ tq) << 4) + 8 * (tp & 1), rb = sb + (4 + tq) * 128 + ((pc ^ (4 + tq)) << 4) + 8 * (tp & 1);
#pragma unroll
        for (int pl = 0; pl < 2; ++pl) {
            *(LAS u32x4*)(L + wa + (((2 * G) ^ (r16 & 7)) << 4)) = pl ? pi0 : pr0;
            *(LAS u32x4*)(L + wa + (((2 * G + 1) ^ (r16 & 7)) << 4)) = pl ? pi1 : pr1;
            asm volatile("" ::: "memory");
            const bf16x8 c0 = ldtr2(L, ra, rb), c1 = ldtr2(L, ra + 1024, rb + 1024);
            asm volatile("" ::: "memory");
            __builtin_amdgcn_raw_buffer_store_b128(__builtin_bit_cast(u32x4, c0), rsWS, ga + pl * 512, gs, SC1);
            __builtin_amdgcn_raw_buffer_store_b128(__builtin_bit_cast(u32x4, c1), rsWS, gb + pl * 512, gs, SC1);
        }
    }
    STAGE(7, 4, CD2(6));
    STAGE(8, 4, {});
    STAGE(9, 2, atomic_inc(rsWS, (w == 4 && lane == 0) ? WS_HCNT + blk * 128 : OOB));
    STAGE(10, 3, {});
    STAGE(11, 2, {});
    if (w == 0) { for (unsigned spin = 0; spin < SPIN_CAP; ++spin) if (__builtin_amdgcn_readfirstlane(atomic_read(rsWS, lane == 0 ? WS_HCNT + blk * 128 : OOB)) == 8) break; }
#define HLD(m) hf[m] = __builtin_amdgcn_raw_buffer_load_b128(rsWS, l16, WS_H + blk * 114688 + th * 57344 + (4 * (m) + kq) * 1024, SC1)
    STAGE(12, 2, { HLD(0); HLD(1); HLD(2); HLD(3); });
    STAGE(13, 6, { HLD(4); HLD(5); HLD(6); HLD(7); });
    STAGE(14, 6, { HLD(8); HLD(9); HLD(10); });
    STAGE(15, 3, { HLD(11); HLD(12); HLD(13); });
    ITER(sA, sB, 30, {}); ITER(sB, sA, 31, {});
    o = MFMA32(v1, asbf(pw1), o);
    STAMP(7);

    const rsrc_t rsWO = mkrs(wo, DM * ROWB);
    const unsigned vwo = (lane >> 3) * ROWB + 112 * j + (((lane & 7) ^ (((lane >> 4) & 1) << 2)) << 4);
    auto wodma = [&](int p) { dma(rsWO, vwo, p * 8 * ROWB, l0 + L_WO + p * 1024); };
    auto merge_write = [&]() {
        const int g8 = kh ? 0 : 8;
        *(LAS f32x4*)(L + L_MRG + w * 2048 + l16) = kh ? (f32x4){o[0], o[1], o[2], o[3]} : (f32x4){o[8], o[9], o[10], o[11]};
        *(LAS f32x4*)(L + L_MRG + w * 2048 + 1024 + l16) = kh ? (f32x4){o[4], o[5], o[6], o[7]} : (f32x4){o[12], o[13], o[14], o[15]};
        if (hi) *(LAS float*)(L + L_LSUM + w * 128 + r32 * 4) = o[12];
        (void)g8; };
    { const bool bad = ABL == 0 && hi && !(o[12] <= 1.8446744e19f);
      if (__any(bad) && lane == 0) *(LAS unsigned*)(L + L_FLAG) = 1u; }
    merge_write();
    lds_bar();
    if (__builtin_expect(__builtin_amdgcn_readfirstlane(*(LAS const unsigned*)(L + L_FLAG)) != 0u, 0)) {
        if (hi) { q1[2] = 0u; q1[3] = 0u; }
        float mx = -INFINITY;
        for (int n = 0; n < 32; ++n) {
            const bf16x8 a = asbf(__builtin_amdgcn_raw_buffer_load_b128(rsWS, l16, kvglob(kh, kvpos(n, 0)), SC1));
            const bf16x8 c = asbf(__builtin_amdgcn_raw_buffer_load_b128(rsWS, l16, kvglob(kh, kvpos(n, 1)), SC1));
            f32x16 s = MFMA32(a, asbf(q0), (f32x16){}); s = MFMA32(c, asbf(q1), s);
            mx = fmaxf(mx, rowmax16(s));
        }
        lds_bar();
        if (hi) *(LAS float*)(L + L_LSUM + w * 128 + r32 * 4) = mx;
        lds_bar();
        mx = fmaxf(mx, *(LAS const float*)(L + L_LSUM + (w ^ 4) * 128 + r32 * 4));
        set_shift(q1, mx, hi);
        o = (f32x16){};
        for (int n = 0; n < 32; ++n) {
            const bf16x8 a = asbf(__builtin_amdgcn_raw_buffer_load_b128(rsWS, l16, kvglob(kh, kvpos(n, 0)), SC1));
            const bf16x8 c = asbf(__builtin_amdgcn_raw_buffer_load_b128(rsWS, l16, kvglob(kh, kvpos(n, 1)), SC1));
            const bf16x8 d = asbf(__builtin_amdgcn_raw_buffer_load_b128(rsWS, l16, kvglob(kh, kvpos(n, 2)), SC1));
            const bf16x8 e = asbf(__builtin_amdgcn_raw_buffer_load_b128(rsWS, l16, kvglob(kh, kvpos(n, 3)), SC1));
            f32x16 s = MFMA32(a, asbf(q0), (f32x16){}); s = MFMA32(c, asbf(q1), s);
#pragma unroll
            for (int r = 0; r < 16; ++r) s[r] = __builtin_amdgcn_exp2f(s[r]);
            o = MFMA32(d, asbf((u32x4){cvt2(s[0], s[1]), cvt2(s[2], s[3]), cvt2(s[4], s[5]), cvt2(s[6], s[7])}), o);
            o = MFMA32(e, asbf((u32x4){cvt2(s[8], s[9]), cvt2(s[10], s[11]), cvt2(s[12], s[13]), cvt2(s[14], s[15])}), o);
        }
        lds_bar();
        merge_write();
        lds_bar();
    }
    {
        const f32x4 m0 = *(LAS const f32x4*)(L + L_MRG + (w ^ 4) * 2048 + l16), m1 = *(LAS const f32x4*)(L + L_MRG + (w ^ 4) * 2048 + 1024 + l16);
        const float ls = *(LAS const float*)(L + L_LSUM + w * 128 + r32 * 4) + *(LAS const float*)(L + L_LSUM + (w ^ 4) * 128 + r32 * 4);
        const float sc = gt / ls;
        const f32x4 t0 = ((kh ? (f32x4){o[8], o[9], o[10], o[11]} : (f32x4){o[0], o[1], o[2], o[3]}) + m0) * sc;
        const f32x4 t1 = ((kh ? (f32x4){o[12], o[13], o[14], o[15]} : (f32x4){o[4], o[5], o[6], o[7]}) + m1) * sc;
        u32x4 ov = {nosent(cvt2(t0[0], t0[1])), nosent(cvt2(t0[2], t0[3])), nosent(cvt2(t1[0], t1[1])), nosent(cvt2(t1[2], t1[3]))};
        if (kh && hi) { ov[2] = 0u; ov[3] = 0u; }
        __builtin_amdgcn_raw_buffer_store_b128(ov, rsWS, l16, WS_O + blk * 65536 + ((th * 16 + 2 * j + hd) * 2 + kh) * 1024, SC1);
    }
#pragma unroll
    for (int t = 0; t < 5; ++t) wodma(min(t * 8 + w, 38));
    lds_bar();
    wodma(39 + w); wodma(47 + w); if (w == 0) wodma(55);
    STAMP(8);

    f32x16 y0 = {}, y1 = {};
    {
        const int ra = r32, rb = min(32 + r32, 55);
        const unsigned ca = hi * L_CIM + ra * 128 + ((kq ^ ((ra >> 1) & 7)) << 4), cb = hi * L_CIM + rb * 128 + ((kq ^ ((rb >> 1) & 7)) << 4);
#pragma unroll
        for (int m = 0; m < 14; ++m) {
            const bf16x8 fa = ld128(L, L_C + (m >> 1) * 7168 + (ca ^ ((m & 1) << 6))), fb = ld128(L, L_C + (m >> 1) * 7168 + (cb ^ ((m & 1) << 6)));
            y0 = MFMA32(fa, asbf(hf[m]), y0); y1 = MFMA32(fb, asbf(hf[m]), y1);
        }
        const float s2 = 2.0f * (1.0f - gt);
        y0 *= s2; y1 *= s2;
    }
    const int tok = blk * 64 + 32 * th + r32, colA = 56 * j + 16 * kq + 4 * hi;
    const u32x2 xa = __builtin_amdgcn_raw_buffer_load_b64(rsX, tok * ROWB + colA * 2, 0, 0), xb = __builtin_amdgcn_raw_buffer_load_b64(rsX, tok * ROWB + colA * 2 + 16, 0, 0);
    const rsrc_t rsD = mkrs(dskip, ROWB);
    const u32x2 da = __builtin_amdgcn_raw_buffer_load_b64(rsD, colA * 2, 0, 0), db = __builtin_amdgcn_raw_buffer_load_b64(rsD, colA * 2 + 16, 0, 0);
    STAMP(9);
    wait_bar<0>();
    u32x4 of[8];
    const unsigned ob = WS_O + blk * 65536 + (th * 16 + 4 * kq) * 2048;
#pragma unroll
    for (int t = 0; t < 8; ++t) of[t] = __builtin_amdgcn_raw_buffer_load_b128(rsWS, l16 + t * 1024, ob, SC1);
    bf16x8 wf0[8], wf1[8];
    {
        const unsigned cx = (32 * (G & 1) + 8 * tp) ^ (((tq >> 1) & 1) << 6), nb = L_WO + (112 * kq + tq) * 128, wa = nb + hi * 512;
#pragma unroll
        for (int t = 0; t < 8; ++t) { const unsigned ro = (28 * (t >> 1) + 16 * (t & 1)) * 128, second = ((t & 1) ? nb : wa) + ro + 1024;
            wf0[t] = ldtr2(L, wa + ro + cx, second + cx); wf1[t] = ldtr2(L, wa + ro + (cx ^ 64), second + (cx ^ 64)); }
    }
    for (unsigned spin = 0; spin < SPIN_CAP; ++spin) {
        unsigned mx = 0;
#pragma unroll
        for (int t = 0; t < 8; ++t) mx = max(max(mx, max(of[t][0], of[t][1])), max(of[t][2], of[t][3]));
        if (__all(mx != ~0u)) break;
#pragma unroll
        for (int t = 0; t < 8; ++t) of[t] = __builtin_amdgcn_raw_buffer_load_b128(rsWS, l16 + t * 1024, ob, SC1);
    }
    STAMP(10);
#pragma unroll
    for (int t = 0; t < 8; ++t) { y0 = MFMA32(wf0[t], asbf(of[t]), y0); y1 = MFMA32(wf1[t], asbf(of[t]), y1); }
#pragma unroll
    for (int d = 0; d < 4; ++d) {
        if (d != kq) {
            const f32x16& y = (d >> 1) ? y1 : y0; const int e = 8 * (d & 1);
            const unsigned a = L_RED + ((2 * d + th) * 4 + kq) * 2048 + l16;
            *(LAS f32x4*)(L + a) = (f32x4){y[e], y[e + 1], y[e + 2], y[e + 3]};
            *(LAS f32x4*)(L + a + 1024) = (f32x4){y[e + 4], y[e + 5], y[e + 6], y[e + 7]};
        }
    }
    f32x4 ra, rb;
    if (kq == 0) { ra = (f32x4){y0[0], y0[1], y0[2], y0[3]}; rb = (f32x4){y0[4], y0[5], y0[6], y0[7]}; }
    else if (kq == 1) { ra = (f32x4){y0[8], y0[9], y0[10], y0[11]}; rb = (f32x4){y0[12], y0[13], y0[14], y0[15]}; }
    else if (kq == 2) { ra = (f32x4){y1[0], y1[1], y1[2], y1[3]}; rb = (f32x4){y1[4], y1[5], y1[6], y1[7]}; }
    else { ra = (f32x4){y1[8], y1[9], y1[10], y1[11]}; rb = (f32x4){y1[12], y1[13], y1[14], y1[15]}; }
    {
        const float g1 = 1.0f - gt;
        ra += g1 * (f32x4){bf2f(xa[0] & 0xffff) * bf2f(da[0] & 0xffff), bf2f(xa[0] >> 16) * bf2f(da[0] >> 16), bf2f(xa[1] & 0xffff) * bf2f(da[1] & 0xffff), bf2f(xa[1] >> 16) * bf2f(da[1] >> 16)};
        rb += g1 * (f32x4){bf2f(xb[0] & 0xffff) * bf2f(db[0] & 0xffff), bf2f(xb[0] >> 16) * bf2f(db[0] >> 16), bf2f(xb[1] & 0xffff) * bf2f(db[1] & 0xffff), bf2f(xb[1] >> 16) * bf2f(db[1] >> 16)};
    }
    lds_bar();
#pragma unroll
    for (int d = 1; d < 4; ++d) {
        const unsigned a = L_RED + (w * 4 + (kq ^ d)) * 2048 + l16;
        ra += *(LAS const f32x4*)(L + a); rb += *(LAS const f32x4*)(L + a + 1024);
    }
    const rsrc_t rsO = mkrs(out, SEQ * DM * 4);
    __builtin_amdgcn_raw_buffer_store_b128(__builtin_bit_cast(u32x4, ra), rsO, (tok * DM + colA) * 4, 0, OAUX);
    if (kq < 3) __builtin_amdgcn_raw_buffer_store_b128(__builtin_bit_cast(u32x4, rb), rsO, (tok * DM + colA + 8) * 4, 0, OAUX);
    STAMP(11);
    STAMP(12);
}

extern "C" void kernel_launch(void* const* d_in, const int* in_sizes, int n_in, void* d_out, int out_size, void* d_ws, size_t ws_size,
                              hipStream_t stream) {
    const uint16_t* const* in = (const uint16_t* const*)d_in;
    gated_mix_kernel<<<256, 512, 0, stream>>>(in[0], in[1], in[2], in[3], in[4], in[6], in[7], in[8], in[9], in[10], in[11], in[12], in[13], in[14],
                                              (float*)d_out, (char*)d_ws);
}
```

```cpp
#include <hip/hip_runtime.h>
#include <hip/hip_bf16.h>
#include <stdint.h>

typedef __bf16 bf16x8 __attribute__((ext_vector_type(8)));
typedef __bf16 bf16x2 __attribute__((ext_vector_type(2)));
typedef float f32x16 __attribute__((ext_vector_type(16)));
typedef float f32x4 __attribute__((ext_vector_type(4)));
typedef float f32x2 __attribute__((ext_vector_type(2)));
typedef unsigned u32x4 __attribute__((ext_vector_type(4)));
typedef unsigned u32x2 __attribute__((ext_vector_type(2)));
typedef short s16x4 __attribute__((ext_vector_type(4)));
typedef __amdgpu_buffer_rsrc_t rsrc_t;
#define LAS __attribute__((address_space(3)))
#define DI __device__ __forceinline__
#define SBAR() __builtin_amdgcn_sched_barrier(0)
#define PIN(x) asm volatile("" : "+v"(x))
#define PINS(x) asm volatile("" : "+s"(x))
#define MFMA32(a, b, c) __builtin_amdgcn_mfma_f32_32x32x16_bf16((a), (b), (c), 0, 0, 0)
#define MFMA16(a, b, c) __builtin_amdgcn_mfma_f32_16x16x32_bf16((a), (b), (c), 0, 0, 0)
#define UNROLL _Pragma("unroll")
#define SC1 16

#define OLDM 11
#define MAP42 1

constexpr int SEQ = 2048, DM = 448, ROWB = DM * 2;
constexpr unsigned WS_KVCNT = 0;
constexpr unsigned WS_HCNT = 16384;
constexpr unsigned WS_X = 20480;
constexpr unsigned OOB = 0x7FFFFFF0u;
constexpr unsigned WS_KV = 65536;
constexpr unsigned WS_E = WS_KV + 262144;
constexpr unsigned WS_H = WS_E + 131072;
constexpr unsigned WS_O = WS_H + 32 * 114688;
constexpr unsigned WS_END = WS_O + 32 * 65536;
constexpr unsigned WS_STAMP = WS_END;

constexpr unsigned L_X = 0, L_WQ = 57344, L_WK = 114688, L_BS = 140288;
constexpr unsigned L_KP = 156672;
constexpr unsigned L_KF = 100472, L_WQF = 100476, L_QF = 100984, L_C2 = 100988, L_FLAG = 101496;
constexpr int NQ = 10, NU = 6;
constexpr unsigned L_QSH = 114688, L_G = 122880;
constexpr unsigned L_C = 0, L_CIM = 50176;
constexpr unsigned L_LS = 102400, L_RO = 106496;
constexpr unsigned L_WO = 106496, L_RED = 0;
constexpr unsigned LDS_BYTES = 163840;
constexpr unsigned SPIN_CAP = 1u << 14;

DI unsigned cvt2(float a, float b) { f32x2 v = {a, b}; return __builtin_bit_cast(unsigned, __builtin_convertvector(v, bf16x2)); }
DI float bf2f(unsigned h) { return __uint_as_float(h << 16); }
DI float bfhi(unsigned h) { return __uint_as_float(h & 0xffff0000u); }
DI float bfround(float x) { return __uint_as_float(cvt2(x, 0.f) << 16); }
DI float fexp(float x) { return __builtin_amdgcn_exp2f(x * 1.4426950408889634f); }
DI rsrc_t mkrs(const void* p, unsigned bytes) { return __builtin_amdgcn_make_buffer_rsrc((void*)p, (short)0, (int)bytes, 0x00020000); }
DI void dma(rsrc_t rs, unsigned voff, unsigned soff, unsigned lds) {
    asm volatile("s_mov_b32 m0, %0\n\ts_nop 0\n\tbuffer_load_dwordx4 %1, %2, %3 offen lds" :: "s"(lds), "v"(voff), "s"(rs), "s"(soff) : "memory"); }
template <int N> DI void wait_bar() { asm volatile("s_waitcnt vmcnt(%0) lgkmcnt(0)\n\ts_barrier" :: "i"(N) : "memory"); }
template <int N> DI void wait_vm() { asm volatile("s_waitcnt vmcnt(%0)" :: "i"(N) : "memory"); }
DI void lds_bar() { asm volatile("s_waitcnt lgkmcnt(0)\n\ts_barrier" ::: "memory"); }
typedef LAS char* lp;
DI lp lbase(LAS char* L, unsigned off) { lp p = L + off; PIN(p); return p; }
DI bf16x8 ld128(lp p) { return __builtin_bit_cast(bf16x8, *(LAS const u32x4*)p); }
DI bf16x8 ldtr2(lp p0, lp p1) {
    s16x4 a = __builtin_amdgcn_ds_read_tr16_b64_v4i16((LAS s16x4*)p0), b = __builtin_amdgcn_ds_read_tr16_b64_v4i16((LAS s16x4*)p1);
    return __builtin_bit_cast(bf16x8, __builtin_shufflevector(a, b, 0, 1, 2, 3, 4, 5, 6, 7)); }
DI bf16x8 asbf(u32x4 v) { return __builtin_bit_cast(bf16x8, v); }
DI unsigned nosent(unsigned v) { return v < 0xFFFFFFFEu ? v : 0xFFFFFFFEu; }
DI unsigned atomic_read(rsrc_t rs, unsigned voff) { return (unsigned)__builtin_amdgcn_raw_ptr_buffer_atomic_add_i32(0, rs, (int)voff, 0, SC1); }
DI void atomic_inc(rsrc_t rs, unsigned voff) { asm volatile("buffer_atomic_add %0, %1, %2, 0 offen sc1" :: "v"(1u), "v"(voff), "s"(rs) : "memory"); }
struct cf { float r, i; };
DI cf cmul(cf a, cf b) { cf o;
    asm("v_mul_f32 %0, %3, %5\n\tv_mul_f32 %1, %3, %4\n\tv_fma_f32 %0, %2, %4, -%0\n\tv_fma_f32 %1, %2, %5, %1" : "=&v"(o.r), "=&v"(o.i) : "v"(a.r), "v"(a.i), "v"(b.r), "v"(b.i)); return o; }
DI cf cfma(cf a, cf b, cf c) { cf o;
    asm("v_fma_f32 %0, -%3, %5, %6\n\tv_fma_f32 %1, %3, %4, %7\n\tv_fma_f32 %0, %2, %4, %0\n\tv_fma_f32 %1, %2, %5, %1"
        : "=&v"(o.r), "=&v"(o.i) : "v"(a.r), "v"(a.i), "v"(b.r), "v"(b.i), "v"(c.r), "v"(c.i)); return o; }
DI void pk_cfma(f32x2& nr, f32x2& ni, f32x2 l, f32x2 hr, f32x2 hm, f32x2 a, f32x2 c) {
    asm("v_pk_fma_f32 %0, %2, %3, %5 op_sel_hi:[0,1,1]\n\tv_pk_fma_f32 %1, %2, %4, %6 op_sel_hi:[0,1,1]\n\t"
        "v_pk_fma_f32 %0, %2, %4, %0 op_sel:[1,0,0] neg_lo:[1,0,0] neg_hi:[1,0,0]\n\tv_pk_fma_f32 %1, %2, %3, %1 op_sel:[1,0,0]"
        : "=&v"(nr), "=&v"(ni) : "v"(l), "v"(hr), "v"(hm), "v"(a), "v"(c)); }
DI void pk_cmul(f32x2& nr, f32x2& ni, f32x2 l, f32x2 hr, f32x2 hm) {
    asm("v_pk_mul_f32 %0, %2, %3 op_sel_hi:[0,1]\n\tv_pk_mul_f32 %1, %2, %4 op_sel_hi:[0,1]\n\t"
        "v_pk_fma_f32 %0, %2, %4, %0 op_sel:[1,0,0] neg_lo:[1,0,0] neg_hi:[1,0,0]\n\tv_pk_fma_f32 %1, %2, %3, %1 op_sel:[1,0,0]"
        : "=&v"(nr), "=&v"(ni) : "v"(l), "v"(hr), "v"(hm)); }
DI f32x2 pk_scale(f32x2 s, f32x2 a) { f32x2 r; asm("v_pk_mul_f32 %0, %1, %2 op_sel_hi:[0,1]" : "=v"(r) : "v"(s), "v"(a)); return r; }
DI float sum4(float v) {
    auto a = __builtin_amdgcn_permlane16_swap(__float_as_uint(v), __float_as_uint(v), false, false); v = __uint_as_float(a[0]) + __uint_as_float(a[1]);
    auto c = __builtin_amdgcn_permlane32_swap(__float_as_uint(v), __float_as_uint(v), false, false); return __uint_as_float(c[0]) + __uint_as_float(c[1]); }
DI void bcast4(float v, float& r0, float& r1, float& r2, float& r3) {
    auto a = __builtin_amdgcn_permlane16_swap(__float_as_uint(v), __float_as_uint(v), false, false);
    auto e = __builtin_amdgcn_permlane32_swap(a[0], a[0], false, false); auto o = __builtin_amdgcn_permlane32_swap(a[1], a[1], false, false);
    r0 = __uint_as_float(e[0]); r2 = __uint_as_float(e[1]); r1 = __uint_as_float(o[0]); r3 = __uint_as_float(o[1]); }
DI float rowmax16(const f32x16& s) {
    float a = fmaxf(fmaxf(s[0], s[1]), s[2]), b = fmaxf(fmaxf(s[3], s[4]), s[5]);
    a = fmaxf(fmaxf(a, s[6]), s[7]); b = fmaxf(fmaxf(b, s[8]), s[9]); a = fmaxf(fmaxf(a, s[10]), s[11]); b = fmaxf(fmaxf(b, s[12]), s[13]);
    a = fmaxf(fmaxf(a, s[14]), s[15]); a = fmaxf(a, b);
    auto rr = __builtin_amdgcn_permlane32_swap(__float_as_uint(a), __float_as_uint(a), false, false);
    return fmaxf(__uint_as_float(rr[0]), __uint_as_float(rr[1])); }
DI void set_shift(u32x4& q1, float c, int hi) {
    const float c1 = bfround(c), c2 = bfround(c - c1), c3 = c - c1 - c2;
    if (hi) { q1[2] = cvt2(-c1, -c2); q1[3] = cvt2(-c3, 0.f); } }
DI void lcount(LAS char* L, unsigned off, int lane) {
    asm volatile("s_waitcnt lgkmcnt(0)" ::: "memory");
    if (lane == 0) __hip_atomic_fetch_add((LAS unsigned*)(L + off), 1u, __ATOMIC_RELAXED, __HIP_MEMORY_SCOPE_WORKGROUP); }
DI void lwait(LAS char* L, unsigned off, unsigned n) {
    _Pragma("unroll 1") for (unsigned spin = 0; __builtin_amdgcn_readfirstlane(*(volatile LAS unsigned*)(L + off)) != n && spin < (1u << 20); ++spin) __builtin_amdgcn_s_sleep(1);
    asm volatile("" ::: "memory"); }
DI int xswz(int row) { return ((row >> 1) & 7) ^ ((row & 16) ? 7 : 0) ^ ((row & 32) ? 6 : 0); }
DI u32x4 pack8(const f32x16& a, int e) { return (u32x4){cvt2(a[e], a[e + 1]), cvt2(a[e + 2], a[e + 3]), cvt2(a[e + 4], a[e + 5]), cvt2(a[e + 6], a[e + 7])}; }
#define PAIR(v, p) (f32x2){v[(p) >> 1][2 * ((p) & 1)], v[(p) >> 1][2 * ((p) & 1) + 1]}

#define STAMP(k)
#define STAMP2(k)

__global__ void __launch_bounds__(512, 2)
gated_mix_kernel(const uint16_t* x, const uint16_t* wq, const uint16_t* wk, const uint16_t* wv, const uint16_t* wo, const uint16_t* lre, const uint16_t* lim,
                 const uint16_t* bre, const uint16_t* bim, const uint16_t* cre, const uint16_t* cim, const uint16_t* logdt, const uint16_t* dskip,
                 const uint16_t* gate, float* out, char* ws) {
    __shared__ __attribute__((aligned(16))) char smem[LDS_BYTES];
    LAS char* L = (LAS char*)smem;
    const unsigned l0 = (unsigned)(uintptr_t)L;
    const int lane = threadIdx.x & 63, w = __builtin_amdgcn_readfirstlane(threadIdx.x >> 6);
    const int b = blockIdx.x;
    STAMP(0);
    const int blk = ((b & 7) >> 1) * 8 + (b >> 5), j = (b & 1) * 4 + ((b >> 3) & 3);
    const int th = w & 1, hd = (w >> 1) & 1, kh = w >> 2, kq = w >> 1;
    const int r32 = lane & 31, hi = lane >> 5, r16 = lane & 15, G = lane >> 4;
    const int tq = r16 >> 2, tp = lane & 3;
    const int sl = lane & 7, r8 = lane >> 3;
    const unsigned l16 = lane * 16;
    const int kind = j & 1, tg = j >> 1;
    unsigned vx = r8 * ROWB + ((sl ^ (r8 >> 1)) << 4); PIN(vx);
    PINS(x); PINS(wq); PINS(wk); PINS(wv); PINS(wo); PINS(lre); PINS(lim); PINS(bre); PINS(bim); PINS(cre); PINS(cim); PINS(logdt); PINS(dskip); PINS(gate); PINS(out); PINS(ws);
    const rsrc_t rsX = mkrs(x, SEQ * ROWB), rsWS = mkrs(ws, WS_END);
    const unsigned opiece = WS_O + blk * 65536 + ((th * 16 + 2 * j + hd) * 2 + kh) * 1024;

    u32x4 ka0, ka1, va0, va1, kb0, kb1, vb0, vb1, q0[4], q1[4], pw0, pw1, hf[14];
    f32x16 sA, sB, o[4];
    float gt;
    const unsigned lv = (lane ^ (kh << 4)) * 16 + 2048;
    const unsigned kvs = WS_KV + (w < 4 ? w * NQ : 4 * NQ + (w - 4) * NU) * 4096;
    const unsigned hoff = lane == 0 ? WS_HCNT + blk * 128 : OOB;
    unsigned hc = 0u;
#define LD2(A0, A1, vo, t) asm volatile("buffer_load_dwordx4 %0, %2, %3, %4 offen sc1\n\tbuffer_load_dwordx4 %1, %2, %3, %4 offen offset:1024 sc1" \
                                        : "=&v"(A0), "=&v"(A1) : "v"(vo), "s"(rsWS), "s"(kvs + (t) * 4096) : "memory")
#define WAIT2(N, A, B) asm volatile("s_waitcnt vmcnt(%2)" : "+v"(A), "+v"(B) : "i"(N) : "memory")
#define HLD(m) hf[m] = __builtin_amdgcn_raw_buffer_load_b128(rsWS, l16, WS_H + blk * 114688 + th * 57344 + (4 * (m) + kq) * 1024, SC1)
#define ATT_OPEN() do { \
    UNROLL for (int q = 0; q < 4; ++q) { const lp a = lbase(L, L_QSH + ((w ^ (q + 1)) & 3) * 2048 + l16); q0[q] = *(LAS const u32x4*)(a); q1[q] = *(LAS const u32x4*)(a + 1024); } \
    gt = *(LAS const float*)(L + L_G + (32 * th + r32) * 4); \
    pw1 = (u32x4){0u, 0u, 0u, 0u}; vb1 = pw1; o[0] = o[1] = o[2] = o[3] = (f32x16){}; \
    PIN(o[0]); PIN(o[1]); PIN(o[2]); PIN(o[3]); PIN(pw1); PIN(vb1); } while (0)
#define EX4(A, i) do { A[i] = __builtin_amdgcn_exp2f(A[i]); A[i + 1] = __builtin_amdgcn_exp2f(A[i + 1]); \
                       A[i + 2] = __builtin_amdgcn_exp2f(A[i + 2]); A[i + 3] = __builtin_amdgcn_exp2f(A[i + 3]); PIN(A); } while (0)
#define UNIT(A, B, KN0, KN1, qn, q, qp, V0, VP1, NEXT, H0, H1, H3) do { SBAR(); H0; \
    if (NEXT) { B = MFMA32(asbf(KN0), asbf(q0[qn]), (f32x16){}); PIN(B); } SBAR(); \
    EX4(A, 0); SBAR(); \
    o[qp] = MFMA32(asbf(VP1), asbf(pw1), o[qp]); PIN(o[qp]); SBAR(); H1; \
    pw0[0] = cvt2(A[0], A[1]); pw0[1] = cvt2(A[2], A[3]); PIN(pw0); SBAR(); EX4(A, 4); SBAR(); \
    if (NEXT) { B = MFMA32(asbf(KN1), asbf(q1[qn]), B); PIN(B); } SBAR(); \
    pw0[2] = cvt2(A[4], A[5]); pw0[3] = cvt2(A[6], A[7]); PIN(pw0); SBAR(); EX4(A, 8); SBAR(); H3; \
    o[q] = MFMA32(asbf(V0), asbf(pw0), o[q]); PIN(o[q]); SBAR(); \
    EX4(A, 12); SBAR(); \
    pw1[0] = cvt2(A[8], A[9]); pw1[1] = cvt2(A[10], A[11]); pw1[2] = cvt2(A[12], A[13]); pw1[3] = cvt2(A[14], A[15]); PIN(pw1); SBAR(); } while (0)
#define TRIP(t, LAST, K0, K1, V0, V1, NK0, NK1, NV0, NV1, NV, NK, EXTRA, EXTRA3) do { \
    UNIT(sA, sB, K0, K1, 1, 0, 3, V0, NV1, true, {}, { if (!(LAST)) LD2(NK0, NK1, l16, (t) + 1); }, WAIT2(NV, V0, V1)); \
    UNIT(sB, sA, K0, K1, 2, 1, 0, V0, V1, true, {}, { if (!(LAST)) LD2(NV0, NV1, lv, (t) + 1); }, {}); \
    UNIT(sA, sB, K0, K1, 3, 2, 1, V0, V1, true, {}, EXTRA, {}); \
    UNIT(sB, sA, NK0, NK1, 0, 3, 2, V0, V1, !(LAST), { if (!(LAST)) WAIT2(NK, NK0, NK1); }, EXTRA3, {}); STAMP2((t) + 1); } while (0)
#define TRIPA(t, LAST, NV, NK, EXTRA, EXTRA3) TRIP(t, LAST, ka0, ka1, va0, va1, kb0, kb1, vb0, vb1, NV, NK, EXTRA, EXTRA3)
#define TRIPB(t, LAST, NV, NK, EXTRA, EXTRA3) TRIP(t, LAST, kb0, kb1, vb0, vb1, ka0, ka1, va0, va1, NV, NK, EXTRA, EXTRA3)
    if (w < 4) {
        const bool master = b == 0 && w == 0;
        const unsigned xoff = lane == 0 ? WS_X + (b & 15) * 128 : OOB, bump = (master && lane < 16) ? WS_X + lane * 128 : OOB;
        const rsrc_t rsWK = mkrs(kind ? wv : wk, DM * 56);
        const unsigned vxa = vx ^ (xswz(16 * tg) << 4), vxb = (vx ^ (xswz(16 * tg + 8) << 4)) + 8 * ROWB, xs = (blk * 64 + tg * 16) * ROWB + w * 256;
        const unsigned xd = l0 + L_X + w * 16384 + tg * 2048, wd = l0 + L_WK + w * 7168;
        STAMP(36);
        dma(rsX, vxa, xs, xd); dma(rsX, vxb, xs, xd + 1024);
        UNROLL for (int n = 0; n < 4; ++n) dma(rsWK, l16, w * 7168 + n * 1024, wd + n * 1024);
        if (w < 3) {
            dma(rsX, vxa, xs + 128, xd + 8192); dma(rsX, vxb, xs + 128, xd + 8192 + 1024);
            UNROLL for (int n = 4; n < 7; ++n) dma(rsWK, l16, w * 7168 + n * 1024, wd + n * 1024);
        } else if (lane < 3) *(LAS u32x2*)(L + L_KF + lane * 512) = (u32x2){0u, 0u};
        unsigned x0 = 0u, x1 = 0u, g0, g1;
        const int gk = min(w * 128, 320) + lane;
        if (w == 0) {
            asm volatile("buffer_atomic_add %0, %1, %2, 0 offen sc0 sc1" : "+v"(x0) : "v"(xoff), "s"(rsWS) : "memory");
            __builtin_amdgcn_raw_buffer_store_b64((u32x2){~0u, ~0u}, rsWS, lane < 56 ? lane * 8 : OOB, WS_E + (blk * DM + 56 * j) * 8, SC1);
        }
        asm volatile("global_load_ushort %0, %2, off\n\tglobal_load_ushort %1, %2, off offset:128" : "=&v"(g0), "=&v"(g1) : "v"(gate + gk) : "memory");
        __builtin_amdgcn_raw_buffer_store_b128((u32x4){~0u, ~0u, ~0u, ~0u}, rsWS, l16, opiece, SC1);
        lds_bar();
        STAMP(14);
        if (master) {
                __builtin_amdgcn_raw_buffer_store_b32(0u, rsWS, lane * 128, 0, SC1);
                __builtin_amdgcn_raw_buffer_store_b32(0u, rsWS, lane * 128 + 8192, 0, SC1);
                __builtin_amdgcn_raw_buffer_store_b32(0u, rsWS, lane < 32 ? lane * 128 + 16384 : OOB, 0, SC1);
                wait_vm<0>();
                atomic_inc(rsWS, bump);
            }
        const int cg0 = kind ? tp : 4 * (tp >> 1) + (tp & 1), cg1 = kind ? 4 + tp : 4 * (tp >> 1) + 2 + (tp & 1);
        const lp aw0 = lbase(L, L_WK + w * 7168 + (8 * G + tq) * 56 + cg0 * 8), aw1 = lbase(L, L_WK + w * 7168 + (8 * G + tq) * 56 + cg1 * 8);
        const unsigned axo = L_X + w * 16384 + (16 * tg + r16) * 128 + ((G ^ xswz(16 * tg + r16)) << 4);
        const lp ax0 = lbase(L, axo), ax1 = lbase(L, axo ^ 64), kp = lbase(L, L_KP + l16);
        const int sg = tg & 1;
        const unsigned tb = WS_KV + (2 * blk + (tg >> 1)) * 4096 + (kind ? 2048 + sg * 1024 : 0);
        unsigned kva = kind ? ((G >> 1) * 32 + r16) * 16 + (G & 1) * 8 : (G * 32 + 16 * sg + ((r16 & 3) | ((r16 & 4) << 1) | ((r16 & 8) >> 1))) * 16; PIN(kva);
        const bool spare = kind ? r16 >= 12 : G == 3;
        unsigned sp2 = (kind && r16 != 12) ? 0u : 0x3F803F80u, sp3 = kind ? sp2 : 0x00003F80u; PIN(sp2); PIN(sp3);
        asm volatile("s_waitcnt vmcnt(1)" : "+v"(g0), "+v"(g1), "+v"(x0) :: "memory");
        STAMP(1);
        bf16x8 xf[4], w0[4], w1[4];
        UNROLL for (int ks = 0; ks < 4; ++ks) { xf[ks] = ld128(((ks & 1) ? ax1 : ax0) + (ks >> 1) * 8192);
            w0[ks] = ldtr2(aw0 + ks * 1792, aw0 + ks * 1792 + 224); w1[ks] = ldtr2(aw1 + ks * 1792, aw1 + ks * 1792 + 224); SBAR(); }
        f32x4 d0 = {0.f, 0.f, 0.f, 0.f}, d1 = d0, e0 = d0, e1 = d0;
        if (kind == 0) { UNROLL for (int ks = 0; ks < 2; ++ks) { d0 = MFMA16(w0[ks], xf[ks], d0); d1 = MFMA16(w1[ks], xf[ks], d1); }
                         UNROLL for (int ks = 2; ks < 4; ++ks) { e0 = MFMA16(w0[ks], xf[ks], e0); e1 = MFMA16(w1[ks], xf[ks], e1); } }
        else           { UNROLL for (int ks = 0; ks < 2; ++ks) { d0 = MFMA16(xf[ks], w0[ks], d0); d1 = MFMA16(xf[ks], w1[ks], d1); }
                         UNROLL for (int ks = 2; ks < 4; ++ks) { e0 = MFMA16(xf[ks], w0[ks], e0); e1 = MFMA16(xf[ks], w1[ks], e1); } }
        UNROLL for (int e = 0; e < 4; ++e) { d0[e] += w == 3 ? 0.f : e0[e]; d1[e] += w == 3 ? 0.f : e1[e]; }
        PIN(d0); PIN(d1); STAMP(32);
        if (w != 0) {
            *(LAS f32x4*)(kp + (w - 1) * 2048) = d0; *(LAS f32x4*)(kp + (w - 1) * 2048 + 1024) = d1;
            asm volatile("" ::: "memory");
            if (lane == 0) __hip_atomic_fetch_add((LAS unsigned*)(L + L_KF), 1u, __ATOMIC_RELAXED, __HIP_MEMORY_SCOPE_WORKGROUP);
        } else {
            _Pragma("unroll 1") for (unsigned spin = 0; __builtin_amdgcn_readfirstlane(*(volatile LAS unsigned*)(L + L_KF)) != 3u && spin < (1u << 20); ++spin) { }
            asm volatile("" ::: "memory");
            STAMP(33);
            d0 += (*(LAS const f32x4*)(kp) + *(LAS const f32x4*)(kp + 2048)) + *(LAS const f32x4*)(kp + 4096);
            d1 += (*(LAS const f32x4*)(kp + 1024) + *(LAS const f32x4*)(kp + 3072)) + *(LAS const f32x4*)(kp + 5120);
            const u32x4 kv = {cvt2(d0[0], d0[1]), cvt2(d0[2], d0[3]), spare ? sp2 : cvt2(d1[0], d1[1]), spare ? sp3 : cvt2(d1[2], d1[3])};
            { u32x4 kk = kv; PIN(kk); STAMP(34); }
            if (kind == 0) __builtin_amdgcn_raw_buffer_store_b128(kv, rsWS, kva, tb, SC1);
            else { __builtin_amdgcn_raw_buffer_store_b64((u32x2){kv[0], kv[1]}, rsWS, kva, tb, SC1); __builtin_amdgcn_raw_buffer_store_b64((u32x2){kv[2], kv[3]}, rsWS, kva + 256, tb, SC1); }
            asm volatile("buffer_atomic_add %0, %1, %2, 0 offen sc0 sc1" : "+v"(x1) : "v"(xoff), "s"(rsWS) : "memory");
            STAMP(12);
        }
        { const unsigned ga = L_WQ + gk * 128 + (112 ^ (((gk >> 1) & 1) << 6)); *(LAS uint16_t*)(L + ga) = (uint16_t)g0; *(LAS uint16_t*)(L + ga + 8192) = (uint16_t)g1; }
        lcount(L, L_WQF, lane);
        const int qrow = 32 * th + r32;
        const unsigned a0o = L_X + qrow * 128 + ((hi ^ xswz(qrow)) << 4);
        const lp a00 = lbase(L, a0o), a01 = lbase(L, a0o ^ 32), a02 = lbase(L, a0o ^ 64), a03 = lbase(L, a0o ^ 96);
        const lp a1 = lbase(L, L_WQ + (8 * hi + tq) * 128 + ((56 * hd + 32 * (G & 1) + 8 * tp) ^ (((tq >> 1) & 1) << 6)));
        asm volatile("s_waitcnt vmcnt(0)" : "+v"(x1) :: "memory");
        STAMP(35);
        if (w == 0) {
            const unsigned s0 = __builtin_amdgcn_readfirstlane(x0); unsigned s1 = __builtin_amdgcn_readfirstlane(x1);
            _Pragma("unroll 1") for (unsigned spin = 0; !master && s1 == s0 && spin < SPIN_CAP; ++spin) s1 = __builtin_amdgcn_readfirstlane(atomic_read(rsWS, xoff));
            atomic_inc(rsWS, lane < 8 ? WS_KVCNT + lane * 2048 + (b & 15) * 128 : OOB);
            atomic_inc(rsWS, bump);
            STAMP(13);
        }
        lwait(L, L_WQF, 8u);
        STAMP(2);
        f32x16 qa = {};
        { bf16x8 wf[28], xq[28];
          UNROLL for (int ks = 0; ks < 32; ++ks) {
            if (ks < 28) { wf[ks] = ldtr2(a1 + ks * 2048, a1 + ks * 2048 + 512); xq[ks] = ld128(((ks & 3) == 0 ? a00 : (ks & 3) == 1 ? a01 : (ks & 3) == 2 ? a02 : a03) + (ks >> 2) * 8192); }
            if (ks >= 4) qa = MFMA32(wf[ks - 4], xq[ks - 4], qa);
            SBAR(); } }
        const f32x2 cs = {0.18898223650461361f * 1.4426950408889634f, 0.f};
        if (hd == 1 && hi) *(LAS float*)(L + L_G + qrow * 4) = __builtin_amdgcn_rcpf(1.0f + fexp(-qa[12]));
        u32x4 t0, t1;
        UNROLL for (int e = 0; e < 4; ++e) { const f32x2 a = pk_scale(cs, (f32x2){qa[2 * e], qa[2 * e + 1]}), c = pk_scale(cs, (f32x2){qa[8 + 2 * e], qa[9 + 2 * e]});
            t0[e] = cvt2(a[0], a[1]); t1[e] = cvt2(c[0], c[1]); }
        if (hi) { t1[2] = 0u; t1[3] = 0u; }
        *(LAS u32x4*)(L + L_QSH + w * 2048 + l16) = t0;
        *(LAS u32x4*)(L + L_QSH + w * 2048 + 1024 + l16) = t1;
        STAMP(3);
        if (w == 0) {
            _Pragma("unroll 1") for (unsigned spin = 0; spin < SPIN_CAP; ++spin) {
                atomic_inc(rsWS, bump);
                const unsigned c = atomic_read(rsWS, lane < 16 ? WS_KVCNT + (b & 7) * 2048 + lane * 128 : OOB);
                if (__builtin_amdgcn_ballot_w64(c != 16 && lane < 16) == 0) break;
            }
        }
        STAMP(4);
        lcount(L, L_QF, lane);
        lwait(L, L_QF, 4u);
        STAMP(5);
        LD2(ka0, ka1, l16, 0); LD2(va0, va1, lv, 0);
        ATT_OPEN();
        lcount(L, L_C2, lane);
        WAIT2(2, ka0, ka1);
        STAMP(6);
        sA = MFMA32(asbf(ka0), asbf(q0[0]), (f32x16){}); sA = MFMA32(asbf(ka1), asbf(q1[0]), sA);
        TRIPA(0, false, 2, 2, {}, {});
        TRIPB(1, false, 2, 2, {}, {});
        TRIPA(2, false, 2, 2, {}, {});
        TRIPB(3, false, 2, 2, {}, {});
        TRIPA(4, false, 2, 2, {}, {});
        TRIPB(5, false, 2, 2, {}, {});
        TRIPA(6, false, 2, 3, { asm volatile("buffer_atomic_add %0, %1, %2, 0 offen sc0 sc1" : "+v"(hc) : "v"(hoff), "s"(rsWS) : "memory"); }, {});
        TRIPB(7, false, 3, 2, {}, {});
        PIN(hc); _Pragma("unroll 1") for (unsigned spin = 0, c = __builtin_amdgcn_readfirstlane(hc); c != 32 && spin < SPIN_CAP; ++spin) c = __builtin_amdgcn_readfirstlane(atomic_read(rsWS, hoff));
        lwait(L, L_C2, 8u);
        TRIPA(8, false, 2, 9, { UNROLL for (int m = 0; m < 7; ++m) HLD(m); }, {});
        TRIPB(9, true, 7, 9, { UNROLL for (int m = 7; m < 14; ++m) HLD(m); }, {});
        o[3] = MFMA32(asbf(vb1), asbf(pw1), o[3]);
    } else {
        const int u = w - 4, st = min(16 * u + r16, 55);
        const unsigned eoff = (G == 0 && 16 * u + r16 < 56) ? st * 8 : OOB;
        const rsrc_t rsWQ = mkrs(wq, DM * ROWB);
        const int pw = sl ^ (((r8 >> 1) & 1) << 2); const unsigned vw = r8 * ROWB + 112 * j + (pw << 4);
        const int ga = 2 * u + (u >= tg ? 2 : 0);
        const unsigned vxa = vx ^ (xswz(8 * ga) << 4), vxb = (vx ^ (xswz(8 * ga + 8) << 4)) + 8 * ROWB, xs = (blk * 64 + 8 * ga) * ROWB;
        unsigned p0, p1, p2;
        const uint16_t *a0 = lre + 56 * j + st, *a1 = lim + 56 * j + st, *a2 = logdt + 56 * j + st; PIN(a0); PIN(a1); PIN(a2);
        asm volatile("s_barrier" ::: "memory");
        asm volatile("global_load_ushort %0, %3, off\n\tglobal_load_ushort %1, %4, off\n\tglobal_load_ushort %2, %5, off"
                     : "=&v"(p0), "=&v"(p1), "=&v"(p2) : "v"(a0), "v"(a1), "v"(a2) : "memory");
        if (u < 3) { UNROLL for (int c = 0; c < 7; ++c) { dma(rsX, vxa, xs + c * 128, l0 + L_X + c * 8192 + ga * 1024); dma(rsX, vxb, xs + c * 128, l0 + L_X + c * 8192 + ga * 1024 + 1024); } }
        else if (pw != 7) { UNROLL for (int n = 0; n < 16; ++n) dma(rsWQ, vw, (40 + n) * 8 * ROWB, l0 + L_WQ + (40 + n) * 1024); }
        if (pw != 7) { UNROLL for (int n = 0; n < 10; ++n) dma(rsWQ, vw, (u * 10 + n) * 8 * ROWB, l0 + L_WQ + (u * 10 + n) * 1024); }
        __builtin_amdgcn_raw_buffer_store_b128((u32x4){~0u, ~0u, ~0u, ~0u}, rsWS, l16, opiece, SC1);
        STAMP(14);
        const rsrc_t rsBR = mkrs(bre, DM * ROWB), rsBI = mkrs(bim, DM * ROWB);
        const unsigned vb0_ = (56 * j + 16 * u + r8) * ROWB + sl * 16, vb1_ = u < 3 ? vb0_ + 8 * ROWB : OOB;
        const unsigned bwo = L_BS + u * 4096 + r8 * 128 + ((sl ^ (r8 >> 1)) << 4), bro = L_BS + u * 4096 + r16 * 128 + ((G ^ (r16 >> 1)) << 4);
        const lp bw0 = lbase(L, bwo), bw1 = lbase(L, (bwo ^ 64) + 1024), br0 = lbase(L, bro), br1 = lbase(L, bro ^ 64);
        lp at[4][2];
        UNROLL for (int m = 0; m < 4; ++m) { const int row = 16 * (r16 >> 2) + 2 * m + ((r16 & 3) >> 1) + 8 * (r16 & 1); const unsigned a = L_X + row * 128 + ((G ^ xswz(row)) << 4);
            at[m][0] = lbase(L, a); at[m][1] = lbase(L, a ^ 64); }
        asm volatile("s_waitcnt vmcnt(25)" : "+v"(p0), "+v"(p1), "+v"(p2) :: "memory");
        f32x2 lam, coef; cf lam8, lam16, lam64, wlo, whi, zG;
        {   const float lr = bf2f(p0), li = bf2f(p1), dt = bfround(fexp(bf2f(p2)));
            const float er = fexp(dt * lr), rev = __builtin_amdgcn_fractf(dt * li * 0.15915494309189535f);
            const cf l1 = {er * __builtin_amdgcn_cosf(rev), er * __builtin_amdgcn_sinf(rev)};
            const float inv = __builtin_amdgcn_rcpf(lr * lr + li * li);
            const cf cq = cmul((cf){l1.r - 1.0f, l1.i}, (cf){lr * inv, -li * inv});
            lam = (f32x2){l1.r, l1.i}; coef = (f32x2){cq.r, cq.i};
            const cf l2 = cmul(l1, l1), l4 = cmul(l2, l2); lam8 = cmul(l4, l4); lam16 = cmul(lam8, lam8);
            const cf l32 = cmul(lam16, lam16); lam64 = cmul(l32, l32);
            const cf l128 = cmul(lam64, lam64), l256 = cmul(l128, l128), l512 = cmul(l256, l256), l1024 = cmul(l512, l512), one = {1.f, 0.f};
            wlo = cmul((G & 1) ? lam16 : one, (G & 2) ? l32 : one); whi = cmul(wlo, lam8);
            zG = cmul((G & 1) ? l512 : one, (G & 2) ? l1024 : one);
            asm volatile("" : "+v"(lam), "+v"(coef), "+v"(lam8.r), "+v"(lam8.i), "+v"(lam16.r), "+v"(lam16.i), "+v"(lam64.r), "+v"(lam64.i),
                              "+v"(wlo.r), "+v"(wlo.i), "+v"(whi.r), "+v"(whi.i), "+v"(zG.r), "+v"(zG.i) :: "memory"); }
        STAMP(12);
        u32x4 bq[7][4];
#define BLD(c) do { bq[c][0] = __builtin_amdgcn_raw_buffer_load_b128(rsBR, vb0_, (c) * 128, 0); bq[c][1] = __builtin_amdgcn_raw_buffer_load_b128(rsBR, vb1_, (c) * 128, 0); \
                    bq[c][2] = __builtin_amdgcn_raw_buffer_load_b128(rsBI, vb0_, (c) * 128, 0); bq[c][3] = __builtin_amdgcn_raw_buffer_load_b128(rsBI, vb1_, (c) * 128, 0); \
                    asm volatile("" ::: "memory"); } while (0)
        BLD(0); BLD(1); BLD(2);
        wait_vm<12>();
        lcount(L, L_WQF, lane);
        STAMP(1);
        BLD(3); BLD(4); BLD(5); BLD(6);
        lwait(L, L_WQF, 8u);
        STAMP(2);
        f32x4 ur[4] = {}, ui[4] = {};
        bf16x8 fb[2][4], fx[2][8];
#define BWR(c) do { *(LAS u32x4*)(bw0) = bq[c][0]; *(LAS u32x4*)(bw1) = bq[c][1]; *(LAS u32x4*)(bw0 + 2048) = bq[c][2]; *(LAS u32x4*)(bw1 + 2048) = bq[c][3]; \
                    asm volatile("" ::: "memory"); } while (0)
#define BRD(c) do { fb[(c) & 1][0] = ld128(br0); fb[(c) & 1][1] = ld128(br0 + 2048); fb[(c) & 1][2] = ld128(br1); fb[(c) & 1][3] = ld128(br1 + 2048); asm volatile("" ::: "memory"); } while (0)
#define XRD(c, i) fx[(c) & 1][i] = ld128(at[(i) & 3][(i) >> 2] + (c) * 8192)
        BWR(0); BRD(0); UNROLL for (int i = 0; i < 8; ++i) XRD(0, i); BWR(1); SBAR();
        UNROLL for (int c = 0; c < 7; ++c) {
            if (c < 6) BRD(c + 1);
            UNROLL for (int i = 0; i < 8; ++i) {
                ur[i & 3] = MFMA16(fx[c & 1][i], fb[c & 1][2 * (i >> 2)], ur[i & 3]); ui[i & 3] = MFMA16(fx[c & 1][i], fb[c & 1][2 * (i >> 2) + 1], ui[i & 3]);
                if (c < 6) XRD(c + 1, i);
                if (i == 3 && c < 5) BWR(c + 2);
                if (i == 0 && c == 3) __builtin_amdgcn_s_setprio(1);
                SBAR();
            }
        }
        lcount(L, L_C2, lane);
        STAMP(3);
        f32x2 hr, hm;
        UNROLL for (int p = 0; p < 8; ++p) {
            f32x2 tr, ti; pk_cmul(tr, ti, coef, PAIR(ur, p), PAIR(ui, p));
            ur[p >> 1][2 * (p & 1)] = tr[0]; ur[p >> 1][2 * (p & 1) + 1] = tr[1]; ui[p >> 1][2 * (p & 1)] = ti[0]; ui[p >> 1][2 * (p & 1) + 1] = ti[1];
            if (p == 0) { hr = tr; hm = ti; } else { f32x2 nr, ni; pk_cfma(nr, ni, lam, hr, hm, tr, ti); hr = nr; hm = ni; }
        }
        const cf elo = {hr[0], hm[0]}, e16 = cfma(lam8, elo, (cf){hr[1], hm[1]});
        cf e0, e1, e2, e3; bcast4(e16.r, e0.r, e1.r, e2.r, e3.r); bcast4(e16.i, e0.i, e1.i, e2.i, e3.i);
        const cf t2 = cfma(lam16, e0, e1), t3 = cfma(lam16, t2, e2), eb = cfma(lam16, t3, e3);
        const cf pg = {G == 0 ? 0.f : G == 1 ? e0.r : G == 2 ? t2.r : t3.r, G == 0 ? 0.f : G == 1 ? e0.i : G == 2 ? t2.i : t3.i}, ph = cfma(lam8, pg, elo);
        STAMP(4);
        lwait(L, L_QF, 4u);
        STAMP(5);
        __builtin_amdgcn_raw_buffer_store_b64((u32x2){nosent(__float_as_uint(eb.r)), nosent(__float_as_uint(eb.i))}, rsWS, eoff, WS_E + (blk * DM + 56 * j) * 8, SC1);
        LD2(ka0, ka1, l16, 0); LD2(va0, va1, lv, 0);
        ATT_OPEN();
        const rsrc_t rsCR = mkrs(cre, DM * ROWB), rsCI = mkrs(cim, DM * ROWB);
        const int c2 = min(u + 4, 6);
        const unsigned vc = (56 * j + 8 * u + r8) * ROWB + ((sl ^ ((4 * u + (r8 >> 1)) & 7)) << 4), vc2 = (56 * j + 8 * c2 + r8) * ROWB + ((sl ^ ((4 * c2 + (r8 >> 1)) & 7)) << 4);
#define CD4(cc) do { dma(rsCR, vc, (cc) * 128, l0 + L_C + (cc) * 7168 + u * 1024); dma(rsCI, vc, (cc) * 128, l0 + L_CIM + (cc) * 7168 + u * 1024); \
                     dma(rsCR, vc2, (cc) * 128, l0 + L_C + (cc) * 7168 + c2 * 1024); dma(rsCI, vc2, (cc) * 128, l0 + L_CIM + (cc) * 7168 + c2 * 1024); } while (0)
        const rsrc_t rsE = mkrs(ws + WS_E + 56 * j * 8, 32 * DM * 8 - 56 * j * 8);
        const int ebase = (blk - 1 - 8 * G) * (DM * 8) + st * 8;
        u32x2 ev[8];
#define ELD() do { UNROLL for (int m = 0; m < 8; ++m) ev[m] = __builtin_amdgcn_raw_buffer_load_b64(rsE, ebase - m * (DM * 8), 0, SC1); } while (0)
        WAIT2(2, ka0, ka1);
        STAMP(6);
        sA = MFMA32(asbf(ka0), asbf(q0[0]), (f32x16){}); sA = MFMA32(asbf(ka1), asbf(q1[0]), sA);
        TRIPA(0, false, 2, 10, { lwait(L, L_C2, 8u); CD4(0); CD4(1); }, {});
        TRIPB(1, false, 10, 2, {}, ELD());
        WAIT2(0, va0, va1);
        {
            UNROLL for (int m = 0; m < 8; ++m) PIN(ev[m]);
            _Pragma("unroll 1") for (unsigned spin = 0; spin < SPIN_CAP; ++spin) {
                unsigned mx = 0;
                UNROLL for (int m = 0; m < 8; ++m) mx = max(mx, max(ev[m][0], ev[m][1]));
                if (__builtin_amdgcn_ballot_w64(mx == ~0u) == 0) break;
                __builtin_amdgcn_s_sleep(8);
                ELD();
            }
            cf part = {__uint_as_float(ev[7][0]), __uint_as_float(ev[7][1])};
            UNROLL for (int m = 6; m >= 0; --m) part = cfma(lam64, part, (cf){__uint_as_float(ev[m][0]), __uint_as_float(ev[m][1])});
            part = cmul(zG, part);
            if (ebase < 0) part = (cf){0.f, 0.f};
            part.r = sum4(part.r); part.i = sum4(part.i);
            const cf ilo = cfma(wlo, part, pg), ihi = cfma(whi, part, ph);
            hr = (f32x2){ilo.r, ihi.r}; hm = (f32x2){ilo.i, ihi.i};
            u32x4 pr0, pr1, pi0, pi1;
            UNROLL for (int p = 0; p < 8; ++p) {
                f32x2 nr, ni; pk_cfma(nr, ni, lam, hr, hm, PAIR(ur, p), PAIR(ui, p)); hr = nr; hm = ni;
                unsigned cr, ci; asm("v_cvt_pk_bf16_f32 %0, %2, %3\n\tv_cvt_pk_bf16_f32 %1, -%4, -%5" : "=&v"(cr), "=&v"(ci) : "v"(hr[0]), "v"(hr[1]), "v"(hm[0]), "v"(hm[1]));
                if (p < 4) { pr0[p & 3] = cr; pi0[p & 3] = ci; } else { pr1[p & 3] = cr; pi1[p & 3] = ci; }
            }
            const unsigned sb = L_BS + u * 4096, wa = sb + r16 * 128 + (((2 * G) ^ (r16 & 7)) << 4);
            const int tok = (lane & 48) + (r16 >> 1) + 8 * (lane & 1);
            const unsigned ga = (tok >> 5) * 57344 + (tok & 31) * 16, gb = u < 3 ? ga + 1024 : OOB, gs = WS_H + blk * 114688 + (7 * j + 2 * u) * 1024;
            const unsigned rao = sb + tq * 128 + (((2 * G + (tp >> 1)) ^ tq) << 4) + 8 * (tp & 1);
            const lp wa0 = lbase(L, wa), wa1 = lbase(L, wa ^ 16), ra = lbase(L, rao), rb = lbase(L, (rao ^ 64) + 512);
            *(LAS u32x4*)(wa0) = pr0; *(LAS u32x4*)(wa1) = pr1; *(LAS u32x4*)(wa0 + 2048) = pi0; *(LAS u32x4*)(wa1 + 2048) = pi1;
            asm volatile("" ::: "memory");
            UNROLL for (int pl = 0; pl < 2; ++pl) {
                const bf16x8 c0 = ldtr2(ra + pl * 2048, rb + pl * 2048), c1 = ldtr2(ra + pl * 2048 + 1024, rb + pl * 2048 + 1024);
                __builtin_amdgcn_raw_buffer_store_b128(__builtin_bit_cast(u32x4, c0), rsWS, ga + pl * 512, gs, SC1);
                __builtin_amdgcn_raw_buffer_store_b128(__builtin_bit_cast(u32x4, c1), rsWS, gb + pl * 512, gs, SC1);
            }
        }
        TRIPA(2, false, 63, 11, { wait_vm<4>(); atomic_inc(rsWS, hoff); __builtin_amdgcn_s_setprio(0);  CD4(2); CD4(3); }, {});
        TRIPB(3, false, 11, 10, { CD4(4); CD4(5); }, {});
        TRIPA(4, false, 10, 7, { CD4(6); asm volatile("buffer_atomic_add %0, %1, %2, 0 offen sc0 sc1" : "+v"(hc) : "v"(hoff), "s"(rsWS) : "memory"); }, {});
        TRIPB(5, true, 5, 16, { wait_vm<0>(); PIN(hc); _Pragma("unroll 1") for (unsigned spin = 0, c = __builtin_amdgcn_readfirstlane(hc); c != 32 && spin < SPIN_CAP; ++spin) c = __builtin_amdgcn_readfirstlane(atomic_read(rsWS, hoff)); UNROLL for (int m = 0; m < 14; ++m) HLD(m); }, {});
        o[3] = MFMA32(asbf(vb1), asbf(pw1), o[3]);
    }
    STAMP(7);
    const rsrc_t rsWO = mkrs(wo, DM * ROWB);
    {
    int lane_ = l16 >> 4; PIN(lane_);
    const int r32 = lane_ & 31, hi = lane_ >> 5, G = lane_ >> 4, tq = (lane_ & 15) >> 2, tp = lane_ & 3, sl = lane_ & 7, r8 = lane_ >> 3;
    const unsigned rob = L_RO + w * 1024 + l16, lsb = L_LS + ((w & 3) * 32 + r32) * 32;
    auto give = [&](const f32x16& oq, int q) __attribute__((always_inline)) {
        const int xa = (q + 1) & 3, xb = xa ^ 4;
        if (q < 3) *(LAS u32x4*)(lbase(L, rob ^ (xa * 1024)) + (xa - 1) * 8192) = pack8(oq, 0);
        *(LAS u32x4*)(lbase(L, rob ^ (xb * 1024)) + (xb - 1) * 8192) = pack8(oq, 8);
        float l4 = oq[4], l12 = oq[12]; PIN(l4); PIN(l12);
        const float ls = kh ? l4 : l12;
        if (hi) *(LAS float*)(L + (lsb ^ (xa * 1024)) + w * 4) = ls;
        return ls; };
    {   bool ok = true;
        UNROLL for (int q = 0; q < 4; ++q) { const float ls = give(o[q], q); ok &= ls >= 5.421011e-20f && ls <= 1.1529215e18f; }
        if (hi && !ok) *(LAS unsigned*)(L + L_FLAG) = 1u; }
    u32x4 sel;
    { const int js = 4 * (r32 >> 3) + (r32 & 3); const unsigned one = (r32 < 16 && ((r32 >> 2) & 1) == hi) ? 0x3F80u << (16 * (js & 1)) : 0u;
      sel = (u32x4){(js >> 1) == 0 ? one : 0u, (js >> 1) == 1 ? one : 0u, (js >> 1) == 2 ? one : 0u, (js >> 1) == 3 ? one : 0u}; PIN(sel); }
    const lp rop = lbase(L, rob), lsp = lbase(L, lsb);
    const int rbc = min(32 + r32, 55);
    const unsigned cao = hi * L_CIM + r32 * 128 + ((kq ^ ((r32 >> 1) & 7)) << 4), cbo = hi * L_CIM + rbc * 128 + ((kq ^ ((rbc >> 1) & 7)) << 4);
    const lp ca0 = lbase(L, cao), ca1 = lbase(L, cao ^ 64), cb0 = lbase(L, cbo), cb1 = lbase(L, cbo ^ 64);
    unsigned vwo = r8 * ROWB + 112 * j + ((sl ^ (((r8 >> 1) & 1) << 2)) << 4); PIN(vwo);
    UNROLL for (int m = 0; m < 14; ++m) PIN(hf[m]);
    lds_bar();
    STAMP(15);
    bf16x8 f[7]; f32x4 m0, m1;
    const unsigned flag = *(LAS const unsigned*)(L + L_FLAG);
    UNROLL for (int t = 0; t < 7; ++t) f[t] = ld128(rop + t * 8192);
    m0 = *(LAS const f32x4*)(lsp); m1 = *(LAS const f32x4*)(lsp + 16);
    SBAR();
    if (__builtin_expect(__builtin_amdgcn_readfirstlane(flag) != 0u, 0)) {
        float mx[4] = {-INFINITY, -INFINITY, -INFINITY, -INFINITY};
        lds_bar();
        _Pragma("unroll 1") for (int t = 0; t < (w < 4 ? NQ : NU); ++t) {
            const bf16x8 a = asbf(__builtin_amdgcn_raw_buffer_load_b128(rsWS, l16, kvs + t * 4096, SC1));
            const bf16x8 c = asbf(__builtin_amdgcn_raw_buffer_load_b128(rsWS, l16 + 1024, kvs + t * 4096, SC1));
            UNROLL for (int q = 0; q < 4; ++q) { f32x16 s = MFMA32(a, asbf(q0[q]), (f32x16){}); s = MFMA32(c, asbf(q1[q]), s); mx[q] = fmaxf(mx[q], rowmax16(s)); SBAR(); }
        }
        UNROLL for (int q = 0; q < 4; ++q) if (hi) *(LAS float*)(L + (lsb ^ (((q + 1) & 3) * 1024)) + w * 4) = mx[q];
        lds_bar();
        UNROLL for (int q = 0; q < 4; ++q) { const unsigned a = lsb ^ (((q + 1) & 3) * 1024);
            const f32x4 n0 = *(LAS const f32x4*)(L + a), n1 = *(LAS const f32x4*)(L + a + 16);
            set_shift(q1[q], fmaxf(fmaxf(fmaxf(n0[0], n0[1]), fmaxf(n0[2], n0[3])), fmaxf(fmaxf(n1[0], n1[1]), fmaxf(n1[2], n1[3]))), hi); }
        lds_bar();
        UNROLL for (int q = 0; q < 4; ++q) {
            f32x16 acc = {};
            _Pragma("unroll 1") for (int t = 0; t < (w < 4 ? NQ : NU); ++t) {
                const bf16x8 a = asbf(__builtin_amdgcn_raw_buffer_load_b128(rsWS, l16, kvs + t * 4096, SC1));
                const bf16x8 c = asbf(__builtin_amdgcn_raw_buffer_load_b128(rsWS, l16, kvs + t * 4096 + 1024, SC1));
                const bf16x8 d = asbf(__builtin_amdgcn_raw_buffer_load_b128(rsWS, lv, kvs + t * 4096, SC1));
                const bf16x8 e = asbf(__builtin_amdgcn_raw_buffer_load_b128(rsWS, lv, kvs + t * 4096 + 1024, SC1));
                f32x16 s = MFMA32(a, asbf(q0[q]), (f32x16){}); s = MFMA32(c, asbf(q1[q]), s);
                UNROLL for (int r = 0; r < 16; ++r) s[r] = __builtin_amdgcn_exp2f(s[r]);
                acc = MFMA32(d, asbf(pack8(s, 0)), acc); acc = MFMA32(e, asbf(pack8(s, 8)), acc);
            }
            give(acc, q);
            if (q == 3) o[3] = acc;
        }
        UNROLL for (int m = 0; m < 14; ++m) HLD(m);
        UNROLL for (int m = 0; m < 14; ++m) PIN(hf[m]);
        lds_bar();
        UNROLL for (int t = 0; t < 7; ++t) f[t] = ld128(rop + t * 8192);
        m0 = *(LAS const f32x4*)(lsp); m1 = *(LAS const f32x4*)(lsp + 16);
    }
    lds_bar();
    f32x16 t = o[3];
    UNROLL for (int k = 0; k < 7; ++k) t = MFMA32(asbf(sel), f[k], t);
    PIN(t); SBAR();
    bf16x8 fa[14], fc[14];
    UNROLL for (int m = 0; m < 4; ++m) { fa[m] = ld128(((m & 1) ? ca1 : ca0) + (m >> 1) * 7168); fc[m] = ld128(((m & 1) ? cb1 : cb0) + (m >> 1) * 7168); }
    {
        float s0 = m0[0] + m1[0], s1 = m0[1] + m1[1], s2 = m0[2] + m1[2], s3 = m0[3] + m1[3]; PIN(s0); PIN(s2); s0 += s1; s2 += s3; PIN(s0);
        const f32x2 sc = {gt * __builtin_amdgcn_rcpf(s0 + s2), 0.f};
        u32x4 ov;
        UNROLL for (int e = 0; e < 4; ++e) { const f32x2 a = pk_scale(sc, (f32x2){t[2 * e], t[2 * e + 1]}); ov[e] = nosent(cvt2(a[0], a[1])); }
        if (kh && hi) { ov[2] = 0u; ov[3] = 0u; }
        __builtin_amdgcn_raw_buffer_store_b128(ov, rsWS, l16, opiece, SC1);
    }
    STAMP(8);

    const int tok = blk * 64 + 32 * th + r32, colA = 56 * j + 16 * kq + 4 * hi;
    const rsrc_t rsD = mkrs(dskip, ROWB), rsO = mkrs(out, SEQ * DM * 4);
    const u32x2 xa = __builtin_amdgcn_raw_buffer_load_b64(rsX, tok * ROWB + colA * 2, 0, 0), xb = __builtin_amdgcn_raw_buffer_load_b64(rsX, tok * ROWB + colA * 2 + 16, 0, 0);
    const u32x2 da = __builtin_amdgcn_raw_buffer_load_b64(rsD, colA * 2, 0, 0), db = __builtin_amdgcn_raw_buffer_load_b64(rsD, colA * 2 + 16, 0, 0);
    u32x4 of[8];
    const unsigned ob = WS_O + blk * 65536 + (th * 16 + 4 * kq) * 2048;
#define OLD() do { UNROLL for (int k = 0; k < 8; ++k) of[k] = __builtin_amdgcn_raw_buffer_load_b128(rsWS, l16 + (k & 3) * 1024, ob + (k >> 2) * 4096, SC1); } while (0)
    if (w >= 4) { UNROLL for (int n = 0; n < 14; ++n) dma(rsWO, vwo, ((w - 4) * 14 + n) * 8 * ROWB, l0 + L_WO + ((w - 4) * 14 + n) * 1024); }
    f32x16 y0 = {}, y1 = {};
    SBAR();
    UNROLL for (int m = 0; m < 14; ++m) {
        if (m + 4 < 14) { fa[m + 4] = ld128((((m + 4) & 1) ? ca1 : ca0) + ((m + 4) >> 1) * 7168); fc[m + 4] = ld128((((m + 4) & 1) ? cb1 : cb0) + ((m + 4) >> 1) * 7168); }
        y0 = MFMA32(fa[m], asbf(hf[m]), y0); y1 = MFMA32(fc[m], asbf(hf[m]), y1);
        if (m == OLDM) OLD();
        SBAR();
    }
    const unsigned cx = (32 * (G & 1) + 8 * tp) ^ (((tq >> 1) & 1) << 6), nb = L_WO + (112 * kq + tq) * 128, wa = nb + hi * 512;
    const lp wa0 = lbase(L, wa + cx), wa1 = lbase(L, wa + (cx ^ 64)), nb0 = lbase(L, nb + cx), nb1 = lbase(L, nb + (cx ^ 64));
    STAMP(9);
    wait_bar<8>();
    bf16x8 wf0[8], wf1[8];
    UNROLL for (int k = 0; k < 8; ++k) { const unsigned ro = (28 * (k >> 1) + 16 * (k & 1)) * 128;
        wf0[k] = ldtr2(wa0 + ro, ((k & 1) ? nb0 : wa0) + ro + 1024); }
    UNROLL for (int k = 0; k < 8; ++k) { const unsigned ro = (28 * (k >> 1) + 16 * (k & 1)) * 128;
        wf1[k] = ldtr2(wa1 + ro, ((k & 1) ? nb1 : wa1) + ro + 1024); }
    const float g1 = 1.0f - gt;
    { const f32x2 s2 = {2.0f * g1, 0.f};
      UNROLL for (int e = 0; e < 8; ++e) { const f32x2 a = pk_scale(s2, (f32x2){y0[2 * e], y0[2 * e + 1]}), c = pk_scale(s2, (f32x2){y1[2 * e], y1[2 * e + 1]});
          y0[2 * e] = a[0]; y0[2 * e + 1] = a[1]; y1[2 * e] = c[0]; y1[2 * e + 1] = c[1]; } }
    float xd[8] = {bf2f(xa[0]) * bf2f(da[0]), bfhi(xa[0]) * bfhi(da[0]), bf2f(xa[1]) * bf2f(da[1]), bfhi(xa[1]) * bfhi(da[1]),
                   bf2f(xb[0]) * bf2f(db[0]), bfhi(xb[0]) * bfhi(db[0]), bf2f(xb[1]) * bf2f(db[1]), bfhi(xb[1]) * bfhi(db[1])};
    UNROLL for (int e = 0; e < 8; ++e) { PIN(xd[e]); xd[e] *= g1; PIN(xd[e]); }
    const unsigned rdb = L_RED + w * 2048 + l16;
    const lp rd0 = lbase(L, rdb);
    lp rdw[4];
    UNROLL for (int e = 0; e < 4; ++e) rdw[e] = lbase(L, (rdb ^ ((e ^ kq) * 4096)) + (e ^ kq) * 16384);
    unsigned oo = (tok * DM + colA) * 4; PIN(oo);
    SBAR();
    _Pragma("unroll 1") for (unsigned spin = 0; spin < SPIN_CAP; ++spin) {
        unsigned mx = 0;
        UNROLL for (int k = 0; k < 8; ++k) mx = max(max(mx, max(of[k][0], of[k][1])), max(of[k][2], of[k][3]));
        if (__builtin_amdgcn_ballot_w64(mx == ~0u) == 0) break;
        OLD();
    }
    STAMP(10);
    auto oct = [&](const f32x16& y, int e8, int e) __attribute__((always_inline)) {
        const f32x4 a = {y[e8], y[e8 + 1], y[e8 + 2], y[e8 + 3]}, c = {y[e8 + 4], y[e8 + 5], y[e8 + 6], y[e8 + 7]};
        *(LAS f32x4*)(rdw[e]) = a; *(LAS f32x4*)(rdw[e] + 1024) = c; };
    UNROLL for (int k = 0; k < 8; ++k) { y0 = MFMA32(wf0[k], asbf(of[k]), y0); } PIN(y0); SBAR();
    UNROLL for (int k = 0; k < 4; ++k) { y1 = MFMA32(wf1[k], asbf(of[k]), y1); } PIN(y1); SBAR();
    oct(y0, 0, 0); oct(y0, 8, 1); SBAR();
    UNROLL for (int k = 4; k < 8; ++k) { y1 = MFMA32(wf1[k], asbf(of[k]), y1); } PIN(y1); SBAR();
    oct(y1, 0, 2); oct(y1, 8, 3);
    lds_bar();
    f32x4 ra = {xd[0], xd[1], xd[2], xd[3]}, rb = {xd[4], xd[5], xd[6], xd[7]};
    UNROLL for (int d = 0; d < 4; ++d) { ra += *(LAS const f32x4*)(rd0 + d * 16384); rb += *(LAS const f32x4*)(rd0 + d * 16384 + 1024); }
    __builtin_amdgcn_raw_buffer_store_b128(__builtin_bit_cast(u32x4, ra), rsO, oo, 0, 0);
    __builtin_amdgcn_raw_buffer_store_b128(__builtin_bit_cast(u32x4, rb), rsO, kq < 3 ? oo + 32 : OOB, 0, 0);
    STAMP(11);
    }
}

extern "C" void kernel_launch(void* const* d_in, const int* in_sizes, int n_in, void* d_out, int out_size, void* d_ws, size_t ws_size,
                              hipStream_t stream) {
    const uint16_t* const* in = (const uint16_t* const*)d_in;
    gated_mix_kernel<<<256, 512, 0, stream>>>(in[0], in[1], in[2], in[3], in[4], in[6], in[7], in[8], in[9], in[10], in[11], in[12], in[13], in[14],
                                              (float*)d_out, (char*)d_ws);
}
```

```cpp
#include <hip/hip_runtime.h>
#include <hip/hip_bf16.h>
#include <stdint.h>

typedef __bf16 bf16x8 __attribute__((ext_vector_type(8)));
typedef __bf16 bf16x2 __attribute__((ext_vector_type(2)));
typedef float f32x16 __attribute__((ext_vector_type(16)));
typedef float f32x4 __attribute__((ext_vector_type(4)));
typedef float f32x2 __attribute__((ext_vector_type(2)));
typedef unsigned u32x4 __attribute__((ext_vector_type(4)));
typedef unsigned u32x2 __attribute__((ext_vector_type(2)));
typedef short s16x4 __attribute__((ext_vector_type(4)));
typedef __amdgpu_buffer_rsrc_t rsrc_t;
#define LAS __attribute__((address_space(3)))
#define DI __device__ __forceinline__
#define SBAR() __builtin_amdgcn_sched_barrier(0)
#define PIN(x) asm volatile("" : "+v"(x))
#define PINS(x) asm volatile("" : "+s"(x))
#define MFMA32(a, b, c) __builtin_amdgcn_mfma_f32_32x32x16_bf16((a), (b), (c), 0, 0, 0)
#define MFMA16(a, b, c) __builtin_amdgcn_mfma_f32_16x16x32_bf16((a), (b), (c), 0, 0, 0)
#define UNROLL _Pragma("unroll")
#define PACE() asm volatile("s_sleep %0" :: "i"(CSLEEP) : "memory")
#define SC1 16

#define CSLEEP 2
#define YSLEEP 8
#define CPREF 32768
#define WQ2 8
#define YD 3
#define OSLEEP 12
#define QREP 2
#define KPOLL 20
#define UPRIO 1
#define SPRIO 0
#define OM0 16
#define OM1 22
#define MAP42 1

constexpr int SEQ = 2048, DM = 448, ROWB = DM * 2;
constexpr unsigned WS_KVCNT = 0;
constexpr unsigned WS_HCNT = 16384;
constexpr unsigned WS_X = 20480;
constexpr unsigned OOB = 0x7FFFFFF0u;
constexpr unsigned WS_KV = 65536;
constexpr unsigned WS_E = WS_KV + 262144;
constexpr unsigned WS_H = WS_E + 131072;
constexpr unsigned WS_O = WS_H + 32 * 114688;
constexpr unsigned WS_END = WS_O + 32 * 65536;
constexpr unsigned WS_STAMP = WS_END;

constexpr unsigned L_X = 0, L_WK = 57344, L_WQ = 86016, L_B0 = 143360, L_G = 160768;
constexpr unsigned L_KD = 161024, L_XA = 161028, L_XF = 161060, L_QD = 161032, L_QF = 161036, L_UF = 161040, L_WF = 161044, L_KR = 161048, L_YF = 161052, L_FLAG = 161056, L_XG = 161064, L_GV = 161068, L_QE = 161072;
constexpr unsigned L_QSH = 122880, L_HS = 106496;
constexpr unsigned L_WO = 0, L_C = 0, L_CIM = 50176;
constexpr unsigned L_RO = 131072, L_LS = 155648;
constexpr unsigned L_RED = 0, L_DUMMY = 157696;
constexpr unsigned LDS_BYTES = 163840;
constexpr unsigned SPIN_CAP = 1u << 14;

DI unsigned cvt2(float a, float b) { f32x2 v = {a, b}; return __builtin_bit_cast(unsigned, __builtin_convertvector(v, bf16x2)); }
DI float bf2f(unsigned h) { return __uint_as_float(h << 16); }
DI float bfhi(unsigned h) { return __uint_as_float(h & 0xffff0000u); }
DI float bfround(float x) { return __uint_as_float(cvt2(x, 0.f) << 16); }
DI float fexp(float x) { return __builtin_amdgcn_exp2f(x * 1.4426950408889634f); }
DI rsrc_t mkrs(const void* p, unsigned bytes) { return __builtin_amdgcn_make_buffer_rsrc((void*)p, (short)0, (int)bytes, 0x00020000); }
DI void dma(rsrc_t rs, unsigned voff, unsigned soff, unsigned lds) {
    asm volatile("s_mov_b32 m0, %0\n\ts_nop 0\n\tbuffer_load_dwordx4 %1, %2, %3 offen lds" :: "s"(lds), "v"(voff), "s"(rs), "s"(soff) : "memory"); }
template <int N> DI void wait_bar() { asm volatile("s_waitcnt vmcnt(%0) lgkmcnt(0)\n\ts_barrier" :: "i"(N) : "memory"); }
template <int N> DI void wait_vm() { asm volatile("s_waitcnt vmcnt(%0)" :: "i"(N) : "memory"); }
DI void lds_bar() { asm volatile("s_waitcnt lgkmcnt(0)\n\ts_barrier" ::: "memory"); }
typedef LAS char* lp;
DI lp lbase(LAS char* L, unsigned off) { lp p = L + off; PIN(p); return p; }
DI bf16x8 ld128(lp p) { return __builtin_bit_cast(bf16x8, *(LAS const u32x4*)p); }
DI bf16x8 ldtr2(lp p0, lp p1) {
    s16x4 a = __builtin_amdgcn_ds_read_tr16_b64_v4i16((LAS s16x4*)p0), b = __builtin_amdgcn_ds_read_tr16_b64_v4i16((LAS s16x4*)p1);
    return __builtin_bit_cast(bf16x8, __builtin_shufflevector(a, b, 0, 1, 2, 3, 4, 5, 6, 7)); }
DI bf16x8 asbf(u32x4 v) { return __builtin_bit_cast(bf16x8, v); }
DI unsigned nosent(unsigned v) { return v < 0xFFFFFFFEu ? v : 0xFFFFFFFEu; }
DI unsigned atomic_read(rsrc_t rs, unsigned voff) { return (unsigned)__builtin_amdgcn_raw_ptr_buffer_atomic_add_i32(0, rs, (int)voff, 0, SC1); }
DI void atomic_inc(rsrc_t rs, unsigned voff) { asm volatile("buffer_atomic_add %0, %1, %2, 0 offen sc1" :: "v"(1u), "v"(voff), "s"(rs) : "memory"); }
struct cf { float r, i; };
DI cf cmul(cf a, cf b) { cf o;
    asm("v_mul_f32 %0, %3, %5\n\tv_mul_f32 %1, %3, %4\n\tv_fma_f32 %0, %2, %4, -%0\n\tv_fma_f32 %1, %2, %5, %1" : "=&v"(o.r), "=&v"(o.i) : "v"(a.r), "v"(a.i), "v"(b.r), "v"(b.i)); return o; }
DI cf cfma(cf a, cf b, cf c) { cf o;
    asm("v_fma_f32 %0, -%3, %5, %6\n\tv_fma_f32 %1, %3, %4, %7\n\tv_fma_f32 %0, %2, %4, %0\n\tv_fma_f32 %1, %2, %5, %1"
        : "=&v"(o.r), "=&v"(o.i) : "v"(a.r), "v"(a.i), "v"(b.r), "v"(b.i), "v"(c.r), "v"(c.i)); return o; }
DI void pk_cfma(f32x2& nr, f32x2& ni, f32x2 l, f32x2 hr, f32x2 hm, f32x2 a, f32x2 c) {
    asm("v_pk_fma_f32 %0, %2, %3, %5 op_sel_hi:[0,1,1]\n\tv_pk_fma_f32 %1, %2, %4, %6 op_sel_hi:[0,1,1]\n\t"
        "v_pk_fma_f32 %0, %2, %4, %0 op_sel:[1,0,0] neg_lo:[1,0,0] neg_hi:[1,0,0]\n\tv_pk_fma_f32 %1, %2, %3, %1 op_sel:[1,0,0]"
        : "=&v"(nr), "=&v"(ni) : "v"(l), "v"(hr), "v"(hm), "v"(a), "v"(c)); }
DI void pk_cmul(f32x2& nr, f32x2& ni, f32x2 l, f32x2 hr, f32x2 hm) {
    asm("v_pk_mul_f32 %0, %2, %3 op_sel_hi:[0,1]\n\tv_pk_mul_f32 %1, %2, %4 op_sel_hi:[0,1]\n\t"
        "v_pk_fma_f32 %0, %2, %4, %0 op_sel:[1,0,0] neg_lo:[1,0,0] neg_hi:[1,0,0]\n\tv_pk_fma_f32 %1, %2, %3, %1 op_sel:[1,0,0]"
        : "=&v"(nr), "=&v"(ni) : "v"(l), "v"(hr), "v"(hm)); }
DI f32x2 pk_scale(f32x2 s, f32x2 a) { f32x2 r; asm("v_pk_mul_f32 %0, %1, %2 op_sel_hi:[0,1]" : "=v"(r) : "v"(s), "v"(a)); return r; }
DI float sum4(float v) {
    auto a = __builtin_amdgcn_permlane16_swap(__float_as_uint(v), __float_as_uint(v), false, false); v = __uint_as_float(a[0]) + __uint_as_float(a[1]);
    auto c = __builtin_amdgcn_permlane32_swap(__float_as_uint(v), __float_as_uint(v), false, false); return __uint_as_float(c[0]) + __uint_as_float(c[1]); }
DI void bcast4(float v, float& r0, float& r1, float& r2, float& r3) {
    auto a = __builtin_amdgcn_permlane16_swap(__float_as_uint(v), __float_as_uint(v), false, false);
    auto e = __builtin_amdgcn_permlane32_swap(a[0], a[0], false, false); auto o = __builtin_amdgcn_permlane32_swap(a[1], a[1], false, false);
    r0 = __uint_as_float(e[0]); r2 = __uint_as_float(e[1]); r1 = __uint_as_float(o[0]); r3 = __uint_as_float(o[1]); }
DI float rowmax16(const f32x16& s) {
    float a = fmaxf(fmaxf(s[0], s[1]), s[2]), b = fmaxf(fmaxf(s[3], s[4]), s[5]);
    a = fmaxf(fmaxf(a, s[6]), s[7]); b = fmaxf(fmaxf(b, s[8]), s[9]); a = fmaxf(fmaxf(a, s[10]), s[11]); b = fmaxf(fmaxf(b, s[12]), s[13]);
    a = fmaxf(fmaxf(a, s[14]), s[15]); a = fmaxf(a, b);
    auto rr = __builtin_amdgcn_permlane32_swap(__float_as_uint(a), __float_as_uint(a), false, false);
    return fmaxf(__uint_as_float(rr[0]), __uint_as_float(rr[1])); }
DI void set_shift(u32x4& q1, float c, int hi) {
    const float c1 = bfround(c), c2 = bfround(c - c1), c3 = c - c1 - c2;
    if (hi) { q1[2] = cvt2(-c1, -c2); q1[3] = cvt2(-c3, 0.f); } }
DI void lcount(LAS char* L, unsigned off, int lane) {
    asm volatile("s_waitcnt lgkmcnt(0)" ::: "memory");
    if (lane == 0) __hip_atomic_fetch_add((LAS unsigned*)(L + off), 1u, __ATOMIC_RELAXED, __HIP_MEMORY_SCOPE_WORKGROUP); }
DI unsigned lread(LAS char* L, unsigned off) { return __builtin_amdgcn_readfirstlane(*(volatile LAS unsigned*)(L + off)); }
DI void lwait(LAS char* L, unsigned off, unsigned n) {
    if (__builtin_expect(lread(L, off) < n, 0)) { _Pragma("unroll 1") for (unsigned spin = 0; lread(L, off) < n && spin < (1u << 20); ++spin) { } }
    asm volatile("" ::: "memory"); }
DI void lwait_idle(LAS char* L, unsigned off, unsigned n) {
    _Pragma("unroll 1") for (unsigned spin = 0; lread(L, off) < n && spin < (1u << 18); ++spin) __builtin_amdgcn_s_sleep(2);
    asm volatile("" ::: "memory"); }
DI int xswz(int row) { return ((row >> 1) & 7) ^ ((row & 16) ? 7 : 0) ^ ((row & 32) ? 6 : 0); }
DI u32x4 pack8(const f32x16& a, int e) { return (u32x4){cvt2(a[e], a[e + 1]), cvt2(a[e + 2], a[e + 3]), cvt2(a[e + 4], a[e + 5]), cvt2(a[e + 6], a[e + 7])}; }
#define PAIR(v, p) (f32x2){v[(p) >> 1][2 * ((p) & 1)], v[(p) >> 1][2 * ((p) & 1) + 1]}

#define STAMP(k)
#define STAMPV(k)

__global__ void __launch_bounds__(512, 2)
gated_mix_kernel(const uint16_t* x, const uint16_t* wq, const uint16_t* wk, const uint16_t* wv, const uint16_t* wo, const uint16_t* lre, const uint16_t* lim,
                 const uint16_t* bre, const uint16_t* bim, const uint16_t* cre, const uint16_t* cim, const uint16_t* logdt, const uint16_t* dskip,
                 const uint16_t* gate, float* out, char* ws) {
    __shared__ __attribute__((aligned(16))) char smem[LDS_BYTES];
    LAS char* L = (LAS char*)smem;
    const unsigned l0 = (unsigned)(uintptr_t)L;
    const int lane = threadIdx.x & 63, w = __builtin_amdgcn_readfirstlane(threadIdx.x >> 6);
    const int b = blockIdx.x;
    STAMP(0);
    {   unsigned long long pc; asm volatile("s_getpc_b64 %0" : "=s"(pc));
        const rsrc_t rsC = mkrs((const void*)(pc & ~255ull), CPREF);
        asm volatile("s_mov_b32 m0, %0\n\ts_nop 4\n\tbuffer_load_dword %1, %2, 0 offen lds" :: "s"(l0 + L_DUMMY), "v"(threadIdx.x * 64), "s"(rsC) : "memory"); }
    const int blk = ((b & 7) >> 1) * 8 + (b >> 5), j = (b & 1) * 4 + ((b >> 3) & 3);
    const int th = w & 1, hd = (w >> 1) & 1;
    const int r32 = lane & 31, hi = lane >> 5, r16 = lane & 15, G = lane >> 4;
    const int tq = r16 >> 2, tp = lane & 3;
    const int sl = lane & 7, r8 = lane >> 3;
    const unsigned l16 = lane * 16;
    const int kind = j & 1, tg = j >> 1;
    unsigned vx = r8 * ROWB + ((sl ^ (r8 >> 1)) << 4); PIN(vx);
    PINS(x); PINS(wq); PINS(wk); PINS(wv); PINS(wo); PINS(lre); PINS(lim); PINS(bre); PINS(bim); PINS(cre); PINS(cim); PINS(logdt); PINS(dskip); PINS(gate); PINS(out); PINS(ws);
    const rsrc_t rsX = mkrs(x, SEQ * ROWB), rsWS = mkrs(ws, WS_END);
    const unsigned opiece = WS_O + blk * 65536 + (th * 16 + 2 * j + hd) * 2048;

    const rsrc_t rsWQ = mkrs(wq, DM * ROWB);
    const int pwq = sl ^ (((r8 >> 1) & 1) << 2);
    const unsigned vw = r8 * ROWB + 112 * j + (pwq << 4);
#define WQD(i0, i1) do { if (pwq != 7) { UNROLL for (int i = (i0); i < (i1); ++i) { const int p = i < 18 ? 3 * i + w - 1 : min(53 + w, 55); dma(rsWQ, vw, p * 8 * ROWB, l0 + L_WQ + p * 1024); } } } while (0)
    if (w < 4) {
        const bool master = b == 0 && w == 0;
        const unsigned bump = (master && lane < 16) ? WS_X + lane * 128 : OOB;
        if (w == 0) {
            const unsigned xoff = lane == 0 ? WS_X + (b & 15) * 128 : OOB;
            const rsrc_t rsWK = mkrs(kind ? wv : wk, DM * 56);
            const unsigned vxa = vx ^ (xswz(16 * tg) << 4), vxb = (vx ^ (xswz(16 * tg + 8) << 4)) + 8 * ROWB, xs = (blk * 64 + tg * 16) * ROWB;
            STAMP(36);
            UNROLL for (int c = 0; c < 7; ++c) {
                dma(rsX, vxa, xs + c * 128, l0 + L_X + c * 8192 + tg * 2048); dma(rsX, vxb, xs + c * 128, l0 + L_X + c * 8192 + tg * 2048 + 1024);
                UNROLL for (int n = (7 * c + 1) / 2; n < (7 * c + 8) / 2; ++n) dma(rsWK, l16, n * 1024, l0 + L_WK + n * 1024);
            }
            asm volatile("s_barrier" ::: "memory");
            STAMP(14);
            unsigned x0 = 0u, x1 = 0u;
            asm volatile("buffer_atomic_add %0, %1, %2, 0 offen sc0 sc1" : "+v"(x0) : "v"(xoff), "s"(rsWS) : "memory");
            __builtin_amdgcn_raw_buffer_store_b64((u32x2){~0u, ~0u}, rsWS, lane < 56 ? lane * 8 : OOB, WS_E + (blk * DM + 56 * j) * 8, SC1);
            if (master) {
                __builtin_amdgcn_raw_buffer_store_b32(0u, rsWS, lane * 128, 0, SC1);
                __builtin_amdgcn_raw_buffer_store_b32(0u, rsWS, lane * 128 + 8192, 0, SC1);
                __builtin_amdgcn_raw_buffer_store_b32(0u, rsWS, lane < 32 ? lane * 128 + 16384 : OOB, 0, SC1);
                wait_vm<0>();
                atomic_inc(rsWS, bump);
            }
            const int cg0 = kind ? tp : 4 * (tp >> 1) + (tp & 1), cg1 = kind ? 4 + tp : 4 * (tp >> 1) + 2 + (tp & 1);
            const lp aw0 = lbase(L, L_WK + (8 * G + tq) * 56 + cg0 * 8), aw1 = lbase(L, L_WK + (8 * G + tq) * 56 + cg1 * 8);
            const unsigned axo = L_X + (16 * tg + r16) * 128 + ((G ^ xswz(16 * tg + r16)) << 4);
            const lp ax0 = lbase(L, axo), ax1 = lbase(L, axo ^ 64);
            const int sg = tg & 1;
            const unsigned tb = WS_KV + (2 * blk + (tg >> 1)) * 4096 + (kind ? 2048 + sg * 1024 : 0);
            unsigned kva = kind ? ((G >> 1) * 32 + r16) * 16 + (G & 1) * 8 : (G * 32 + 16 * sg + ((r16 & 3) | ((r16 & 4) << 1) | ((r16 & 8) >> 1))) * 16; PIN(kva);
            unsigned kvb = kind ? kva + 256 : kva + 8; PIN(kvb);
            const bool spare = kind ? r16 >= 12 : G == 3;
            unsigned sp2 = (kind && r16 != 12) ? 0u : 0x3F803F80u, sp3 = kind ? sp2 : 0x00003F80u; PIN(sp2); PIN(sp3);
            f32x4 d0 = {0.f, 0.f, 0.f, 0.f}, d1 = d0;
            bf16x8 xf[14], w0[14], w1[14];
#define KVR(c) do { wait_vm<2 + 39 - 2 * ((c) + 1) - (7 * (c) + 8) / 2>(); UNROLL for (int ks = 2 * (c); ks < 2 * (c) + 2; ++ks) { xf[ks] = ld128(((ks & 1) ? ax1 : ax0) + (c) * 8192); \
                        w0[ks] = ldtr2(aw0 + ks * 1792, aw0 + ks * 1792 + 224); w1[ks] = ldtr2(aw1 + ks * 1792, aw1 + ks * 1792 + 224); } SBAR(); } while (0)
#define KVM(c, K) do { UNROLL for (int ks = 2 * (c); ks < 2 * (c) + 2; ++ks) { \
                        if (K) { d0 = MFMA16(xf[ks], w0[ks], d0); d1 = MFMA16(xf[ks], w1[ks], d1); } else { d0 = MFMA16(w0[ks], xf[ks], d0); d1 = MFMA16(w1[ks], xf[ks], d1); } } SBAR(); } while (0)
#define KVP(K) do { KVR(0); STAMP(1); KVR(1); KVM(0, K); KVR(2); KVM(1, K); KVR(3); KVM(2, K); KVR(4); KVM(3, K); KVR(5); KVM(4, K); KVR(6); STAMP(32); KVM(5, K); KVM(6, K); } while (0)
            if (kind) KVP(1); else KVP(0);
            __builtin_amdgcn_raw_buffer_store_b64((u32x2){cvt2(d0[0], d0[1]), cvt2(d0[2], d0[3])}, rsWS, kva, tb, SC1);
            __builtin_amdgcn_raw_buffer_store_b64((u32x2){spare ? sp2 : cvt2(d1[0], d1[1]), spare ? sp3 : cvt2(d1[2], d1[3])}, rsWS, kvb, tb, SC1);
            asm volatile("buffer_atomic_add %0, %1, %2, 0 offen sc0 sc1" : "+v"(x1) : "v"(xoff), "s"(rsWS) : "memory");
            if (lane < 2) __hip_atomic_fetch_add((LAS unsigned*)(L + L_KD + lane * 4), 1u, __ATOMIC_RELAXED, __HIP_MEMORY_SCOPE_WORKGROUP);
            STAMP(12);
            asm volatile("s_waitcnt vmcnt(0)" : "+v"(x0), "+v"(x1) :: "memory");
            STAMP(35);
            const unsigned s0 = __builtin_amdgcn_readfirstlane(x0); unsigned s1 = __builtin_amdgcn_readfirstlane(x1);
            _Pragma("unroll 1") for (unsigned spin = 0; !master && s1 == s0 && spin < SPIN_CAP; ++spin) s1 = __builtin_amdgcn_readfirstlane(atomic_read(rsWS, xoff));
            atomic_inc(rsWS, lane < 8 ? WS_KVCNT + lane * 2048 + (b & 15) * 128 : OOB);
            atomic_inc(rsWS, bump);
            STAMP(13);
        } else {
            unsigned g0, g1, g2;
            const int gk = min((w - 1) * 192, 256) + lane;
            const uint16_t* gp = gate + gk; PIN(gp);
            if (w == 1 && lane < 13) *(LAS unsigned*)(L + L_KD + lane * 4) = 0u;
            lds_bar();
            asm volatile("global_load_ushort %0, %3, off\n\tglobal_load_ushort %1, %3, off offset:128\n\tglobal_load_ushort %2, %3, off offset:256"
                         : "=&v"(g0), "=&v"(g1), "=&v"(g2) : "v"(gp) : "memory");
            WQD(0, 19);
            STAMP(14);
            asm volatile("s_waitcnt vmcnt(9)" : "+v"(g0), "+v"(g1), "+v"(g2) :: "memory");
            const lp gl = L + (L_WQ + gk * 128 + (112 ^ (((gk >> 1) & 1) << 6)));
            *(LAS uint16_t*)(gl) = (uint16_t)g0; *(LAS uint16_t*)(gl + 8192) = (uint16_t)g1; *(LAS uint16_t*)(gl + 16384) = (uint16_t)g2;
            lcount(L, L_XF, lane);
            STAMP(1);
        }
        const int qrow = 32 * th + r32;
        const unsigned a0o = L_X + qrow * 128 + ((hi ^ xswz(qrow)) << 4);
        const lp a00 = lbase(L, a0o), a01 = lbase(L, a0o ^ 32), a02 = lbase(L, a0o ^ 64), a03 = lbase(L, a0o ^ 96);
        const lp a1 = lbase(L, L_WQ + (8 * hi + tq) * 128 + ((56 * hd + 32 * (G & 1) + 8 * tp) ^ (((tq >> 1) & 1) << 6)));
        unsigned kco = (w == 0 && lane < 16) ? WS_KVCNT + (b & 7) * 2048 + lane * 128 : OOB, kc = 0u; PIN(kco);
        f32x16 o[4];
        o[0] = o[1] = o[2] = o[3] = (f32x16){}; PIN(o[0]); PIN(o[1]); PIN(o[2]); PIN(o[3]);
        lwait(L, L_XA, 5u); lwait(L, L_XF, 3u);
        STAMP(2);
        f32x16 qa = {};
        { bf16x8 wf[28], xq[28];
          UNROLL for (int ks = 0; ks < 33; ++ks) {
            if (ks == WQ2) { wait_vm<0>(); lcount(L, L_XG, lane); }
            if (ks == 15) lwait(L, L_XG, 4u);
            if (ks < 28) { wf[ks] = ldtr2(a1 + ks * 2048, a1 + ks * 2048 + 512); xq[ks] = ld128(((ks & 3) == 0 ? a00 : (ks & 3) == 1 ? a01 : (ks & 3) == 2 ? a02 : a03) + (ks >> 2) * 8192); }
            if (ks >= 5) qa = MFMA32(wf[ks - 5], xq[ks - 5], qa);
            if (ks == KPOLL) asm volatile("buffer_atomic_add %0, %1, %2, 0 offen sc0 sc1" : "+v"(kc) : "v"(kco), "s"(rsWS) : "memory");
            if (ks == 28 && lane == 0) __hip_atomic_fetch_add((LAS unsigned*)(L + L_QD), 1u, __ATOMIC_RELAXED, __HIP_MEMORY_SCOPE_WORKGROUP);
            SBAR(); } }
        u32x4 kf[4][2], vf[4][2], pw0, pw1;
        const unsigned kv0 = WS_KV + w * 65536;
        unsigned kvs = kv0;
#define LD2(A0, A1, off) asm volatile("buffer_load_dwordx4 %0, %2, %3, %4 offen offset:%5 sc1\n\tbuffer_load_dwordx4 %1, %2, %3, %4 offen offset:%6 sc1" \
                                      : "=&v"(A0), "=&v"(A1) : "v"(l16), "s"(rsWS), "s"(kvs), "i"(off), "i"((off) + 1024) : "memory")
#define WAIT2(N, A, B) asm volatile("s_waitcnt vmcnt(%2)" : "+v"(A), "+v"(B) : "i"(N) : "memory")
        asm volatile("s_waitcnt vmcnt(0)" : "+v"(kc) :: "memory");
        const bool kvok = __builtin_amdgcn_ballot_w64(kc != 16 && lane < 16) == 0;
        if (w == 0 && kvok) lcount(L, L_KR, lane);
        lwait(L, L_QD, 4u);
        asm volatile("s_nop 11" : "+v"(qa));
        const f32x2 cs = {0.18898223650461361f * 1.4426950408889634f, 0.f};
        u32x4 q0[4], q1[4];
        UNROLL for (int e = 0; e < 4; ++e) { const f32x2 a = pk_scale(cs, (f32x2){qa[2 * e], qa[2 * e + 1]}), c = pk_scale(cs, (f32x2){qa[8 + 2 * e], qa[9 + 2 * e]});
            q0[3][e] = cvt2(a[0], a[1]); q1[3][e] = cvt2(c[0], c[1]); }
        if (hi) { q1[3][2] = 0u; q1[3][3] = 0u; }
        *(LAS u32x4*)(L + L_QSH + w * 2048 + l16) = q0[3];
        *(LAS u32x4*)(L + L_QSH + w * 2048 + 1024 + l16) = q1[3];
        if (hd == 1 && hi) *(LAS float*)(L + L_G + qrow * 4) = __builtin_amdgcn_rcpf(1.0f + fexp(-qa[12]));
        STAMP(3);
        lcount(L, L_QF, lane);
        if (w == 0 && !kvok) {
            _Pragma("unroll 1") for (unsigned spin = 0; spin < SPIN_CAP; ++spin) {
                atomic_inc(rsWS, bump); if (__builtin_amdgcn_ballot_w64(atomic_read(rsWS, kco) != 16 && lane < 16) == 0) break; }
            lcount(L, L_KR, lane);
        }
        lwait(L, L_KR, 1u);
        STAMP(4);

        f32x16 sA, sB;
        LD2(kf[0][0], kf[0][1], 0); LD2(vf[0][0], vf[0][1], 2048);
        lwait(L, L_QF, 4u);
        STAMP(5);
        UNROLL for (int q = 0; q < 3; ++q) { const lp a = lbase(L, L_QSH + (w ^ (q + 1)) * 2048 + l16); q0[q] = *(LAS const u32x4*)(a); q1[q] = *(LAS const u32x4*)(a + 1024); }
        const float gt = *(LAS const float*)(L + L_G + qrow * 4);
        pw1 = (u32x4){0u, 0u, 0u, 0u}; vf[3][1] = pw1; PIN(pw1); PIN(vf[3][1]);
        UNROLL for (int n = 1; n < 3; ++n) { kvs += 4096; LD2(kf[n][0], kf[n][1], 0); LD2(vf[n][0], vf[n][1], 2048); }
        WAIT2(10, kf[0][0], kf[0][1]);
        STAMP(6);
        sA = MFMA32(asbf(kf[0][0]), asbf(q0[0]), (f32x16){}); sA = MFMA32(asbf(kf[0][1]), asbf(q1[0]), sA);
#define EXP(A, i) A[i] = __builtin_amdgcn_exp2f(A[i])
#define EX4(A, i) do { EXP(A, i); EXP(A, i + 1); EXP(A, i + 2); EXP(A, i + 3); PIN(A); } while (0)
#define UNIT(A, B, KN0, KN1, qn, q, qp, V0, VP1, NEXT, H0, H1, H3) do { SBAR(); H0; \
        if (NEXT) { B = MFMA32(asbf(KN0), asbf(q0[qn]), (f32x16){}); PIN(B); } SBAR(); \
        EX4(A, 0); SBAR(); \
        o[qp] = MFMA32(asbf(VP1), asbf(pw1), o[qp]); PIN(o[qp]); SBAR(); H1; \
        pw0[0] = cvt2(A[0], A[1]); pw0[1] = cvt2(A[2], A[3]); PIN(pw0); SBAR(); EX4(A, 4); SBAR(); \
        if (NEXT) { B = MFMA32(asbf(KN1), asbf(q1[qn]), B); PIN(B); } SBAR(); \
        pw0[2] = cvt2(A[4], A[5]); pw0[3] = cvt2(A[6], A[7]); PIN(pw0); SBAR(); EX4(A, 8); SBAR(); H3; \
        o[q] = MFMA32(asbf(V0), asbf(pw0), o[q]); PIN(o[q]); SBAR(); \
        { float e12 = __builtin_amdgcn_exp2f(A[12]), e13 = __builtin_amdgcn_exp2f(A[13]), e14 = __builtin_amdgcn_exp2f(A[14]), e15 = __builtin_amdgcn_exp2f(A[15]); \
          asm volatile("" : "+v"(e12), "+v"(e13), "+v"(e14), "+v"(e15)); SBAR(); \
          pw1[0] = cvt2(A[8], A[9]); pw1[1] = cvt2(A[10], A[11]); SBAR(); pw1[2] = cvt2(e12, e13); pw1[3] = cvt2(e14, e15); PIN(pw1); } SBAR(); } while (0)
#define TRIP(c, n, p, LD, NV, NK, NEXT) do { \
        UNIT(sA, sB, kf[c][0], kf[c][1], 1, 0, 3, vf[c][0], vf[p][1], true, {}, { if (LD) { kvs += 4096; LD2(kf[p][0], kf[p][1], 0); } }, WAIT2(NV, vf[c][0], vf[c][1])); \
        UNIT(sB, sA, kf[c][0], kf[c][1], 2, 1, 0, vf[c][0], vf[c][1], true, {}, { if (LD) LD2(vf[p][0], vf[p][1], 2048); }, {}); \
        UNIT(sA, sB, kf[c][0], kf[c][1], 3, 2, 1, vf[c][0], vf[c][1], true, {}, {}, {}); \
        UNIT(sB, sA, kf[n][0], kf[n][1], 0, 3, 2, vf[c][0], vf[c][1], NEXT, { if (NEXT) WAIT2(NK, kf[n][0], kf[n][1]); }, {}, {}); } while (0)
        _Pragma("unroll 1") for (int it = 0; it < 3; ++it) {
            TRIP(0, 1, 3, true, 10, 10, true); TRIP(1, 2, 0, true, 10, 10, true); TRIP(2, 3, 1, true, 10, 10, true); TRIP(3, 0, 2, true, 10, 10, true);
            STAMPV(17 + it);
        }
        TRIP(0, 1, 3, true, 10, 10, true); TRIP(1, 2, 0, false, 8, 6, true); TRIP(2, 3, 1, false, 4, 2, true);
        if (lane == 0) __hip_atomic_fetch_add((LAS unsigned*)(L + L_QE), 1u, __ATOMIC_RELAXED, __HIP_MEMORY_SCOPE_WORKGROUP);
        TRIP(3, 0, 2, false, 0, 0, false);
        STAMPV(20);
        o[3] = MFMA32(asbf(vf[3][1]), asbf(pw1), o[3]);
        lwait(L, L_UF, 4u);
        STAMP(7);

        const unsigned rob = L_RO + w * 2048 + l16, lsb = L_LS + (w * 32 + r32) * 16;
        auto give = [&](const f32x16& oq, int q) __attribute__((always_inline)) {
            const int xa = (q + 1) & 3;
            if (q < 3) { const lp p = lbase(L, rob ^ (xa * 2048)) + (xa - 1) * 8192; *(LAS u32x4*)(p) = pack8(oq, 0); *(LAS u32x4*)(p + 1024) = pack8(oq, 8); }
            if (hi) *(LAS float*)(L + (lsb ^ (xa * 512)) + w * 4) = oq[12];
            return oq[12]; };
        {   bool ok = true;
            UNROLL for (int q = 0; q < 4; ++q) { const float ls = give(o[q], q); ok &= ls >= 5.421011e-20f && ls <= 1.1529215e18f; }
            if (hi && !ok) *(LAS unsigned*)(L + L_FLAG) = 1u; }
        u32x4 sel0, sel1;
        { const int js = 4 * ((r32 >> 3) & 1) + (r32 & 3); const unsigned one = ((r32 >> 2) & 1) == hi ? 0x3F80u << (16 * (js & 1)) : 0u;
          UNROLL for (int e = 0; e < 4; ++e) { const unsigned v = (js >> 1) == e ? one : 0u; sel0[e] = r32 < 16 ? v : 0u; sel1[e] = r32 < 16 ? 0u : v; } PIN(sel0); PIN(sel1); }
        const lp rop = lbase(L, rob), lsp = lbase(L, lsb);
        lcount(L, L_GV, lane); lwait(L, L_GV, 4u);
        STAMP(15);
        bf16x8 f[6]; f32x4 m0;
        const unsigned flag = *(LAS const unsigned*)(L + L_FLAG);
        UNROLL for (int t = 0; t < 6; ++t) f[t] = ld128(rop + (t >> 1) * 8192 + (t & 1) * 1024);
        m0 = *(LAS const f32x4*)(lsp);
        SBAR();
        if (__builtin_expect(__builtin_amdgcn_readfirstlane(flag) != 0u, 0)) {
            float mx[4] = {-INFINITY, -INFINITY, -INFINITY, -INFINITY};
            lcount(L, L_GV, lane); lwait(L, L_GV, 8u);
            _Pragma("unroll 1") for (int t = 0; t < 16; ++t) {
                const bf16x8 a = asbf(__builtin_amdgcn_raw_buffer_load_b128(rsWS, l16, kv0 + t * 4096, SC1));
                const bf16x8 c = asbf(__builtin_amdgcn_raw_buffer_load_b128(rsWS, l16, kv0 + t * 4096 + 1024, SC1));
                UNROLL for (int q = 0; q < 4; ++q) { f32x16 s = MFMA32(a, asbf(q0[q]), (f32x16){}); s = MFMA32(c, asbf(q1[q]), s); mx[q] = fmaxf(mx[q], rowmax16(s)); SBAR(); }
            }
            UNROLL for (int q = 0; q < 4; ++q) if (hi) *(LAS float*)(L + (lsb ^ (((q + 1) & 3) * 512)) + w * 4) = mx[q];
            lcount(L, L_GV, lane); lwait(L, L_GV, 12u);
            UNROLL for (int q = 0; q < 4; ++q) { const f32x4 n0 = *(LAS const f32x4*)(L + (lsb ^ (((q + 1) & 3) * 512)));
                set_shift(q1[q], fmaxf(fmaxf(n0[0], n0[1]), fmaxf(n0[2], n0[3])), hi); }
            lcount(L, L_GV, lane); lwait(L, L_GV, 16u);
            UNROLL for (int q = 0; q < 4; ++q) {
                f32x16 acc = {};
                _Pragma("unroll 1") for (int t = 0; t < 16; ++t) {
                    const bf16x8 a = asbf(__builtin_amdgcn_raw_buffer_load_b128(rsWS, l16, kv0 + t * 4096, SC1));
                    const bf16x8 c = asbf(__builtin_amdgcn_raw_buffer_load_b128(rsWS, l16, kv0 + t * 4096 + 1024, SC1));
                    const bf16x8 d = asbf(__builtin_amdgcn_raw_buffer_load_b128(rsWS, l16, kv0 + t * 4096 + 2048, SC1));
                    const bf16x8 e = asbf(__builtin_amdgcn_raw_buffer_load_b128(rsWS, l16, kv0 + t * 4096 + 3072, SC1));
                    f32x16 s = MFMA32(a, asbf(q0[q]), (f32x16){}); s = MFMA32(c, asbf(q1[q]), s);
                    UNROLL for (int r = 0; r < 16; ++r) s[r] = __builtin_amdgcn_exp2f(s[r]);
                    acc = MFMA32(d, asbf(pack8(s, 0)), acc); acc = MFMA32(e, asbf(pack8(s, 8)), acc);
                }
                give(acc, q);
                if (q == 3) o[3] = acc;
            }
            lcount(L, L_GV, lane); lwait(L, L_GV, 20u);
            UNROLL for (int t = 0; t < 6; ++t) f[t] = ld128(rop + (t >> 1) * 8192 + (t & 1) * 1024);
            m0 = *(LAS const f32x4*)(lsp);
        }
        __builtin_amdgcn_s_setprio(1);
        f32x16 t = o[3];
        UNROLL for (int k = 0; k < 6; ++k) t = MFMA32(asbf((k & 1) ? sel1 : sel0), f[k], t);
        {
            float s0 = m0[0] + m0[1], s2 = m0[2] + m0[3]; PIN(s0); PIN(s2);
            f32x2 sc = {gt * __builtin_amdgcn_rcpf(s0 + s2), 0.f};
            asm volatile("s_nop 11" : "+v"(t), "+v"(sc));
            u32x4 oa, ob;
            UNROLL for (int e = 0; e < 4; ++e) { const f32x2 a = pk_scale(sc, (f32x2){t[2 * e], t[2 * e + 1]}); oa[e] = nosent(cvt2(a[0], a[1])); }
            __builtin_amdgcn_raw_buffer_store_b128(oa, rsWS, l16, opiece, SC1);
            UNROLL for (int e = 0; e < 4; ++e) { const f32x2 a = pk_scale(sc, (f32x2){t[8 + 2 * e], t[9 + 2 * e]}); ob[e] = nosent(cvt2(a[0], a[1])); }
            if (hi) { ob[2] = 0u; ob[3] = 0u; }
            __builtin_amdgcn_raw_buffer_store_b128(ob, rsWS, l16, opiece + 1024, SC1);
        }
        __builtin_amdgcn_s_setprio(0);
        STAMP(8);
        const int tok = blk * 64 + qrow, col = 56 * j + 32 * hd + 4 * hi;
        const rsrc_t rsD = mkrs(dskip, ROWB), rsO = mkrs(out, SEQ * DM * 4);
        f32x4 r[4];
        UNROLL for (int e = 0; e < 4; ++e) {
            const u32x2 xv = __builtin_amdgcn_raw_buffer_load_b64(rsX, tok * ROWB + col * 2 + e * 16, 0, 0), dv = __builtin_amdgcn_raw_buffer_load_b64(rsD, col * 2 + e * 16, 0, 0);
            float t0 = bf2f(xv[0]) * bf2f(dv[0]), t1 = bfhi(xv[0]) * bfhi(dv[0]), t2 = bf2f(xv[1]) * bf2f(dv[1]), t3 = bfhi(xv[1]) * bfhi(dv[1]);
            PIN(t0); PIN(t1); PIN(t2); PIN(t3); t0 *= 1.0f - gt; t1 *= 1.0f - gt; t2 *= 1.0f - gt; t3 *= 1.0f - gt; PIN(t0); PIN(t1); PIN(t2); PIN(t3);
            r[e] = (f32x4){t0, t1, t2, t3}; }
        const lp rp = lbase(L, L_RED + hd * 32768 + th * 16384 + l16);
        unsigned oo = (tok * DM + col) * 4, o3 = hd ? OOB : oo + 96; PIN(oo); PIN(o3);
        lds_bar();
        STAMP(10);
        UNROLL for (int e = 0; e < 4; ++e) {
            r[e] += (*(LAS const f32x4*)(rp + e * 1024) + *(LAS const f32x4*)(rp + 4096 + e * 1024)) + (*(LAS const f32x4*)(rp + 8192 + e * 1024) + *(LAS const f32x4*)(rp + 12288 + e * 1024));
            __builtin_amdgcn_raw_buffer_store_b128(__builtin_bit_cast(u32x4, r[e]), rsO, e < 3 ? oo + e * 32 : o3, 0, 0); }
    } else {
        const int u = w - 4, st = min(16 * u + r16, 55);
        const unsigned eoff = (G == 0 && 16 * u + r16 < 56) ? st * 8 : OOB;
        const rsrc_t rsBR = mkrs(bre, DM * ROWB), rsBI = mkrs(bim, DM * ROWB);
        const int rb_ = u < 3 ? 8 : 0;
        const unsigned vb0_ = (56 * j + 16 * u + r8) * ROWB + ((sl ^ (r8 >> 1)) << 4), vb1_ = u < 3 ? (vb0_ ^ 64) + 8 * ROWB : vb0_;
        const int ga = 2 * u + (u >= tg ? 2 : 0);
        const unsigned vxa = vx ^ (xswz(8 * ga) << 4), vxb = (vx ^ (xswz(8 * ga + 8) << 4)) + 8 * ROWB, xs = (blk * 64 + 8 * ga) * ROWB;
        unsigned p0, p1, p2;
        const uint16_t *a0 = lre + 56 * j + st, *a1 = lim + 56 * j + st, *a2 = logdt + 56 * j + st; PIN(a0); PIN(a1); PIN(a2);
        asm volatile("s_barrier" ::: "memory");
        asm volatile("global_load_ushort %0, %3, off\n\tglobal_load_ushort %1, %4, off\n\tglobal_load_ushort %2, %5, off"
                     : "=&v"(p0), "=&v"(p1), "=&v"(p2) : "v"(a0), "v"(a1), "v"(a2) : "memory");
        if (u < 3) { UNROLL for (int c = 0; c < 7; ++c) { dma(rsX, vxa, xs + c * 128, l0 + L_X + c * 8192 + ga * 1024); dma(rsX, vxb, xs + c * 128, l0 + L_X + c * 8192 + ga * 1024 + 1024); } }
#define BOFF(c) ((c) % 3 == 0 ? L_B0 : L_WK + ((c) % 3 - 1) * 14336)
#define BD(c) do { dma(rsBR, vb0_, (c) * 128, l0 + BOFF(c) + u * 2048); dma(rsBI, vb0_, (c) * 128, l0 + BOFF(c) + 7168 + u * 2048); \
                   dma(rsBR, vb1_, (c) * 128, l0 + BOFF(c) + u * 2048 + rb_ * 128); dma(rsBI, vb1_, (c) * 128, l0 + BOFF(c) + 7168 + u * 2048 + rb_ * 128); } while (0)
        BD(0);
        STAMP(14);
        asm volatile("s_waitcnt vmcnt(4)" : "+v"(p0), "+v"(p1), "+v"(p2) :: "memory");
        lcount(L, L_XA, lane);
        STAMP(1);
        f32x2 lam, coef; cf lam8, lam16, lam64, wlo, whi, zG;
        {   const float lr = bf2f(p0), li = bf2f(p1), dt = bfround(fexp(bf2f(p2)));
            const float er = fexp(dt * lr), rev = __builtin_amdgcn_fractf(dt * li * 0.15915494309189535f);
            const cf l1 = {er * __builtin_amdgcn_cosf(rev), er * __builtin_amdgcn_sinf(rev)};
            const float inv = __builtin_amdgcn_rcpf(lr * lr + li * li);
            const cf cq = cmul((cf){l1.r - 1.0f, l1.i}, (cf){lr * inv, -li * inv});
            lam = (f32x2){l1.r, l1.i}; coef = (f32x2){cq.r, cq.i};
            const cf l2 = cmul(l1, l1), l4 = cmul(l2, l2); lam8 = cmul(l4, l4); lam16 = cmul(lam8, lam8);
            const cf l32 = cmul(lam16, lam16); lam64 = cmul(l32, l32);
            const cf l128 = cmul(lam64, lam64), l256 = cmul(l128, l128), l512 = cmul(l256, l256), l1024 = cmul(l512, l512), one = {1.f, 0.f};
            wlo = cmul((G & 1) ? lam16 : one, (G & 2) ? l32 : one); whi = cmul(wlo, lam8);
            zG = cmul((G & 1) ? l512 : one, (G & 2) ? l1024 : one);
            asm volatile("" : "+v"(lam), "+v"(coef), "+v"(lam8.r), "+v"(lam8.i), "+v"(lam16.r), "+v"(lam16.i), "+v"(lam64.r), "+v"(lam64.i),
                              "+v"(wlo.r), "+v"(wlo.i), "+v"(whi.r), "+v"(whi.i), "+v"(zG.r), "+v"(zG.i) :: "memory"); }
        const int rr = u < 3 ? r16 : r16 & 7;
        const unsigned bro = u * 2048 + rr * 128 + ((G ^ (rr >> 1)) << 4);
        const lp br0 = lbase(L, L_WK + bro), br1 = lbase(L, L_WK + (bro ^ 64)), br2 = lbase(L, L_B0 + bro), br3 = lbase(L, L_B0 + (bro ^ 64));
        lp at[4][2];
        UNROLL for (int m = 0; m < 4; ++m) { const int row = 16 * (r16 >> 2) + 2 * m + ((r16 & 3) >> 1) + 8 * (r16 & 1); const unsigned a = L_X + row * 128 + ((G ^ xswz(row)) << 4);
            at[m][0] = lbase(L, a); at[m][1] = lbase(L, a ^ 64); }
        lwait(L, L_KD, 1u);
        BD(1); BD(2);
        f32x4 ur[4] = {}, ui[4] = {};
        bf16x8 fb[2][4], fx[2][8];
#define BP(c, k) ((c) % 3 == 0 ? ((k) ? br3 : br2) : ((k) ? br1 : br0) + ((c) % 3 - 1) * 14336)
#define BRD(c) do { wait_vm<(c) < 5 ? 8 : 4 * (6 - (c))>(); fb[(c) & 1][0] = ld128(BP(c, 0)); fb[(c) & 1][1] = ld128(BP(c, 0) + 7168); fb[(c) & 1][2] = ld128(BP(c, 1)); fb[(c) & 1][3] = ld128(BP(c, 1) + 7168); \
                    if ((c) < 4) { asm volatile("s_waitcnt lgkmcnt(0)" ::: "memory"); BD((c) + 3); } } while (0)
#define XRD(c, i) fx[(c) & 1][i] = ld128(at[(i) & 3][(i) >> 2] + (c) * 8192)
#define UCH(c) UNROLL for (int i = 0; i < 8; ++i) { \
            if (i == 0 && (c) < 6) BRD((c) < 6 ? (c) + 1 : 6); \
            ur[i & 3] = MFMA16(fx[(c) & 1][i], fb[(c) & 1][2 * (i >> 2)], ur[i & 3]); ui[i & 3] = MFMA16(fx[(c) & 1][i], fb[(c) & 1][2 * (i >> 2) + 1], ui[i & 3]); \
            if ((c) < 6) XRD((c) + 1, i); \
            SBAR(); }
        BRD(0);
        lwait(L, L_XA, 5u);
        STAMP(2);
        UNROLL for (int i = 0; i < 8; ++i) XRD(0, i);
        SBAR();
        UCH(0) UCH(1) UCH(2)
        __builtin_amdgcn_s_setprio(UPRIO);
        UCH(3) UCH(4) UCH(5) UCH(6)
        lcount(L, L_UF, lane);
        asm volatile("s_nop 7" : "+v"(ur[0]), "+v"(ur[1]), "+v"(ur[2]), "+v"(ur[3]), "+v"(ui[0]), "+v"(ui[1]), "+v"(ui[2]), "+v"(ui[3]));
        STAMP(3);
        f32x2 hr, hm;
        UNROLL for (int p = 0; p < 8; ++p) {
            f32x2 tr, ti; pk_cmul(tr, ti, coef, PAIR(ur, p), PAIR(ui, p));
            ur[p >> 1][2 * (p & 1)] = tr[0]; ur[p >> 1][2 * (p & 1) + 1] = tr[1]; ui[p >> 1][2 * (p & 1)] = ti[0]; ui[p >> 1][2 * (p & 1) + 1] = ti[1];
            if (p == 0) { hr = tr; hm = ti; } else { f32x2 nr, ni; pk_cfma(nr, ni, lam, hr, hm, tr, ti); hr = nr; hm = ni; }
        }
        const cf elo = {hr[0], hm[0]}, e16 = cfma(lam8, elo, (cf){hr[1], hm[1]});
        cf e0, e1, e2, e3; bcast4(e16.r, e0.r, e1.r, e2.r, e3.r); bcast4(e16.i, e0.i, e1.i, e2.i, e3.i);
        const cf t2 = cfma(lam16, e0, e1), t3 = cfma(lam16, t2, e2), eb = cfma(lam16, t3, e3);
        lwait(L, L_KR, 1u); lwait(L, L_QF, 4u);
        __builtin_amdgcn_raw_buffer_store_b64((u32x2){nosent(__float_as_uint(eb.r)), nosent(__float_as_uint(eb.i))}, rsWS, eoff, WS_E + (blk * DM + 56 * j) * 8, SC1);
        const cf pg = {G == 0 ? 0.f : G == 1 ? e0.r : G == 2 ? t2.r : t3.r, G == 0 ? 0.f : G == 1 ? e0.i : G == 2 ? t2.i : t3.i}, ph = cfma(lam8, pg, elo);
        STAMP(4);
        asm volatile("s_mov_b32 m0, %0\n\ts_nop 0\n\tbuffer_load_dword %1, %2, %3 offen sc1 lds"
                     :: "s"(l0 + L_DUMMY), "v"(u == 0 ? lane * 128 : OOB), "s"(rsWS), "s"(WS_KV + (b >> 3) * 8192) : "memory");
        __builtin_amdgcn_raw_buffer_store_b128((u32x4){~0u, ~0u, ~0u, ~0u}, rsWS, l16, opiece, SC1);
        __builtin_amdgcn_raw_buffer_store_b128((u32x4){~0u, ~0u, ~0u, ~0u}, rsWS, l16, opiece + 1024, SC1);
        const float gt = *(LAS const float*)(L + L_G + (32 * th + r32) * 4), go = *(LAS const float*)(L + L_G + (32 * (th ^ 1) + r32) * 4);
        lwait(L, L_UF, 4u);
        { const rsrc_t rsWO = mkrs(wo, DM * ROWB);
          UNROLL for (int n = 0; n < 14; ++n) { dma(rsWO, vw, (u * 14 + n) * 8 * ROWB, l0 + L_WO + (u * 14 + n) * 1024); PACE(); } }
        const rsrc_t rsE = mkrs(ws + WS_E + 56 * j * 8, 32 * DM * 8 - 56 * j * 8);
        const int ebase = (blk - 1 - 8 * G) * (DM * 8) + st * 8;
        const unsigned hoff = lane == 0 ? WS_HCNT + blk * 128 : OOB;
        wait_vm<0>();
        lcount(L, L_WF, lane);
        STAMP(38);
        {
            u32x2 ev[8];
            _Pragma("unroll 1") for (unsigned spin = 0; spin < SPIN_CAP; ++spin) {
                asm volatile("" ::: "memory");
                UNROLL for (int m = 0; m < 8; ++m) ev[m] = __builtin_amdgcn_raw_buffer_load_b64(rsE, ebase - m * (DM * 8), 0, SC1);
                unsigned mx = 0;
                UNROLL for (int m = 0; m < 8; ++m) mx = max(mx, max(ev[m][0], ev[m][1]));
                if (__builtin_amdgcn_ballot_w64(mx == ~0u) == 0) break;
                __builtin_amdgcn_s_sleep(8);
            }
            STAMP(39);
            cf part = {__uint_as_float(ev[7][0]), __uint_as_float(ev[7][1])};
            UNROLL for (int m = 6; m >= 0; --m) part = cfma(lam64, part, (cf){__uint_as_float(ev[m][0]), __uint_as_float(ev[m][1])});
            part = cmul(zG, part);
            if (ebase < 0) part = (cf){0.f, 0.f};
            part.r = sum4(part.r); part.i = sum4(part.i);
            const cf ilo = cfma(wlo, part, pg), ihi = cfma(whi, part, ph);
            hr = (f32x2){ilo.r, ihi.r}; hm = (f32x2){ilo.i, ihi.i};
            u32x4 pr0, pr1, pi0, pi1;
            UNROLL for (int p = 0; p < 8; ++p) {
                f32x2 nr, ni; pk_cfma(nr, ni, lam, hr, hm, PAIR(ur, p), PAIR(ui, p)); hr = nr; hm = ni;
                unsigned cr, ci; asm("v_cvt_pk_bf16_f32 %0, %2, %3\n\tv_cvt_pk_bf16_f32 %1, -%4, -%5" : "=&v"(cr), "=&v"(ci) : "v"(hr[0]), "v"(hr[1]), "v"(hm[0]), "v"(hm[1]));
                if (p < 4) { pr0[p & 3] = cr; pi0[p & 3] = ci; } else { pr1[p & 3] = cr; pi1[p & 3] = ci; }
            }
            const unsigned sb = L_HS + u * 4096, wa = sb + r16 * 128 + (((2 * G) ^ (r16 & 7)) << 4);
            const int tok = (lane & 48) + (r16 >> 1) + 8 * (lane & 1);
            const unsigned gha = (tok >> 5) * 57344 + (tok & 31) * 16, ghb = u < 3 ? gha + 1024 : OOB, gs = WS_H + blk * 114688 + (7 * j + 2 * u) * 1024;
            const unsigned rao = sb + tq * 128 + (((2 * G + (tp >> 1)) ^ tq) << 4) + 8 * (tp & 1);
            const lp wa0 = lbase(L, wa), wa1 = lbase(L, wa ^ 16), ra = lbase(L, rao), rb = lbase(L, (rao ^ 64) + 512);
            *(LAS u32x4*)(wa0) = pr0; *(LAS u32x4*)(wa1) = pr1; *(LAS u32x4*)(wa0 + 2048) = pi0; *(LAS u32x4*)(wa1 + 2048) = pi1;
            asm volatile("" ::: "memory");
            UNROLL for (int pl = 0; pl < 2; ++pl) {
                const bf16x8 c0 = ldtr2(ra + pl * 2048, rb + pl * 2048), c1 = ldtr2(ra + pl * 2048 + 1024, rb + pl * 2048 + 1024);
                __builtin_amdgcn_raw_buffer_store_b128(__builtin_bit_cast(u32x4, c0), rsWS, gha + pl * 512, gs, SC1);
                __builtin_amdgcn_raw_buffer_store_b128(__builtin_bit_cast(u32x4, c1), rsWS, ghb + pl * 512, gs, SC1);
            }
        }
        STAMP(5);
        const unsigned cx = (32 * (G & 1) + 8 * tp) ^ (((tq >> 1) & 1) << 6), nb = L_WO + (112 * u + tq) * 128, wa_ = nb + hi * 512;
        const lp wa0_ = lbase(L, wa_ + cx), wa1_ = lbase(L, wa_ + (cx ^ 64)), nb0 = lbase(L, nb + cx), nb1 = lbase(L, nb + (cx ^ 64));
        lwait(L, L_WF, 4u);
        bf16x8 wf0[8], wf1[8];
        UNROLL for (int k = 0; k < 8; ++k) { const unsigned ro = (28 * (k >> 1) + 16 * (k & 1)) * 128;
            wf0[k] = ldtr2(wa0_ + ro, ((k & 1) ? nb0 : wa0_) + ro + 1024); wf1[k] = ldtr2(wa1_ + ro, ((k & 1) ? nb1 : wa1_) + ro + 1024); }
        lcount(L, L_WF, lane);
        wait_vm<0>();
        atomic_inc(rsWS, hoff);
        lwait(L, L_WF, 8u);
        {   const rsrc_t rsCR = mkrs(cre, DM * ROWB), rsCI = mkrs(cim, DM * ROWB);
            const int c2 = min(u + 4, 6);
            const unsigned vc = (56 * j + 8 * u + r8) * ROWB + ((sl ^ ((4 * u + (r8 >> 1)) & 7)) << 4), vc2 = (56 * j + 8 * c2 + r8) * ROWB + ((sl ^ ((4 * c2 + (r8 >> 1)) & 7)) << 4);
            UNROLL for (int cc = 0; cc < 7; ++cc) {
                dma(rsCR, vc, cc * 128, l0 + L_C + cc * 7168 + u * 1024); PACE(); dma(rsCI, vc, cc * 128, l0 + L_CIM + cc * 7168 + u * 1024); PACE();
                dma(rsCR, vc2, cc * 128, l0 + L_C + cc * 7168 + c2 * 1024); PACE(); dma(rsCI, vc2, cc * 128, l0 + L_CIM + cc * 7168 + c2 * 1024); PACE(); } }
        STAMP(40);
        _Pragma("unroll 1") for (unsigned spin = 0; __builtin_amdgcn_readfirstlane(atomic_read(rsWS, hoff)) != 32 && spin < SPIN_CAP; ++spin) __builtin_amdgcn_s_sleep(8);
        const unsigned hb = WS_H + blk * 114688 + u * 1024;
        u32x4 h0[14], h1[14];
        UNROLL for (int m = 0; m < 14; ++m) { h0[m] = __builtin_amdgcn_raw_buffer_load_b128(rsWS, l16, hb + th * 57344 + m * 4096, SC1); PACE(); }
        UNROLL for (int m = 0; m < 14; ++m) { h1[m] = __builtin_amdgcn_raw_buffer_load_b128(rsWS, l16, hb + (th ^ 1) * 57344 + m * 4096, SC1); PACE(); }
        const int rbc = min(32 + r32, 55);
        const unsigned cao = hi * L_CIM + r32 * 128 + ((u ^ ((r32 >> 1) & 7)) << 4), cbo = hi * L_CIM + rbc * 128 + ((u ^ ((rbc >> 1) & 7)) << 4);
        const lp ca0 = lbase(L, cao), ca1 = lbase(L, cao ^ 64), cb0 = lbase(L, cbo), cb1 = lbase(L, cbo ^ 64);
        const lp rp = lbase(L, L_RED + th * 16384 + u * 4096 + l16), rq = lbase(L, L_RED + (th ^ 1) * 16384 + u * 4096 + l16);
        const unsigned oa = WS_O + blk * 65536 + (th * 16 + 4 * u) * 2048, ob = WS_O + blk * 65536 + ((th ^ 1) * 16 + 4 * u) * 2048;
        f32x2 s2 = {2.0f * (1.0f - gt), 0.f}, s3 = {2.0f * (1.0f - go), 0.f}; PIN(s2); PIN(s3);
        UNROLL for (int m = 0; m < 14; ++m) { PIN(h0[m]); PIN(h1[m]); }
        STAMP(41);
        lcount(L, L_WF, lane); lwait(L, L_WF, 12u);
        __builtin_amdgcn_s_setprio(0);
        lwait_idle(L, L_QE, 4u);
        __builtin_amdgcn_s_sleep(YSLEEP);
        STAMP(15);
        f32x16 y[4]; y[0] = y[1] = y[2] = y[3] = (f32x16){};
        u32x4 of[16];
#define OLD(k0) UNROLL for (int k = (k0); k < (k0) + 8; ++k) of[k] = __builtin_amdgcn_raw_buffer_load_b128(rsWS, l16 + (k & 3) * 1024, (k < 8 ? oa : ob) + ((k >> 2) & 1) * 4096, SC1)
        {   bf16x8 fa[28], fc[28];
            UNROLL for (int m = -YD; m < 28; ++m) {
                if (m + YD < 28) { const int n = (m + YD) % 14; fa[m + YD] = ld128(((n & 1) ? ca1 : ca0) + (n >> 1) * 7168); fc[m + YD] = ld128(((n & 1) ? cb1 : cb0) + (n >> 1) * 7168); }
                if (m >= 0 && m < 14) { y[0] = MFMA32(fa[m], asbf(h0[m]), y[0]); y[1] = MFMA32(fc[m], asbf(h0[m]), y[1]); }
                if (m >= 14) { y[2] = MFMA32(fa[m], asbf(h1[m - 14]), y[2]); y[3] = MFMA32(fc[m], asbf(h1[m - 14]), y[3]); }
                if (m >= 16 && m < 24) { UNROLL for (int k = 0; k < 2; ++k) { const f32x2 a = pk_scale(s2, (f32x2){y[k][2 * (m - 16)], y[k][2 * (m - 16) + 1]}); y[k][2 * (m - 16)] = a[0]; y[k][2 * (m - 16) + 1] = a[1]; } PIN(y[0]); PIN(y[1]); }
                if (m == 26 && lane == 0) __hip_atomic_fetch_add((LAS unsigned*)(L + L_YF), 1u, __ATOMIC_RELAXED, __HIP_MEMORY_SCOPE_WORKGROUP);
                SBAR();
            } }
        STAMP(9);
        lwait(L, L_GV, 4u);
        __builtin_amdgcn_s_sleep(OSLEEP);
        OLD(0); OLD(8);
        SBAR();
        lwait(L, L_YF, 4u);
        asm volatile("s_nop 11" : "+v"(y[2]), "+v"(y[3]));
        UNROLL for (int k = 2; k < 4; ++k) UNROLL for (int e = 0; e < 8; ++e) { const f32x2 a = pk_scale(s3, (f32x2){y[k][2 * e], y[k][2 * e + 1]}); y[k][2 * e] = a[0]; y[k][2 * e + 1] = a[1]; }
        asm volatile("s_nop 1" : "+v"(y[2]), "+v"(y[3]));
        _Pragma("unroll 1") for (unsigned spin = 0; spin < SPIN_CAP; ++spin) {
            unsigned mx = 0;
            UNROLL for (int k = 0; k < 16; ++k) mx = max(max(mx, max(of[k][0], of[k][1])), max(of[k][2], of[k][3]));
            if (__builtin_amdgcn_ballot_w64(mx == ~0u) == 0) break;
            asm volatile("" ::: "memory");
            OLD(0); OLD(8);
        }
        STAMP(10);
#define REDW(p, Y, e) *(LAS f32x4*)((p) + (e) * 1024) = (f32x4){Y[4 * (e)], Y[4 * (e) + 1], Y[4 * (e) + 2], Y[4 * (e) + 3]}
        UNROLL for (int k = 0; k < 8; ++k) y[0] = MFMA32(wf0[k], asbf(of[k]), y[0]);
        PIN(y[0]); SBAR();
        UNROLL for (int k = 0; k < 8; ++k) { y[1] = MFMA32(wf1[k], asbf(of[k]), y[1]); SBAR(); if (k >= 3 && k < 7) REDW(rp, y[0], k - 3); SBAR(); }
        UNROLL for (int k = 0; k < 8; ++k) { y[2] = MFMA32(wf0[k], asbf(of[8 + k]), y[2]); SBAR(); if (k >= 3 && k < 7) REDW(rp + 32768, y[1], k - 3); SBAR(); }
        UNROLL for (int k = 0; k < 8; ++k) { y[3] = MFMA32(wf1[k], asbf(of[8 + k]), y[3]); SBAR(); if (k >= 3 && k < 7) REDW(rq, y[2], k - 3); SBAR(); }
        UNROLL for (int e = 0; e < 4; ++e) REDW(rq + 32768, y[3], e);
        lds_bar();
    }
    STAMP(11);
}

extern "C" void kernel_launch(void* const* d_in, const int* in_sizes, int n_in, void* d_out, int out_size, void* d_ws, size_t ws_size,
                              hipStream_t stream) {
    const uint16_t* const* in = (const uint16_t* const*)d_in;
    gated_mix_kernel<<<256, 512, 0, stream>>>(in[0], in[1], in[2], in[3], in[4], in[6], in[7], in[8], in[9], in[10], in[11], in[12], in[13], in[14],
                                              (float*)d_out, (char*)d_ws);
}
```

```cpp
#include <hip/hip_runtime.h>
#include <hip/hip_bf16.h>
#include <stdint.h>

typedef __bf16 bf16x8 __attribute__((ext_vector_type(8)));
typedef __bf16 bf16x2 __attribute__((ext_vector_type(2)));
typedef float f32x16 __attribute__((ext_vector_type(16)));
typedef float f32x4 __attribute__((ext_vector_type(4)));
typedef float f32x2 __attribute__((ext_vector_type(2)));
typedef unsigned u32x4 __attribute__((ext_vector_type(4)));
typedef unsigned u32x2 __attribute__((ext_vector_type(2)));
typedef short s16x4 __attribute__((ext_vector_type(4)));
typedef __amdgpu_buffer_rsrc_t rsrc_t;
#define LAS __attribute__((address_space(3)))
#define DI __device__ __forceinline__
#define SBAR() __builtin_amdgcn_sched_barrier(0)
#define PIN(x) asm volatile("" : "+v"(x))
#define PINS(x) asm volatile("" : "+s"(x))
#define MFMA32(a, b, c) __builtin_amdgcn_mfma_f32_32x32x16_bf16((a), (b), (c), 0, 0, 0)
#define MFMA16(a, b, c) __builtin_amdgcn_mfma_f32_16x16x32_bf16((a), (b), (c), 0, 0, 0)
#define UNROLL _Pragma("unroll")
#define PACE() asm volatile("s_sleep %0" :: "i"(CSLEEP) : "memory")
#define SC1 16

#define CSLEEP 2
#define YSLEEP 8
#define CPREF 32768
#define WQ2 8
#define YD 3
#define OSLEEP 0
#define PACKED 1
#define ODN 4u
#define PFC 0
#define QREP 2
#define KPOLL 20
#define UPRIO 1
#define SPRIO 0
#define OM0 16
#define OM1 22
#define MAP42 0

constexpr int SEQ = 2048, DM = 448, ROWB = DM * 2;
constexpr unsigned WS_KVCNT = 0;
constexpr unsigned WS_HCNT = 16384;
constexpr unsigned WS_X = 20480;
constexpr unsigned OOB = 0x7FFFFFF0u;
constexpr unsigned WS_KV = 65536;
constexpr unsigned WS_E = WS_KV + 262144;
constexpr unsigned WS_H = WS_E + 131072;
constexpr unsigned WS_O = WS_H + 32 * 114688;
constexpr unsigned WS_END = WS_O + 32 * 65536;
constexpr unsigned WS_STAMP = WS_END;

constexpr unsigned L_X = 0, L_WK = 57344, L_WQ = 86016, L_B0 = 143360, L_G = 160768;
constexpr unsigned L_KD = 161024, L_XA = 161028, L_XF = 161060, L_QD = 161032, L_QF = 161036, L_UF = 161040, L_WF = 161044, L_KR = 161048, L_YF = 161052, L_XG = 161064, L_GV = 161068, L_QE = 161072, L_SF = 161076, L_QL = 161080, L_OD = 161056, L_FB = 161084;
constexpr unsigned L_QSH = 122880, L_HS = 106496;
constexpr unsigned L_C = 0, L_CIM = 50176, L_WO = 100352;
constexpr unsigned L_RO = 131072, L_LS = 155648;
constexpr unsigned L_RED = 0, L_DUMMY = 157696;
constexpr unsigned LDS_BYTES = 163840;
constexpr unsigned SPIN_CAP = 1u << 14;

DI unsigned cvt2(float a, float b) { f32x2 v = {a, b}; return __builtin_bit_cast(unsigned, __builtin_convertvector(v, bf16x2)); }
DI float bf2f(unsigned h) { return __uint_as_float(h << 16); }
DI float bfhi(unsigned h) { return __uint_as_float(h & 0xffff0000u); }
DI float bfround(float x) { return __uint_as_float(cvt2(x, 0.f) << 16); }
DI float fexp(float x) { return __builtin_amdgcn_exp2f(x * 1.4426950408889634f); }
DI rsrc_t mkrs(const void* p, unsigned bytes) { return __builtin_amdgcn_make_buffer_rsrc((void*)p, (short)0, (int)bytes, 0x00020000); }
DI void dma(rsrc_t rs, unsigned voff, unsigned soff, unsigned lds) {
    asm volatile("s_mov_b32 m0, %0\n\ts_nop 0\n\tbuffer_load_dwordx4 %1, %2, %3 offen lds" :: "s"(lds), "v"(voff), "s"(rs), "s"(soff) : "memory"); }
template <int N> DI void wait_bar() { asm volatile("s_waitcnt vmcnt(%0) lgkmcnt(0)\n\ts_barrier" :: "i"(N) : "memory"); }
template <int N> DI void wait_vm() { asm volatile("s_waitcnt vmcnt(%0)" :: "i"(N) : "memory"); }
DI void lds_bar() { asm volatile("s_waitcnt lgkmcnt(0)\n\ts_barrier" ::: "memory"); }
typedef LAS char* lp;
DI lp lbase(LAS char* L, unsigned off) { lp p = L + off; PIN(p); return p; }
DI bf16x8 ld128(lp p) { return __builtin_bit_cast(bf16x8, *(LAS const u32x4*)p); }
DI bf16x8 ldtr2(lp p0, lp p1) {
    s16x4 a = __builtin_amdgcn_ds_read_tr16_b64_v4i16((LAS s16x4*)p0), b = __builtin_amdgcn_ds_read_tr16_b64_v4i16((LAS s16x4*)p1);
    return __builtin_bit_cast(bf16x8, __builtin_shufflevector(a, b, 0, 1, 2, 3, 4, 5, 6, 7)); }
DI bf16x8 asbf(u32x4 v) { return __builtin_bit_cast(bf16x8, v); }
DI void st128(u32x4 v, rsrc_t rs, unsigned voff, unsigned soff, int aux) {
    if (aux) __builtin_amdgcn_raw_buffer_store_b128(v, rs, voff, soff, SC1); else __builtin_amdgcn_raw_buffer_store_b128(v, rs, voff, soff, 0);
    asm volatile("s_nop 1" :: "v"(v)); }
DI unsigned nosent(unsigned v) { return v < 0xFFFFFFFEu ? v : 0xFFFFFFFEu; }
DI unsigned atomic_read(rsrc_t rs, unsigned voff) { return (unsigned)__builtin_amdgcn_raw_ptr_buffer_atomic_add_i32(0, rs, (int)voff, 0, SC1); }
DI void atomic_inc(rsrc_t rs, unsigned voff) { asm volatile("buffer_atomic_add %0, %1, %2, 0 offen sc1" :: "v"(1u), "v"(voff), "s"(rs) : "memory"); }
struct cf { float r, i; };
DI cf cmul(cf a, cf b) { cf o;
    asm("v_mul_f32 %0, %3, %5\n\tv_mul_f32 %1, %3, %4\n\tv_fma_f32 %0, %2, %4, -%0\n\tv_fma_f32 %1, %2, %5, %1" : "=&v"(o.r), "=&v"(o.i) : "v"(a.r), "v"(a.i), "v"(b.r), "v"(b.i)); return o; }
DI cf cfma(cf a, cf b, cf c) { cf o;
    asm("v_fma_f32 %0, -%3, %5, %6\n\tv_fma_f32 %1, %3, %4, %7\n\tv_fma_f32 %0, %2, %4, %0\n\tv_fma_f32 %1, %2, %5, %1"
        : "=&v"(o.r), "=&v"(o.i) : "v"(a.r), "v"(a.i), "v"(b.r), "v"(b.i), "v"(c.r), "v"(c.i)); return o; }
DI void pk_cfma(f32x2& nr, f32x2& ni, f32x2 l, f32x2 hr, f32x2 hm, f32x2 a, f32x2 c) {
    asm("v_pk_fma_f32 %0, %2, %3, %5 op_sel_hi:[0,1,1]\n\tv_pk_fma_f32 %1, %2, %4, %6 op_sel_hi:[0,1,1]\n\t"
        "v_pk_fma_f32 %0, %2, %4, %0 op_sel:[1,0,0] neg_lo:[1,0,0] neg_hi:[1,0,0]\n\tv_pk_fma_f32 %1, %2, %3, %1 op_sel:[1,0,0]"
        : "=&v"(nr), "=&v"(ni) : "v"(l), "v"(hr), "v"(hm), "v"(a), "v"(c)); }
DI void pk_cmul(f32x2& nr, f32x2& ni, f32x2 l, f32x2 hr, f32x2 hm) {
    asm("v_pk_mul_f32 %0, %2, %3 op_sel_hi:[0,1]\n\tv_pk_mul_f32 %1, %2, %4 op_sel_hi:[0,1]\n\t"
        "v_pk_fma_f32 %0, %2, %4, %0 op_sel:[1,0,0] neg_lo:[1,0,0] neg_hi:[1,0,0]\n\tv_pk_fma_f32 %1, %2, %3, %1 op_sel:[1,0,0]"
        : "=&v"(nr), "=&v"(ni) : "v"(l), "v"(hr), "v"(hm)); }
DI f32x2 pk_scale(f32x2 s, f32x2 a) { f32x2 r; asm("v_pk_mul_f32 %0, %1, %2 op_sel_hi:[0,1]" : "=v"(r) : "v"(s), "v"(a)); return r; }
DI float sum4(float v) {
    auto a = __builtin_amdgcn_permlane16_swap(__float_as_uint(v), __float_as_uint(v), false, false); v = __uint_as_float(a[0]) + __uint_as_float(a[1]);
    auto c = __builtin_amdgcn_permlane32_swap(__float_as_uint(v), __float_as_uint(v), false, false); return __uint_as_float(c[0]) + __uint_as_float(c[1]); }
DI void bcast4(float v, float& r0, float& r1, float& r2, float& r3) {
    auto a = __builtin_amdgcn_permlane16_swap(__float_as_uint(v), __float_as_uint(v), false, false);
    auto e = __builtin_amdgcn_permlane32_swap(a[0], a[0], false, false); auto o = __builtin_amdgcn_permlane32_swap(a[1], a[1], false, false);
    r0 = __uint_as_float(e[0]); r2 = __uint_as_float(e[1]); r1 = __uint_as_float(o[0]); r3 = __uint_as_float(o[1]); }
DI float rowmax16(const f32x16& s) {
    float a = fmaxf(fmaxf(s[0], s[1]), s[2]), b = fmaxf(fmaxf(s[3], s[4]), s[5]);
    a = fmaxf(fmaxf(a, s[6]), s[7]); b = fmaxf(fmaxf(b, s[8]), s[9]); a = fmaxf(fmaxf(a, s[10]), s[11]); b = fmaxf(fmaxf(b, s[12]), s[13]);
    a = fmaxf(fmaxf(a, s[14]), s[15]); a = fmaxf(a, b);
    auto rr = __builtin_amdgcn_permlane32_swap(__float_as_uint(a), __float_as_uint(a), false, false);
    return fmaxf(__uint_as_float(rr[0]), __uint_as_float(rr[1])); }
DI void set_shift(u32x4& q1, float c, int hi) {
    const float c1 = bfround(c), c2 = bfround(c - c1), c3 = c - c1 - c2;
    if (hi) { q1[2] = cvt2(-c1, -c2); q1[3] = cvt2(-c3, 0.f); } }
DI void ladd(LAS char* L, unsigned off, unsigned v) {
    asm volatile("s_mov_b64 exec, 1\n\tds_add_u32 %0, %1\n\ts_mov_b64 exec, -1" :: "v"(L + off), "v"(v) : "memory"); }
DI void lcount(LAS char* L, unsigned off, int) { ladd(L, off, 1u); }
DI unsigned lread(LAS char* L, unsigned off) { return __builtin_amdgcn_readfirstlane(*(volatile LAS unsigned*)(L + off)); }
DI void lwait(LAS char* L, unsigned off, unsigned n) {
    if (__builtin_expect(lread(L, off) < n, 0)) { _Pragma("unroll 1") for (unsigned spin = 0; lread(L, off) < n && spin < (1u << 20); ++spin) { } }
    asm volatile("" ::: "memory"); }
DI void lwait_idle(LAS char* L, unsigned off, unsigned n) {
    _Pragma("unroll 1") for (unsigned spin = 0; lread(L, off) < n && spin < (1u << 18); ++spin) __builtin_amdgcn_s_sleep(2);
    asm volatile("" ::: "memory"); }
DI int xswz(int row) { return ((row >> 1) & 7) ^ ((row & 16) ? 7 : 0) ^ ((row & 32) ? 6 : 0); }
DI u32x4 pack8(const f32x16& a, int e) { return (u32x4){cvt2(a[e], a[e + 1]), cvt2(a[e + 2], a[e + 3]), cvt2(a[e + 4], a[e + 5]), cvt2(a[e + 6], a[e + 7])}; }
#define PAIR(v, p) (f32x2){v[(p) >> 1][2 * ((p) & 1)], v[(p) >> 1][2 * ((p) & 1) + 1]}

#define STAMP(k)
#define STAMPV(k)

__global__ void __launch_bounds__(512, 2)
gated_mix_kernel(const uint16_t* x, const uint16_t* wq, const uint16_t* wk, const uint16_t* wv, const uint16_t* wo, const uint16_t* lre, const uint16_t* lim,
                 const uint16_t* bre, const uint16_t* bim, const uint16_t* cre, const uint16_t* cim, const uint16_t* logdt, const uint16_t* dskip,
                 const uint16_t* gate, float* out, char* ws) {
    __shared__ __attribute__((aligned(16))) char smem[LDS_BYTES];
    LAS char* L = (LAS char*)smem;
    const unsigned l0 = (unsigned)(uintptr_t)L;
    const int lane = threadIdx.x & 63, w = __builtin_amdgcn_readfirstlane(threadIdx.x >> 6);
    const int b = blockIdx.x;
    STAMP(0);
    {   unsigned long long pc; asm volatile("s_getpc_b64 %0" : "=s"(pc));
        const rsrc_t rsC = mkrs((const void*)(pc & ~255ull), CPREF);
        asm volatile("s_mov_b32 m0, %0\n\ts_nop 4\n\tbuffer_load_dword %1, %2, 0 offen lds" :: "s"(l0 + L_DUMMY), "v"(threadIdx.x * 64), "s"(rsC) : "memory"); }
    const int blk = (b & 7) * 4 + (b >> 6), j = (b >> 3) & 7;
    const int th = w & 1, hd = (w >> 1) & 1;
    const int r32 = lane & 31, hi = lane >> 5, r16 = lane & 15, G = lane >> 4;
    const int tq = r16 >> 2, tp = lane & 3;
    const int sl = lane & 7, r8 = lane >> 3;
    const unsigned l16 = lane * 16;
    const int kind = j & 1, tg = j >> 1;
    unsigned vx = r8 * ROWB + ((sl ^ (r8 >> 1)) << 4); PIN(vx);
    PINS(x); PINS(wq); PINS(wk); PINS(wv); PINS(wo); PINS(lre); PINS(lim); PINS(bre); PINS(bim); PINS(cre); PINS(cim); PINS(logdt); PINS(dskip); PINS(gate); PINS(out); PINS(ws);
    const rsrc_t rsX = mkrs(x, SEQ * ROWB), rsWS = mkrs(ws, WS_END);
    const unsigned opiece = WS_O + blk * 65536 + (th * 16 + 2 * j + hd) * 2048;

    const rsrc_t rsWQ = mkrs(wq, DM * ROWB);
    const int pwq = sl ^ (((r8 >> 1) & 1) << 2);
    const unsigned vw = r8 * ROWB + 112 * j + (pwq << 4);
#define WQD(i0, i1) do { if (pwq != 7) { UNROLL for (int i = (i0); i < (i1); ++i) { const int p = i < 18 ? 3 * i + w - 1 : min(53 + w, 55); dma(rsWQ, vw, p * 8 * ROWB, l0 + L_WQ + p * 1024); } } } while (0)
#define XREST() UNROLL for (int n = 0; n < 6; ++n) { const int g = n + (n >= 2 * tg ? 2 : 0); \
        dma(rsX, vx ^ (xswz(8 * g) << 4), (blk * 64 + 8 * g) * ROWB + (w - 1) * 128, l0 + L_X + (w - 1) * 8192 + g * 1024); }
    if (w < 4) {
        const bool master = b == 0 && w == 0;
        const unsigned bump = (master && lane < 16) ? WS_X + lane * 128 : OOB;
        if (w == 0) {
            const unsigned xoff = lane == 0 ? WS_X + (b & 15) * 128 : OOB;
            const rsrc_t rsWK = mkrs(kind ? wv : wk, DM * 56);
            const unsigned vxa = vx ^ (xswz(16 * tg) << 4), vxb = (vx ^ (xswz(16 * tg + 8) << 4)) + 8 * ROWB, xs = (blk * 64 + tg * 16) * ROWB;
            STAMP(36);
            UNROLL for (int c = 0; c < 7; ++c) {
                dma(rsX, vxa, xs + c * 128, l0 + L_X + c * 8192 + tg * 2048); dma(rsX, vxb, xs + c * 128, l0 + L_X + c * 8192 + tg * 2048 + 1024);
                UNROLL for (int n = (7 * c + 1) / 2; n < (7 * c + 8) / 2; ++n) dma(rsWK, l16, n * 1024, l0 + L_WK + n * 1024);
            }
            asm volatile("s_barrier" ::: "memory");
            STAMP(14);
            unsigned x0 = 0u, x1 = 0u;
            asm volatile("buffer_atomic_add %0, %1, %2, 0 offen sc0 sc1" : "+v"(x0) : "v"(xoff), "s"(rsWS) : "memory");
            __builtin_amdgcn_raw_buffer_store_b64((u32x2){~0u, ~0u}, rsWS, lane < 56 ? lane * 8 : OOB, WS_E + (blk * DM + 56 * j) * 8, SC1);
            if (master) {
                __builtin_amdgcn_raw_buffer_store_b32(0u, rsWS, lane * 128, 0, SC1);
                __builtin_amdgcn_raw_buffer_store_b32(0u, rsWS, lane * 128 + 8192, 0, SC1);
                __builtin_amdgcn_raw_buffer_store_b32(0u, rsWS, lane < 32 ? lane * 128 + 16384 : OOB, 0, SC1);
                wait_vm<0>();
                atomic_inc(rsWS, bump);
            }
            const int cg0 = kind ? tp : 4 * (tp >> 1) + (tp & 1), cg1 = kind ? 4 + tp : 4 * (tp >> 1) + 2 + (tp & 1);
            const lp aw0 = lbase(L, L_WK + (8 * G + tq) * 56 + cg0 * 8), aw1 = lbase(L, L_WK + (8 * G + tq) * 56 + cg1 * 8);
            const unsigned axo = L_X + (16 * tg + r16) * 128 + ((G ^ xswz(16 * tg + r16)) << 4);
            const lp ax0 = lbase(L, axo), ax1 = lbase(L, axo ^ 64);
            const int sg = tg & 1;
            const unsigned tb = WS_KV + (2 * blk + (tg >> 1)) * 4096 + (kind ? 2048 + sg * 1024 : 0);
            unsigned kva = kind ? ((G >> 1) * 32 + r16) * 16 + (G & 1) * 8 : (G * 32 + 16 * sg + ((r16 & 3) | ((r16 & 4) << 1) | ((r16 & 8) >> 1))) * 16; PIN(kva);
            unsigned kvb = kind ? kva + 256 : kva + 8; PIN(kvb);
            const bool spare = kind ? r16 >= 12 : G == 3;
            unsigned sp2 = (kind && r16 != 12) ? 0u : 0x3F803F80u, sp3 = kind ? sp2 : 0x00003F80u; PIN(sp2); PIN(sp3);
            f32x4 d0 = {0.f, 0.f, 0.f, 0.f}, d1 = d0;
            bf16x8 xf[14], w0[14], w1[14];
#define KVR(c) do { wait_vm<2 + 39 - 2 * ((c) + 1) - (7 * (c) + 8) / 2>(); UNROLL for (int ks = 2 * (c); ks < 2 * (c) + 2; ++ks) { xf[ks] = ld128(((ks & 1) ? ax1 : ax0) + (c) * 8192); \
                        w0[ks] = ldtr2(aw0 + ks * 1792, aw0 + ks * 1792 + 224); w1[ks] = ldtr2(aw1 + ks * 1792, aw1 + ks * 1792 + 224); } SBAR(); } while (0)
#define KVM(c, K) do { UNROLL for (int ks = 2 * (c); ks < 2 * (c) + 2; ++ks) { \
                        if (K) { d0 = MFMA16(xf[ks], w0[ks], d0); d1 = MFMA16(xf[ks], w1[ks], d1); } else { d0 = MFMA16(w0[ks], xf[ks], d0); d1 = MFMA16(w1[ks], xf[ks], d1); } } SBAR(); } while (0)
#define KVP(K) do { KVR(0); STAMP(1); KVR(1); KVM(0, K); KVR(2); KVM(1, K); KVR(3); KVM(2, K); KVR(4); KVM(3, K); KVR(5); KVM(4, K); KVR(6); STAMP(32); KVM(5, K); KVM(6, K); } while (0)
            if (kind) KVP(1); else KVP(0);
            __builtin_amdgcn_raw_buffer_store_b64((u32x2){cvt2(d0[0], d0[1]), cvt2(d0[2], d0[3])}, rsWS, kva, tb, SC1);
            __builtin_amdgcn_raw_buffer_store_b64((u32x2){spare ? sp2 : cvt2(d1[0], d1[1]), spare ? sp3 : cvt2(d1[2], d1[3])}, rsWS, kvb, tb, SC1);
            asm volatile("buffer_atomic_add %0, %1, %2, 0 offen sc0 sc1" : "+v"(x1) : "v"(xoff), "s"(rsWS) : "memory");
            ladd(L, L_KD, 1u); ladd(L, L_XA, 1u);
            STAMP(12);
            asm volatile("s_waitcnt vmcnt(0)" : "+v"(x0), "+v"(x1) :: "memory");
            STAMP(35);
            const unsigned s0 = __builtin_amdgcn_readfirstlane(x0); unsigned s1 = __builtin_amdgcn_readfirstlane(x1);
            _Pragma("unroll 1") for (unsigned spin = 0; !master && s1 == s0 && spin < SPIN_CAP; ++spin) s1 = __builtin_amdgcn_readfirstlane(atomic_read(rsWS, xoff));
            atomic_inc(rsWS, lane < 8 ? WS_KVCNT + lane * 2048 + (b & 15) * 128 : OOB);
            atomic_inc(rsWS, bump);
            STAMP(13);
        } else {
            unsigned g0, g1, g2;
            const int gk = min((w - 1) * 192, 256) + lane;
            const uint16_t* gp = gate + gk; PIN(gp);
            if (w == 1 && lane < 16) *(LAS unsigned*)(L + L_KD + lane * 4) = 0u;
            lds_bar();
            XREST();
            asm volatile("global_load_ushort %0, %3, off\n\tglobal_load_ushort %1, %3, off offset:128\n\tglobal_load_ushort %2, %3, off offset:256"
                         : "=&v"(g0), "=&v"(g1), "=&v"(g2) : "v"(gp) : "memory");
            WQD(0, 6);
            wait_vm<9>();
            lcount(L, L_XA, lane);
            WQD(6, 19);
            STAMP(14);
            asm volatile("s_waitcnt vmcnt(9)" : "+v"(g0), "+v"(g1), "+v"(g2) :: "memory");
            const lp gl = L + (L_WQ + gk * 128 + (112 ^ (((gk >> 1) & 1) << 6)));
            *(LAS uint16_t*)(gl) = (uint16_t)g0; *(LAS uint16_t*)(gl + 8192) = (uint16_t)g1; *(LAS uint16_t*)(gl + 16384) = (uint16_t)g2;
            lcount(L, L_XF, lane);
            STAMP(1);
        }
        const int qrow = 32 * th + r32;
        const unsigned a0o = L_X + qrow * 128 + ((hi ^ xswz(qrow)) << 4);
        const lp a00 = lbase(L, a0o), a01 = lbase(L, a0o ^ 32), a02 = lbase(L, a0o ^ 64), a03 = lbase(L, a0o ^ 96);
        const lp a1 = lbase(L, L_WQ + (8 * hi + tq) * 128 + ((56 * hd + 32 * (G & 1) + 8 * tp) ^ (((tq >> 1) & 1) << 6)));
        unsigned kco = (w == 0 && lane < 16) ? WS_KVCNT + (b & 7) * 2048 + lane * 128 : OOB, kc = 0u; PIN(kco);
        f32x16 o[4];
        o[0] = o[1] = o[2] = o[3] = (f32x16){}; PIN(o[0]); PIN(o[1]); PIN(o[2]); PIN(o[3]);
        lwait(L, L_XA, 8u); lwait(L, L_XF, 3u);
        STAMP(2);
        f32x16 qa = {};
        { bf16x8 wf[28], xq[28];
          UNROLL for (int ks = 0; ks < 33; ++ks) {
            if (ks == WQ2) { wait_vm<0>(); lcount(L, L_XG, lane); }
            if (ks == 15) lwait(L, L_XG, 4u);
            if (ks < 28) { wf[ks] = ldtr2(a1 + ks * 2048, a1 + ks * 2048 + 512); xq[ks] = ld128(((ks & 3) == 0 ? a00 : (ks & 3) == 1 ? a01 : (ks & 3) == 2 ? a02 : a03) + (ks >> 2) * 8192); }
            if (ks >= 5) qa = MFMA32(wf[ks - 5], xq[ks - 5], qa);
            if (ks == KPOLL) asm volatile("buffer_atomic_add %0, %1, %2, 0 offen sc0 sc1" : "+v"(kc) : "v"(kco), "s"(rsWS) : "memory");
            if (ks == 28) ladd(L, L_QD, 1u);
            SBAR(); } }
        u32x4 kf[4][2], vf[4][2], pw0, pw1;
        const unsigned kv0 = WS_KV + w * 65536;
        unsigned kvs = kv0;
#define LD2(A0, A1, off) asm volatile("buffer_load_dwordx4 %0, %2, %3, %4 offen offset:%5 sc1\n\tbuffer_load_dwordx4 %1, %2, %3, %4 offen offset:%6 sc1" \
                                      : "=&v"(A0), "=&v"(A1) : "v"(l16), "s"(rsWS), "s"(kvs), "i"(off), "i"((off) + 1024) : "memory")
#define WAIT2(N, A, B) asm volatile("s_waitcnt vmcnt(%2)" : "+v"(A), "+v"(B) : "i"(N) : "memory")
        asm volatile("s_waitcnt vmcnt(0)" : "+v"(kc) :: "memory");
        const bool kvok = __builtin_amdgcn_ballot_w64(kc != 16 && lane < 16) == 0;
        if (w == 0 && kvok) lcount(L, L_KR, lane);
        lwait(L, L_QD, 4u);
        asm volatile("s_nop 11" : "+v"(qa));
        const f32x2 cs = {0.18898223650461361f * 1.4426950408889634f, 0.f};
        u32x4 q0[4], q1[4];
        UNROLL for (int e = 0; e < 4; ++e) { const f32x2 a = pk_scale(cs, (f32x2){qa[2 * e], qa[2 * e + 1]}), c = pk_scale(cs, (f32x2){qa[8 + 2 * e], qa[9 + 2 * e]});
            q0[3][e] = cvt2(a[0], a[1]); q1[3][e] = cvt2(c[0], c[1]); }
        if (hi) { q1[3][2] = 0u; q1[3][3] = 0u; }
        *(LAS u32x4*)(L + L_QSH + w * 2048 + l16) = q0[3];
        *(LAS u32x4*)(L + L_QSH + w * 2048 + 1024 + l16) = q1[3];
        if (hd == 1 && hi) *(LAS float*)(L + L_G + qrow * 4) = __builtin_amdgcn_rcpf(1.0f + fexp(-qa[12]));
        STAMP(3);
        lcount(L, L_QF, lane);
        if (w == 0 && !kvok) {
            _Pragma("unroll 1") for (unsigned spin = 0; spin < SPIN_CAP; ++spin) {
                atomic_inc(rsWS, bump); if (__builtin_amdgcn_ballot_w64(atomic_read(rsWS, kco) != 16 && lane < 16) == 0) break; }
            lcount(L, L_KR, lane);
        }
        lwait(L, L_KR, 1u);
        STAMP(4);

        f32x16 sA, sB;
        LD2(kf[0][0], kf[0][1], 0); LD2(vf[0][0], vf[0][1], 2048);
        lwait(L, L_QF, 4u);
        STAMP(5);
        UNROLL for (int q = 0; q < 3; ++q) { const lp a = lbase(L, L_QSH + (w ^ (q + 1)) * 2048 + l16); q0[q] = *(LAS const u32x4*)(a); q1[q] = *(LAS const u32x4*)(a + 1024); }
        const float gt = *(LAS const float*)(L + L_G + qrow * 4);
        ladd(L, L_SF, 1u);
        const unsigned rob = L_RO + w * 2048 + l16, lsb = L_LS + (w * 32 + r32) * 16;
        const lp ro0 = lbase(L, rob ^ 2048), ro1 = lbase(L, (rob ^ 4096) + 8192), ro2 = lbase(L, (rob ^ 6144) + 16384), rop = lbase(L, rob), lsp = lbase(L, lsb);
        const lp lw0 = lbase(L, hi ? (lsb ^ 512) + w * 4 : L_DUMMY), lw1 = lbase(L, hi ? (lsb ^ 1024) + w * 4 : L_DUMMY), lw2 = lbase(L, hi ? (lsb ^ 1536) + w * 4 : L_DUMMY), lw3 = lbase(L, hi ? lsb + w * 4 : L_DUMMY);
        u32x4 sel0, sel1;
        { const int js = 4 * ((r32 >> 3) & 1) + (r32 & 3); const unsigned one = ((r32 >> 2) & 1) == hi ? 0x3F80u << (16 * (js & 1)) : 0u;
          UNROLL for (int e = 0; e < 4; ++e) { const unsigned v = (js >> 1) == e ? one : 0u; sel0[e] = r32 < 16 ? v : 0u; sel1[e] = r32 < 16 ? 0u : v; } PIN(sel0); PIN(sel1); }
#define GIVE(O, RP, LP) do { *(LAS u32x4*)(RP) = pack8(O, 0); *(LAS u32x4*)((RP) + 1024) = pack8(O, 8); *(LAS float*)(LP) = O[12]; } while (0)
        pw1 = (u32x4){0u, 0u, 0u, 0u}; vf[3][1] = pw1; PIN(pw1); PIN(vf[3][1]);
        UNROLL for (int n = 1; n < 3; ++n) { kvs += 4096; LD2(kf[n][0], kf[n][1], 0); LD2(vf[n][0], vf[n][1], 2048); }
        WAIT2(10, kf[0][0], kf[0][1]);
        STAMP(6);
        sA = MFMA32(asbf(kf[0][0]), asbf(q0[0]), (f32x16){}); sA = MFMA32(asbf(kf[0][1]), asbf(q1[0]), sA);
#define EXP(A, i) A[i] = __builtin_amdgcn_exp2f(A[i])
#define EX4(A, i) do { EXP(A, i); EXP(A, i + 1); EXP(A, i + 2); EXP(A, i + 3); PIN(A); } while (0)
#define UNIT(A, B, KN0, KN1, qn, q, qp, V0, VP1, NEXT, H0, H1, H3) do { SBAR(); H0; \
        if (NEXT) { B = MFMA32(asbf(KN0), asbf(q0[qn]), (f32x16){}); PIN(B); } SBAR(); \
        EX4(A, 0); SBAR(); \
        o[qp] = MFMA32(asbf(VP1), asbf(pw1), o[qp]); PIN(o[qp]); SBAR(); H1; \
        pw0[0] = cvt2(A[0], A[1]); pw0[1] = cvt2(A[2], A[3]); PIN(pw0); SBAR(); EX4(A, 4); SBAR(); \
        if (NEXT) { B = MFMA32(asbf(KN1), asbf(q1[qn]), B); PIN(B); } SBAR(); \
        pw0[2] = cvt2(A[4], A[5]); pw0[3] = cvt2(A[6], A[7]); PIN(pw0); SBAR(); EX4(A, 8); SBAR(); H3; \
        o[q] = MFMA32(asbf(V0), asbf(pw0), o[q]); PIN(o[q]); SBAR(); \
        { float e12 = __builtin_amdgcn_exp2f(A[12]), e13 = __builtin_amdgcn_exp2f(A[13]), e14 = __builtin_amdgcn_exp2f(A[14]), e15 = __builtin_amdgcn_exp2f(A[15]); \
          asm volatile("" : "+v"(e12), "+v"(e13), "+v"(e14), "+v"(e15)); SBAR(); \
          pw1[0] = cvt2(A[8], A[9]); pw1[1] = cvt2(A[10], A[11]); SBAR(); pw1[2] = cvt2(e12, e13); pw1[3] = cvt2(e14, e15); PIN(pw1); } SBAR(); } while (0)
#define TRIP(c, n, p, LD, NV, NK, NEXT) do { \
        UNIT(sA, sB, kf[c][0], kf[c][1], 1, 0, 3, vf[c][0], vf[p][1], true, {}, { if (LD) { kvs += 4096; LD2(kf[p][0], kf[p][1], 0); } }, WAIT2(NV, vf[c][0], vf[c][1])); \
        UNIT(sB, sA, kf[c][0], kf[c][1], 2, 1, 0, vf[c][0], vf[c][1], true, {}, { if (LD) LD2(vf[p][0], vf[p][1], 2048); }, {}); \
        UNIT(sA, sB, kf[c][0], kf[c][1], 3, 2, 1, vf[c][0], vf[c][1], true, {}, {}, {}); \
        UNIT(sB, sA, kf[n][0], kf[n][1], 0, 3, 2, vf[c][0], vf[c][1], NEXT, { if (NEXT) WAIT2(NK, kf[n][0], kf[n][1]); }, {}, {}); } while (0)
        _Pragma("unroll 1") for (int it = 0; it < 3; ++it) {
            TRIP(0, 1, 3, true, 10, 10, true); TRIP(1, 2, 0, true, 10, 10, true); TRIP(2, 3, 1, true, 10, 10, true); TRIP(3, 0, 2, true, 10, 10, true);
            STAMPV(17 + it);
        }
        TRIP(0, 1, 3, true, 10, 10, true);
        ladd(L, L_QL, 1u);
        TRIP(1, 2, 0, false, 8, 6, true); TRIP(2, 3, 1, false, 4, 2, true);
        ladd(L, L_QE, 1u);
        lwait(L, L_UF, 8u);
        UNIT(sA, sB, kf[3][0], kf[3][1], 1, 0, 3, vf[3][0], vf[2][1], true, {}, {}, WAIT2(0, vf[3][0], vf[3][1]));
        UNIT(sB, sA, kf[3][0], kf[3][1], 2, 1, 0, vf[3][0], vf[3][1], true, {}, {}, {});
        UNIT(sA, sB, kf[3][0], kf[3][1], 3, 2, 1, vf[3][0], vf[3][1], true, {}, GIVE(o[0], ro0, lw0), {});
        UNIT(sB, sA, kf[0][0], kf[0][1], 0, 3, 2, vf[3][0], vf[3][1], false, {}, GIVE(o[1], ro1, lw1), {});
        STAMPV(20);
        o[3] = MFMA32(asbf(vf[3][1]), asbf(pw1), o[3]); PIN(o[3]); SBAR();
        GIVE(o[2], ro2, lw2); SBAR();
        STAMP(7);

        float l3 = o[3][12];
        *(LAS float*)(lw3) = l3;
        {   const unsigned u0 = __float_as_uint(o[0][12]), u1 = __float_as_uint(o[1][12]), u2 = __float_as_uint(o[2][12]), u3 = __float_as_uint(l3);
            const unsigned lo = min(min(u0, u1), min(u2, u3)), up = max(max(u0, u1), max(u2, u3));
            const bool bad = __builtin_amdgcn_ballot_w64(hi && (lo < 0x1F800000u || up > 0x5D800000u)) != 0;
            ladd(L, L_GV, bad ? 0x10001u : 1u); }
        unsigned gv = lread(L, L_GV);
        if (__builtin_expect((gv & 0xffffu) < 4u, 0)) { _Pragma("unroll 1") for (unsigned spin = 0; (gv & 0xffffu) < 4u && spin < (1u << 20); ++spin) gv = lread(L, L_GV); }
        asm volatile("" ::: "memory");
        STAMP(15);
        bf16x8 f[6]; f32x4 m0;
        UNROLL for (int t = 0; t < 6; ++t) f[t] = ld128(rop + (t >> 1) * 8192 + (t & 1) * 1024);
        m0 = *(LAS const f32x4*)(lsp);
        SBAR();
        if (__builtin_expect(gv > 0xffffu, 0)) {
            auto qbar = [&](unsigned n) __attribute__((always_inline)) { asm volatile("s_waitcnt lgkmcnt(0)" ::: "memory"); ladd(L, L_FB, 1u); lwait(L, L_FB, n); };
            float mx[4] = {-INFINITY, -INFINITY, -INFINITY, -INFINITY};
            qbar(4u);
            _Pragma("unroll 1") for (int t = 0; t < 16; ++t) {
                const bf16x8 a = asbf(__builtin_amdgcn_raw_buffer_load_b128(rsWS, l16, kv0 + t * 4096, SC1));
                const bf16x8 c = asbf(__builtin_amdgcn_raw_buffer_load_b128(rsWS, l16, kv0 + t * 4096 + 1024, SC1));
                UNROLL for (int q = 0; q < 4; ++q) { f32x16 s = MFMA32(a, asbf(q0[q]), (f32x16){}); s = MFMA32(c, asbf(q1[q]), s); mx[q] = fmaxf(mx[q], rowmax16(s)); SBAR(); }
            }
            *(LAS float*)(lw0) = mx[0]; *(LAS float*)(lw1) = mx[1]; *(LAS float*)(lw2) = mx[2]; *(LAS float*)(lw3) = mx[3];
            qbar(8u);
            UNROLL for (int q = 0; q < 4; ++q) { const f32x4 n0 = *(LAS const f32x4*)(L + (lsb ^ (((q + 1) & 3) * 512)));
                set_shift(q1[q], fmaxf(fmaxf(n0[0], n0[1]), fmaxf(n0[2], n0[3])), hi); }
            qbar(12u);
            UNROLL for (int q = 0; q < 4; ++q) {
                f32x16 acc = {};
                _Pragma("unroll 1") for (int t = 0; t < 16; ++t) {
                    const bf16x8 a = asbf(__builtin_amdgcn_raw_buffer_load_b128(rsWS, l16, kv0 + t * 4096, SC1));
                    const bf16x8 c = asbf(__builtin_amdgcn_raw_buffer_load_b128(rsWS, l16, kv0 + t * 4096 + 1024, SC1));
                    const bf16x8 d = asbf(__builtin_amdgcn_raw_buffer_load_b128(rsWS, l16, kv0 + t * 4096 + 2048, SC1));
                    const bf16x8 e = asbf(__builtin_amdgcn_raw_buffer_load_b128(rsWS, l16, kv0 + t * 4096 + 3072, SC1));
                    f32x16 s = MFMA32(a, asbf(q0[q]), (f32x16){}); s = MFMA32(c, asbf(q1[q]), s);
                    UNROLL for (int r = 0; r < 16; ++r) s[r] = __builtin_amdgcn_exp2f(s[r]);
                    acc = MFMA32(d, asbf(pack8(s, 0)), acc); acc = MFMA32(e, asbf(pack8(s, 8)), acc);
                }
                if (q == 0) { GIVE(acc, ro0, lw0); } else if (q == 1) { GIVE(acc, ro1, lw1); } else if (q == 2) { GIVE(acc, ro2, lw2); } else { *(LAS float*)(lw3) = acc[12]; o[3] = acc; }
            }
            qbar(16u);
            UNROLL for (int t = 0; t < 6; ++t) f[t] = ld128(rop + (t >> 1) * 8192 + (t & 1) * 1024);
            m0 = *(LAS const f32x4*)(lsp);
        }
        __builtin_amdgcn_s_setprio(1);
        f32x16 t = o[3];
        UNROLL for (int k = 0; k < 6; ++k) t = MFMA32(asbf((k & 1) ? sel1 : sel0), f[k], t);
        {
            float s0 = m0[0] + m0[1], s2 = m0[2] + m0[3]; PIN(s0); PIN(s2);
            f32x2 sc; sc[0] = sc[1] = gt * __builtin_amdgcn_rcpf(s0 + s2); PIN(sc);
            u32x4 oa, ob;
            UNROLL for (int e = 0; e < 4; ++e) { const f32x2 a = sc * (f32x2){t[2 * e], t[2 * e + 1]}; oa[e] = nosent(cvt2(a[0], a[1])); }
            st128(oa, rsWS, l16, opiece, SC1);
            SBAR();
            UNROLL for (int e = 0; e < 4; ++e) { const f32x2 a = sc * (f32x2){t[8 + 2 * e], t[9 + 2 * e]}; ob[e] = nosent(cvt2(a[0], a[1])); }
            if (hi) { ob[2] = 0u; ob[3] = 0u; }
            st128(ob, rsWS, l16, opiece + 1024, SC1);
        }
        __builtin_amdgcn_s_setprio(0);
        STAMP(8);
        const int tok = blk * 64 + qrow, col = 56 * j + 32 * hd + 4 * hi;
        const rsrc_t rsD = mkrs(dskip, ROWB), rsO = mkrs(out, SEQ * DM * 4);
        wait_vm<0>();
        ladd(L, L_OD, 1u);
        f32x4 r[4]; u32x2 xv[4], dv[4];
        UNROLL for (int e = 0; e < 4; ++e) { xv[e] = __builtin_amdgcn_raw_buffer_load_b64(rsX, tok * ROWB + col * 2 + e * 16, 0, 0); dv[e] = __builtin_amdgcn_raw_buffer_load_b64(rsD, col * 2 + e * 16, 0, 0); }
        SBAR();
        UNROLL for (int e = 0; e < 4; ++e) {
            float t0 = bf2f(xv[e][0]) * bf2f(dv[e][0]), t1 = bfhi(xv[e][0]) * bfhi(dv[e][0]), t2 = bf2f(xv[e][1]) * bf2f(dv[e][1]), t3 = bfhi(xv[e][1]) * bfhi(dv[e][1]);
            PIN(t0); PIN(t1); PIN(t2); PIN(t3); t0 *= 1.0f - gt; t1 *= 1.0f - gt; t2 *= 1.0f - gt; t3 *= 1.0f - gt; PIN(t0); PIN(t1); PIN(t2); PIN(t3);
            r[e] = (f32x4){t0, t1, t2, t3}; }
        const lp rp = lbase(L, L_RED + hd * 32768 + th * 16384 + l16);
        unsigned oo = (tok * DM + col) * 4, o3 = hd ? OOB : oo + 96; PIN(oo); PIN(o3);
        lds_bar();
        STAMP(10);
        UNROLL for (int e = 0; e < 4; ++e) {
            r[e] += (*(LAS const f32x4*)(rp + e * 1024) + *(LAS const f32x4*)(rp + 4096 + e * 1024)) + (*(LAS const f32x4*)(rp + 8192 + e * 1024) + *(LAS const f32x4*)(rp + 12288 + e * 1024));
            st128(__builtin_bit_cast(u32x4, r[e]), rsO, e < 3 ? oo + e * 32 : o3, 0, 0); }
    } else {
        const int u = w - 4, st = min(16 * u + r16, 55);
        const unsigned eoff = (G == 0 && 16 * u + r16 < 56) ? st * 8 : OOB;
        const rsrc_t rsBR = mkrs(bre, DM * ROWB), rsBI = mkrs(bim, DM * ROWB);
        const unsigned vb0_ = (56 * j + 16 * u + r8) * ROWB + ((sl ^ (r8 >> 1)) << 4), vb1_ = u < 3 ? (vb0_ ^ 64) + 8 * ROWB : OOB;
#define DST2(a) (u < 3 ? (a) : l0 + L_DUMMY)
        unsigned p0, p1, p2;
        const uint16_t *a0 = lre + 56 * j + st, *a1 = lim + 56 * j + st, *a2 = logdt + 56 * j + st; PIN(a0); PIN(a1); PIN(a2);
        asm volatile("s_barrier" ::: "memory");
        asm volatile("global_load_ushort %0, %3, off\n\tglobal_load_ushort %1, %4, off\n\tglobal_load_ushort %2, %5, off"
                     : "=&v"(p0), "=&v"(p1), "=&v"(p2) : "v"(a0), "v"(a1), "v"(a2) : "memory");
        XREST();
#define BOFF(c) ((c) % 3 == 0 ? L_B0 : L_WK + ((c) % 3 - 1) * 14336)
#define BD(c) do { dma(rsBR, vb0_, (c) * 128, l0 + BOFF(c) + u * 2048); dma(rsBI, vb0_, (c) * 128, l0 + BOFF(c) + 7168 + u * 2048); \
                   dma(rsBR, vb1_, (c) * 128, DST2(l0 + BOFF(c) + u * 2048 + 1024)); dma(rsBI, vb1_, (c) * 128, DST2(l0 + BOFF(c) + 7168 + u * 2048 + 1024)); } while (0)
        STAMP(14);
        asm volatile("s_waitcnt vmcnt(6)" : "+v"(p0), "+v"(p1), "+v"(p2) :: "memory");
        f32x2 lam, coef; cf lam8, lam16, lam64, wlo, whi, zG;
        {   const float lr = bf2f(p0), li = bf2f(p1), dt = bfround(fexp(bf2f(p2)));
            const float er = fexp(dt * lr), rev = __builtin_amdgcn_fractf(dt * li * 0.15915494309189535f);
            const cf l1 = {er * __builtin_amdgcn_cosf(rev), er * __builtin_amdgcn_sinf(rev)};
            const float inv = __builtin_amdgcn_rcpf(lr * lr + li * li);
            const cf cq = cmul((cf){l1.r - 1.0f, l1.i}, (cf){lr * inv, -li * inv});
            lam = (f32x2){l1.r, l1.i}; coef = (f32x2){cq.r, cq.i};
            const cf l2 = cmul(l1, l1), l4 = cmul(l2, l2); lam8 = cmul(l4, l4); lam16 = cmul(lam8, lam8);
            const cf l32 = cmul(lam16, lam16); lam64 = cmul(l32, l32);
            const cf l128 = cmul(lam64, lam64), l256 = cmul(l128, l128), l512 = cmul(l256, l256), l1024 = cmul(l512, l512), one = {1.f, 0.f};
            wlo = cmul((G & 1) ? lam16 : one, (G & 2) ? l32 : one); whi = cmul(wlo, lam8);
            zG = cmul((G & 1) ? l512 : one, (G & 2) ? l1024 : one);
            asm volatile("" : "+v"(lam), "+v"(coef), "+v"(lam8.r), "+v"(lam8.i), "+v"(lam16.r), "+v"(lam16.i), "+v"(lam64.r), "+v"(lam64.i),
                              "+v"(wlo.r), "+v"(wlo.i), "+v"(whi.r), "+v"(whi.i), "+v"(zG.r), "+v"(zG.i) :: "memory"); }
        const int rr = u < 3 ? r16 : r16 & 7;
        const unsigned bro = u * 2048 + rr * 128 + ((G ^ (rr >> 1)) << 4);
        const lp br0 = lbase(L, L_WK + bro), br1 = lbase(L, L_WK + (bro ^ 64)), br2 = lbase(L, L_B0 + bro), br3 = lbase(L, L_B0 + (bro ^ 64));
        lp at[4][2];
        UNROLL for (int m = 0; m < 4; ++m) { const int row = 16 * (r16 >> 2) + 2 * m + ((r16 & 3) >> 1) + 8 * (r16 & 1); const unsigned a = L_X + row * 128 + ((G ^ xswz(row)) << 4);
            at[m][0] = lbase(L, a); at[m][1] = lbase(L, a ^ 64); }
        wait_vm<0>();
        lcount(L, L_XA, lane);
        STAMP(1);
        BD(0);
        lwait(L, L_KD, 1u);
        BD(1); BD(2);
        f32x4 ur[4] = {}, ui[4] = {};
        bf16x8 fb[2][4], fx[2][8];
#define BP(c, k) ((c) % 3 == 0 ? ((k) ? br3 : br2) : ((k) ? br1 : br0) + ((c) % 3 - 1) * 14336)
#define BRD(c) do { wait_vm<(c) < 5 ? 8 : 4 * (6 - (c))>(); fb[(c) & 1][0] = ld128(BP(c, 0)); fb[(c) & 1][1] = ld128(BP(c, 0) + 7168); fb[(c) & 1][2] = ld128(BP(c, 1)); fb[(c) & 1][3] = ld128(BP(c, 1) + 7168); \
                    if ((c) < 4) { asm volatile("s_waitcnt lgkmcnt(0)" ::: "memory"); BD((c) + 3); } } while (0)
#define XRD(c, i) fx[(c) & 1][i] = ld128(at[(i) & 3][(i) >> 2] + (c) * 8192)
#define UCH(c) UNROLL for (int i = 0; i < 8; ++i) { \
            if (i == 0 && (c) < 6) BRD((c) < 6 ? (c) + 1 : 6); \
            ur[i & 3] = MFMA16(fx[(c) & 1][i], fb[(c) & 1][2 * (i >> 2)], ur[i & 3]); ui[i & 3] = MFMA16(fx[(c) & 1][i], fb[(c) & 1][2 * (i >> 2) + 1], ui[i & 3]); \
            if ((c) < 6) XRD((c) + 1, i); \
            SBAR(); }
        BRD(0);
        lwait(L, L_XA, 8u);
        STAMP(2);
        UNROLL for (int i = 0; i < 8; ++i) XRD(0, i);
        SBAR();
        UCH(0) UCH(1) UCH(2)
        __builtin_amdgcn_s_setprio(UPRIO);
        UCH(3) UCH(4) UCH(5) UCH(6)
        lcount(L, L_UF, lane);
        asm volatile("s_nop 7" : "+v"(ur[0]), "+v"(ur[1]), "+v"(ur[2]), "+v"(ur[3]), "+v"(ui[0]), "+v"(ui[1]), "+v"(ui[2]), "+v"(ui[3]));
        STAMP(3);
        f32x2 hr, hm;
        UNROLL for (int p = 0; p < 8; ++p) {
            f32x2 tr, ti; pk_cmul(tr, ti, coef, PAIR(ur, p), PAIR(ui, p));
            ur[p >> 1][2 * (p & 1)] = tr[0]; ur[p >> 1][2 * (p & 1) + 1] = tr[1]; ui[p >> 1][2 * (p & 1)] = ti[0]; ui[p >> 1][2 * (p & 1) + 1] = ti[1];
            if (p == 0) { hr = tr; hm = ti; } else { f32x2 nr, ni; pk_cfma(nr, ni, lam, hr, hm, tr, ti); hr = nr; hm = ni; }
        }
        const cf elo = {hr[0], hm[0]}, e16 = cfma(lam8, elo, (cf){hr[1], hm[1]});
        cf e0, e1, e2, e3; bcast4(e16.r, e0.r, e1.r, e2.r, e3.r); bcast4(e16.i, e0.i, e1.i, e2.i, e3.i);
        const cf t2 = cfma(lam16, e0, e1), t3 = cfma(lam16, t2, e2), eb = cfma(lam16, t3, e3);
        lwait(L, L_KR, 1u); lwait(L, L_QF, 4u);
        __builtin_amdgcn_raw_buffer_store_b64((u32x2){nosent(__float_as_uint(eb.r)), nosent(__float_as_uint(eb.i))}, rsWS, eoff, WS_E + (blk * DM + 56 * j) * 8, SC1);
        const cf pg = {G == 0 ? 0.f : G == 1 ? e0.r : G == 2 ? t2.r : t3.r, G == 0 ? 0.f : G == 1 ? e0.i : G == 2 ? t2.i : t3.i}, ph = cfma(lam8, pg, elo);
        STAMP(4);
        asm volatile("s_mov_b32 m0, %0\n\ts_nop 0\n\tbuffer_load_dword %1, %2, %3 offen sc1 lds"
                     :: "s"(l0 + L_DUMMY), "v"(u == 0 ? lane * 128 : OOB), "s"(rsWS), "s"(WS_KV + (b >> 3) * 8192) : "memory");
        st128((u32x4){~0u, ~0u, ~0u, ~0u}, rsWS, l16, opiece, SC1);
        st128((u32x4){~0u, ~0u, ~0u, ~0u}, rsWS, l16, opiece + 1024, SC1);
        const float gt = *(LAS const float*)(L + L_G + (32 * th + r32) * 4), go = *(LAS const float*)(L + L_G + (32 * (th ^ 1) + r32) * 4);
        lwait(L, L_UF, 4u);
        {   const rsrc_t rsCR = mkrs(cre, DM * ROWB), rsCI = mkrs(cim, DM * ROWB);
            const unsigned vc = (56 * j + 8 * u + r8) * ROWB + ((sl ^ ((4 * u + (r8 >> 1)) & 7)) << 4), vc2 = u < 3 ? vc + 32 * ROWB : OOB;
            UNROLL for (int cc = 0; cc < 7; ++cc) {
                dma(rsCR, vc, cc * 128, l0 + L_C + cc * 7168 + u * 1024); PACE(); dma(rsCI, vc, cc * 128, l0 + L_CIM + cc * 7168 + u * 1024); PACE();
                dma(rsCR, vc2, cc * 128, DST2(l0 + L_C + cc * 7168 + (u + 4) * 1024)); PACE(); dma(rsCI, vc2, cc * 128, DST2(l0 + L_CIM + cc * 7168 + (u + 4) * 1024)); PACE(); } }
        const rsrc_t rsE = mkrs(ws + WS_E + 56 * j * 8, 32 * DM * 8 - 56 * j * 8);
        const int ebase = (blk - 1 - 8 * G) * (DM * 8) + st * 8;
        const unsigned hoff = lane == 0 ? WS_HCNT + blk * 128 : OOB;
        STAMP(38);
        {
            u32x2 ev[8];
            _Pragma("unroll 1") for (unsigned spin = 0; spin < SPIN_CAP; ++spin) {
                asm volatile("" ::: "memory");
                UNROLL for (int m = 0; m < 8; ++m) ev[m] = __builtin_amdgcn_raw_buffer_load_b64(rsE, ebase - m * (DM * 8), 0, SC1);
                unsigned mx = 0;
                UNROLL for (int m = 0; m < 8; ++m) mx = max(mx, max(ev[m][0], ev[m][1]));
                if (__builtin_amdgcn_ballot_w64(mx == ~0u) == 0) break;
                __builtin_amdgcn_s_sleep(8);
            }
            STAMP(39);
            cf part = {__uint_as_float(ev[7][0]), __uint_as_float(ev[7][1])};
            UNROLL for (int m = 6; m >= 0; --m) part = cfma(lam64, part, (cf){__uint_as_float(ev[m][0]), __uint_as_float(ev[m][1])});
            part = cmul(zG, part);
            if (ebase < 0) part = (cf){0.f, 0.f};
            part.r = sum4(part.r); part.i = sum4(part.i);
            const cf ilo = cfma(wlo, part, pg), ihi = cfma(whi, part, ph);
            hr = (f32x2){ilo.r, ihi.r}; hm = (f32x2){ilo.i, ihi.i};
            u32x4 pr0, pr1, pi0, pi1;
            UNROLL for (int p = 0; p < 8; ++p) {
                f32x2 nr, ni; pk_cfma(nr, ni, lam, hr, hm, PAIR(ur, p), PAIR(ui, p)); hr = nr; hm = ni;
                unsigned cr, ci; asm("v_cvt_pk_bf16_f32 %0, %2, %3\n\tv_cvt_pk_bf16_f32 %1, -%4, -%5" : "=&v"(cr), "=&v"(ci) : "v"(hr[0]), "v"(hr[1]), "v"(hm[0]), "v"(hm[1]));
                if (p < 4) { pr0[p & 3] = cr; pi0[p & 3] = ci; } else { pr1[p & 3] = cr; pi1[p & 3] = ci; }
            }
            const unsigned sb = L_HS + u * 4096, wa = sb + r16 * 128 + (((2 * G) ^ (r16 & 7)) << 4);
            const int tok = (lane & 48) + (r16 >> 1) + 8 * (lane & 1);
            const unsigned gha = (tok >> 5) * 57344 + (tok & 31) * 16, ghb = u < 3 ? gha + 1024 : OOB, gs = WS_H + blk * 114688 + (7 * j + 2 * u) * 1024;
            const unsigned rao = sb + tq * 128 + (((2 * G + (tp >> 1)) ^ tq) << 4) + 8 * (tp & 1);
            const lp wa0 = lbase(L, wa), wa1 = lbase(L, wa ^ 16), ra = lbase(L, rao), rb = lbase(L, (rao ^ 64) + 512);
            *(LAS u32x4*)(wa0) = pr0; *(LAS u32x4*)(wa1) = pr1; *(LAS u32x4*)(wa0 + 2048) = pi0; *(LAS u32x4*)(wa1 + 2048) = pi1;
            asm volatile("" ::: "memory");
            UNROLL for (int pl = 0; pl < 2; ++pl) {
                const bf16x8 c0 = ldtr2(ra + pl * 2048, rb + pl * 2048), c1 = ldtr2(ra + pl * 2048 + 1024, rb + pl * 2048 + 1024);
                st128(__builtin_bit_cast(u32x4, c0), rsWS, gha + pl * 512, gs, SC1);
                st128(__builtin_bit_cast(u32x4, c1), rsWS, ghb + pl * 512, gs, SC1);
            }
        }
        STAMP(5);
        lcount(L, L_SF, lane);
        wait_vm<0>();
        atomic_inc(rsWS, hoff);
        lwait(L, L_SF, 8u);
        { const rsrc_t rsWO = mkrs(wo, DM * ROWB);
          UNROLL for (int n = 0; n < 14; ++n) { dma(rsWO, vw, (u * 14 + n) * 8 * ROWB, l0 + L_WO + (u * 14 + n) * 1024); PACE(); } }
        STAMP(40);
        _Pragma("unroll 1") for (unsigned spin = 0; __builtin_amdgcn_readfirstlane(atomic_read(rsWS, hoff)) != 32 && spin < SPIN_CAP; ++spin) __builtin_amdgcn_s_sleep(8);
        lcount(L, L_WF, lane);
        const unsigned hb = WS_H + blk * 114688 + u * 1024;
        u32x4 h0[14], h1[14];
        UNROLL for (int m = 0; m < 14; ++m) { h0[m] = __builtin_amdgcn_raw_buffer_load_b128(rsWS, l16, hb + th * 57344 + m * 4096, SC1); PACE(); }
        const unsigned cx = (32 * (G & 1) + 8 * tp) ^ (((tq >> 1) & 1) << 6), nb = L_WO + (112 * u + tq) * 128, wa_ = nb + hi * 512;
        const lp wa0_ = lbase(L, wa_ + cx), wa1_ = lbase(L, wa_ + (cx ^ 64)), nb0 = lbase(L, nb + cx), nb1 = lbase(L, nb + (cx ^ 64));
        lwait(L, L_WF, 4u);
        bf16x8 wf0[8], wf1[8];
        UNROLL for (int k = 0; k < 8; ++k) { const unsigned ro = (28 * (k >> 1) + 16 * (k & 1)) * 128;
            wf0[k] = ldtr2(wa0_ + ro, ((k & 1) ? nb0 : wa0_) + ro + 1024); wf1[k] = ldtr2(wa1_ + ro, ((k & 1) ? nb1 : wa1_) + ro + 1024); }
        lcount(L, L_UF, lane);
        STAMP(42);
        lwait_idle(L, L_QL, 4u);
        UNROLL for (int m = 0; m < 14; ++m) h1[m] = __builtin_amdgcn_raw_buffer_load_b128(rsWS, l16, hb + (th ^ 1) * 57344 + m * 4096, SC1);
        const int rbc = min(32 + r32, 55);
        const unsigned cao = hi * L_CIM + r32 * 128 + ((u ^ ((r32 >> 1) & 7)) << 4), cbo = hi * L_CIM + rbc * 128 + ((u ^ ((rbc >> 1) & 7)) << 4);
        const lp ca0 = lbase(L, cao), ca1 = lbase(L, cao ^ 64), cb0 = lbase(L, cbo), cb1 = lbase(L, cbo ^ 64);
        const lp rp = lbase(L, L_RED + th * 16384 + u * 4096 + l16), rq = lbase(L, L_RED + (th ^ 1) * 16384 + u * 4096 + l16);
        const unsigned oa = WS_O + blk * 65536 + (th * 16 + 4 * u) * 2048, ob = WS_O + blk * 65536 + ((th ^ 1) * 16 + 4 * u) * 2048;
        f32x2 s2 = {2.0f * (1.0f - gt), 0.f}, s3 = {2.0f * (1.0f - go), 0.f}; PIN(s2); PIN(s3);
        STAMP(41);
        __builtin_amdgcn_s_setprio(0);
        lwait_idle(L, L_QE, 4u);
        __builtin_amdgcn_s_sleep(YSLEEP);
        STAMP(15);
        f32x16 y[4]; y[0] = y[1] = y[2] = y[3] = (f32x16){};
        u32x4 of[16];
#define OLD(k0) UNROLL for (int k = (k0); k < (k0) + 8; ++k) of[k] = __builtin_amdgcn_raw_buffer_load_b128(rsWS, l16 + (k & 3) * 1024, (k < 8 ? oa : ob) + ((k >> 2) & 1) * 4096, SC1)
        {   bf16x8 fa[28], fc[28];
            UNROLL for (int m = -YD; m < 28; ++m) {
                if (m + YD < 28) { const int n = (m + YD) % 14; fa[m + YD] = ld128(((n & 1) ? ca1 : ca0) + (n >> 1) * 7168); fc[m + YD] = ld128(((n & 1) ? cb1 : cb0) + (n >> 1) * 7168); }
                if (m >= 0 && m < 14) { y[0] = MFMA32(fa[m], asbf(h0[m]), y[0]); y[1] = MFMA32(fc[m], asbf(h0[m]), y[1]); }
                if (m >= 14) { y[2] = MFMA32(fa[m], asbf(h1[m - 14]), y[2]); y[3] = MFMA32(fc[m], asbf(h1[m - 14]), y[3]); }
                if (m >= 16 && m < 24) { UNROLL for (int k = 0; k < 2; ++k) { const f32x2 a = pk_scale(s2, (f32x2){y[k][2 * (m - 16)], y[k][2 * (m - 16) + 1]}); y[k][2 * (m - 16)] = a[0]; y[k][2 * (m - 16) + 1] = a[1]; } PIN(y[0]); PIN(y[1]); }
                if (m == 26) ladd(L, L_YF, 1u);
                SBAR();
            } }
        STAMP(9);
        lwait(L, L_YF, 4u);
        asm volatile("s_nop 11" : "+v"(y[2]), "+v"(y[3]));
        UNROLL for (int k = 2; k < 4; ++k) UNROLL for (int e = 0; e < 8; ++e) { const f32x2 a = pk_scale(s3, (f32x2){y[k][2 * e], y[k][2 * e + 1]}); y[k][2 * e] = a[0]; y[k][2 * e + 1] = a[1]; }
        asm volatile("s_nop 1" : "+v"(y[2]), "+v"(y[3]));
        lwait(L, L_OD, ODN);
        if (OSLEEP) __builtin_amdgcn_s_sleep(OSLEEP);
        STAMP(43);
        OLD(0); OLD(8);
        _Pragma("unroll 1") for (unsigned spin = 0; spin < SPIN_CAP; ++spin) {
            unsigned mx = 0;
            UNROLL for (int k = 0; k < 16; ++k) mx = max(max(mx, max(of[k][0], of[k][1])), max(of[k][2], of[k][3]));
            if (__builtin_amdgcn_ballot_w64(mx == ~0u) == 0) break;
            asm volatile("" ::: "memory");
            OLD(0); OLD(8);
        }
        STAMP(10);
#define REDW(p, Y, e) *(LAS f32x4*)((p) + (e) * 1024) = (f32x4){Y[4 * (e)], Y[4 * (e) + 1], Y[4 * (e) + 2], Y[4 * (e) + 3]}
        UNROLL for (int k = 0; k < 8; ++k) y[0] = MFMA32(wf0[k], asbf(of[k]), y[0]);
        PIN(y[0]); SBAR();
        UNROLL for (int k = 0; k < 8; ++k) { y[1] = MFMA32(wf1[k], asbf(of[k]), y[1]); SBAR(); if (k >= 3 && k < 7) REDW(rp, y[0], k - 3); SBAR(); }
        UNROLL for (int k = 0; k < 8; ++k) { y[2] = MFMA32(wf0[k], asbf(of[8 + k]), y[2]); SBAR(); if (k >= 3 && k < 7) REDW(rp + 32768, y[1], k - 3); SBAR(); }
        UNROLL for (int k = 0; k < 8; ++k) { y[3] = MFMA32(wf1[k], asbf(of[8 + k]), y[3]); SBAR(); if (k >= 3 && k < 7) REDW(rq, y[2], k - 3); SBAR(); }
        UNROLL for (int e = 0; e < 4; ++e) REDW(rq + 32768, y[3], e);
        lds_bar();
    }
    STAMP(11);
}

extern "C" void kernel_launch(void* const* d_in, const int* in_sizes, int n_in, void* d_out, int out_size, void* d_ws, size_t ws_size,
                              hipStream_t stream) {
    const uint16_t* const* in = (const uint16_t* const*)d_in;
    gated_mix_kernel<<<256, 512, 0, stream>>>(in[0], in[1], in[2], in[3], in[4], in[6], in[7], in[8], in[9], in[10], in[11], in[12], in[13], in[14],
                                              (float*)d_out, (char*)d_ws);
}
```

```cpp
#include <hip/hip_runtime.h>
#include <hip/hip_bf16.h>
#include <stdint.h>

typedef __bf16 bf16x8 __attribute__((ext_vector_type(8)));
typedef __bf16 bf16x2 __attribute__((ext_vector_type(2)));
typedef float f32x16 __attribute__((ext_vector_type(16)));
typedef float f32x4 __attribute__((ext_vector_type(4)));
typedef float f32x2 __attribute__((ext_vector_type(2)));
typedef unsigned u32x4 __attribute__((ext_vector_type(4)));
typedef unsigned u32x2 __attribute__((ext_vector_type(2)));
typedef short s16x4 __attribute__((ext_vector_type(4)));
typedef __amdgpu_buffer_rsrc_t rsrc_t;
#define LAS __attribute__((address_space(3)))
#define DI __device__ __forceinline__
#define SBAR() __builtin_amdgcn_sched_barrier(0)
#define PIN(x) asm volatile("" : "+v"(x))
#define PINS(x) asm volatile("" : "+s"(x))
#define MFMA32(a, b, c) __builtin_amdgcn_mfma_f32_32x32x16_bf16((a), (b), (c), 0, 0, 0)
#define MFMA16(a, b, c) __builtin_amdgcn_mfma_f32_16x16x32_bf16((a), (b), (c), 0, 0, 0)
#define UNROLL _Pragma("unroll")
#define PACE() asm volatile("s_sleep %0" :: "i"(CSLEEP) : "memory")
#define SC1 16

#define CSLEEP 2
#define YSLEEP 8
#define CPREF 32768
#define WQ2 8
#define YD 3
#define OSLEEP 6
#define PACKED 1
#define ODN 4u
#define PFC 0
#define B0EARLY 0
#define KCHK 29
#define PFE 0
#define ODRAIN 0
#define QREP 2
#define KPOLL 16
#define UPRIO 1
#define SPRIO 0
#define OM0 16
#define OM1 22
#define MAP42 0

constexpr int SEQ = 2048, DM = 448, ROWB = DM * 2;
constexpr unsigned WS_KVCNT = 0;
constexpr unsigned WS_HCNT = 16384;
constexpr unsigned WS_X = 20480;
constexpr unsigned OOB = 0x7FFFFFF0u;
constexpr unsigned WS_KV = 65536;
constexpr unsigned WS_E = WS_KV + 262144;
constexpr unsigned WS_H = WS_E + 131072;
constexpr unsigned WS_O = WS_H + 32 * 114688;
constexpr unsigned WS_END = WS_O + 32 * 65536;
constexpr unsigned WS_STAMP = WS_END;

constexpr unsigned L_X = 0, L_WK = 57344, L_WQ = 86016, L_B0 = 143360, L_G = 160768;
constexpr unsigned L_KD = 161024, L_XA = 161028, L_XF = 161060, L_QD = 161032, L_QF = 161036, L_UF = 161040, L_WF = 161044, L_KR = 161048, L_YF = 161052, L_XG = 161064, L_GV = 161068, L_QE = 161072, L_SF = 161076, L_QL = 161080, L_OD = 161056, L_FB = 161084;
constexpr unsigned L_QSH = 122880, L_HS = 106496;
constexpr unsigned L_C = 0, L_CIM = 50176, L_WO = 100352;
constexpr unsigned L_RO = 131072, L_LS = 155648;
constexpr unsigned L_RED = 0, L_DUMMY = 157696;
constexpr unsigned LDS_BYTES = 163840;
constexpr unsigned SPIN_CAP = 1u << 14;

DI unsigned cvt2(float a, float b) { f32x2 v = {a, b}; return __builtin_bit_cast(unsigned, __builtin_convertvector(v, bf16x2)); }
DI float bf2f(unsigned h) { return __uint_as_float(h << 16); }
DI float bfhi(unsigned h) { return __uint_as_float(h & 0xffff0000u); }
DI float bfround(float x) { return __uint_as_float(cvt2(x, 0.f) << 16); }
DI float fexp(float x) { return __builtin_amdgcn_exp2f(x * 1.4426950408889634f); }
DI rsrc_t mkrs(const void* p, unsigned bytes) { return __builtin_amdgcn_make_buffer_rsrc((void*)p, (short)0, (int)bytes, 0x00020000); }
DI void dma(rsrc_t rs, unsigned voff, unsigned soff, unsigned lds) {
    asm volatile("s_mov_b32 m0, %0\n\ts_nop 0\n\tbuffer_load_dwordx4 %1, %2, %3 offen lds" :: "s"(lds), "v"(voff), "s"(rs), "s"(soff) : "memory"); }
template <int N> DI void wait_bar() { asm volatile("s_waitcnt vmcnt(%0) lgkmcnt(0)\n\ts_barrier" :: "i"(N) : "memory"); }
template <int N> DI void wait_vm() { asm volatile("s_waitcnt vmcnt(%0)" :: "i"(N) : "memory"); }
DI void lds_bar() { asm volatile("s_waitcnt lgkmcnt(0)\n\ts_barrier" ::: "memory"); }
typedef LAS char* lp;
DI lp lbase(LAS char* L, unsigned off) { lp p = L + off; PIN(p); return p; }
DI bf16x8 ld128(lp p) { return __builtin_bit_cast(bf16x8, *(LAS const u32x4*)p); }
DI bf16x8 ldtr2(lp p0, lp p1) {
    s16x4 a = __builtin_amdgcn_ds_read_tr16_b64_v4i16((LAS s16x4*)p0), b = __builtin_amdgcn_ds_read_tr16_b64_v4i16((LAS s16x4*)p1);
    return __builtin_bit_cast(bf16x8, __builtin_shufflevector(a, b, 0, 1, 2, 3, 4, 5, 6, 7)); }
DI bf16x8 asbf(u32x4 v) { return __builtin_bit_cast(bf16x8, v); }
DI void st128(u32x4 v, rsrc_t rs, unsigned voff, unsigned soff, int aux) {
    if (aux) __builtin_amdgcn_raw_buffer_store_b128(v, rs, voff, soff, SC1); else __builtin_amdgcn_raw_buffer_store_b128(v, rs, voff, soff, 0);
    asm volatile("s_nop 1" :: "v"(v)); }
DI unsigned nosent(unsigned v) { return v < 0xFFFFFFFEu ? v : 0xFFFFFFFEu; }
DI unsigned atomic_read(rsrc_t rs, unsigned voff) { return (unsigned)__builtin_amdgcn_raw_ptr_buffer_atomic_add_i32(0, rs, (int)voff, 0, SC1); }
DI void atomic_inc(rsrc_t rs, unsigned voff) { asm volatile("buffer_atomic_add %0, %1, %2, 0 offen sc1" :: "v"(1u), "v"(voff), "s"(rs) : "memory"); }
struct cf { float r, i; };
DI cf cmul(cf a, cf b) { cf o;
    asm("v_mul_f32 %0, %3, %5\n\tv_mul_f32 %1, %3, %4\n\tv_fma_f32 %0, %2, %4, -%0\n\tv_fma_f32 %1, %2, %5, %1" : "=&v"(o.r), "=&v"(o.i) : "v"(a.r), "v"(a.i), "v"(b.r), "v"(b.i)); return o; }
DI cf cfma(cf a, cf b, cf c) { cf o;
    asm("v_fma_f32 %0, -%3, %5, %6\n\tv_fma_f32 %1, %3, %4, %7\n\tv_fma_f32 %0, %2, %4, %0\n\tv_fma_f32 %1, %2, %5, %1"
        : "=&v"(o.r), "=&v"(o.i) : "v"(a.r), "v"(a.i), "v"(b.r), "v"(b.i), "v"(c.r), "v"(c.i)); return o; }
DI void pk_cfma(f32x2& nr, f32x2& ni, f32x2 l, f32x2 hr, f32x2 hm, f32x2 a, f32x2 c) {
    asm("v_pk_fma_f32 %0, %2, %3, %5 op_sel_hi:[0,1,1]\n\tv_pk_fma_f32 %1, %2, %4, %6 op_sel_hi:[0,1,1]\n\t"
        "v_pk_fma_f32 %0, %2, %4, %0 op_sel:[1,0,0] neg_lo:[1,0,0] neg_hi:[1,0,0]\n\tv_pk_fma_f32 %1, %2, %3, %1 op_sel:[1,0,0]"
        : "=&v"(nr), "=&v"(ni) : "v"(l), "v"(hr), "v"(hm), "v"(a), "v"(c)); }
DI void pk_cmul(f32x2& nr, f32x2& ni, f32x2 l, f32x2 hr, f32x2 hm) {
    asm("v_pk_mul_f32 %0, %2, %3 op_sel_hi:[0,1]\n\tv_pk_mul_f32 %1, %2, %4 op_sel_hi:[0,1]\n\t"
        "v_pk_fma_f32 %0, %2, %4, %0 op_sel:[1,0,0] neg_lo:[1,0,0] neg_hi:[1,0,0]\n\tv_pk_fma_f32 %1, %2, %3, %1 op_sel:[1,0,0]"
        : "=&v"(nr), "=&v"(ni) : "v"(l), "v"(hr), "v"(hm)); }
DI f32x2 pk_scale(f32x2 s, f32x2 a) { f32x2 r; asm("v_pk_mul_f32 %0, %1, %2 op_sel_hi:[0,1]" : "=v"(r) : "v"(s), "v"(a)); return r; }
DI float sum4(float v) {
    auto a = __builtin_amdgcn_permlane16_swap(__float_as_uint(v), __float_as_uint(v), false, false); v = __uint_as_float(a[0]) + __uint_as_float(a[1]);
    auto c = __builtin_amdgcn_permlane32_swap(__float_as_uint(v), __float_as_uint(v), false, false); return __uint_as_float(c[0]) + __uint_as_float(c[1]); }
DI void bcast4(float v, float& r0, float& r1, float& r2, float& r3) {
    auto a = __builtin_amdgcn_permlane16_swap(__float_as_uint(v), __float_as_uint(v), false, false);
    auto e = __builtin_amdgcn_permlane32_swap(a[0], a[0], false, false); auto o = __builtin_amdgcn_permlane32_swap(a[1], a[1], false, false);
    r0 = __uint_as_float(e[0]); r2 = __uint_as_float(e[1]); r1 = __uint_as_float(o[0]); r3 = __uint_as_float(o[1]); }
DI float rowmax16(const f32x16& s) {
    float a = fmaxf(fmaxf(s[0], s[1]), s[2]), b = fmaxf(fmaxf(s[3], s[4]), s[5]);
    a = fmaxf(fmaxf(a, s[6]), s[7]); b = fmaxf(fmaxf(b, s[8]), s[9]); a = fmaxf(fmaxf(a, s[10]), s[11]); b = fmaxf(fmaxf(b, s[12]), s[13]);
    a = fmaxf(fmaxf(a, s[14]), s[15]); a = fmaxf(a, b);
    auto rr = __builtin_amdgcn_permlane32_swap(__float_as_uint(a), __float_as_uint(a), false, false);
    return fmaxf(__uint_as_float(rr[0]), __uint_as_float(rr[1])); }
DI void set_shift(u32x4& q1, float c, int hi) {
    const float c1 = bfround(c), c2 = bfround(c - c1), c3 = c - c1 - c2;
    if (hi) { q1[2] = cvt2(-c1, -c2); q1[3] = cvt2(-c3, 0.f); } }
DI void ladd(LAS char* L, unsigned off, unsigned v) {
    asm volatile("s_mov_b64 exec, 1\n\tds_add_u32 %0, %1\n\ts_mov_b64 exec, -1" :: "v"(L + off), "v"(v) : "memory"); }
DI void lcount(LAS char* L, unsigned off, int) { ladd(L, off, 1u); }
DI unsigned lread(LAS char* L, unsigned off) { return __builtin_amdgcn_readfirstlane(*(volatile LAS unsigned*)(L + off)); }
DI void lwait(LAS char* L, unsigned off, unsigned n) {
    if (__builtin_expect(lread(L, off) < n, 0)) { _Pragma("unroll 1") for (unsigned spin = 0; lread(L, off) < n && spin < (1u << 20); ++spin) { } }
    asm volatile("" ::: "memory"); }
DI void lwait_idle(LAS char* L, unsigned off, unsigned n) {
    _Pragma("unroll 1") for (unsigned spin = 0; lread(L, off) < n && spin < (1u << 18); ++spin) __builtin_amdgcn_s_sleep(2);
    asm volatile("" ::: "memory"); }
DI int xswz(int row) { return ((row >> 1) & 7) ^ ((row & 16) ? 7 : 0) ^ ((row & 32) ? 6 : 0); }
DI u32x4 pack8(const f32x16& a, int e) { return (u32x4){cvt2(a[e], a[e + 1]), cvt2(a[e + 2], a[e + 3]), cvt2(a[e + 4], a[e + 5]), cvt2(a[e + 6], a[e + 7])}; }
#define PAIR(v, p) (f32x2){v[(p) >> 1][2 * ((p) & 1)], v[(p) >> 1][2 * ((p) & 1) + 1]}

#define STAMP(k)
#define STAMPV(k)

__global__ void __launch_bounds__(512, 2)
gated_mix_kernel(const uint16_t* x, const uint16_t* wq, const uint16_t* wk, const uint16_t* wv, const uint16_t* wo, const uint16_t* lre, const uint16_t* lim,
                 const uint16_t* bre, const uint16_t* bim, const uint16_t* cre, const uint16_t* cim, const uint16_t* logdt, const uint16_t* dskip,
                 const uint16_t* gate, float* out, char* ws) {
    __shared__ __attribute__((aligned(16))) char smem[LDS_BYTES];
    LAS char* L = (LAS char*)smem;
    const unsigned l0 = (unsigned)(uintptr_t)L;
    const int lane = threadIdx.x & 63, w = __builtin_amdgcn_readfirstlane(threadIdx.x >> 6);
    const int b = blockIdx.x;
    STAMP(0);
    {   unsigned long long pc; asm volatile("s_getpc_b64 %0" : "=s"(pc));
        const rsrc_t rsC = mkrs((const void*)(pc & ~255ull), CPREF);
        asm volatile("s_mov_b32 m0, %0\n\ts_nop 4\n\tbuffer_load_dword %1, %2, 0 offen lds" :: "s"(l0 + L_DUMMY), "v"(threadIdx.x * 64), "s"(rsC) : "memory"); }
    const int blk = (b & 7) * 4 + (b >> 6), j = (b >> 3) & 7;
    const int th = w & 1, hd = (w >> 1) & 1;
    const int r32 = lane & 31, hi = lane >> 5, r16 = lane & 15, G = lane >> 4;
    const int tq = r16 >> 2, tp = lane & 3;
    const int sl = lane & 7, r8 = lane >> 3;
    const unsigned l16 = lane * 16;
    const int kind = j & 1, tg = j >> 1;
    unsigned vx = r8 * ROWB + ((sl ^ (r8 >> 1)) << 4); PIN(vx);
    PINS(x); PINS(wq); PINS(wk); PINS(wv); PINS(wo); PINS(lre); PINS(lim); PINS(bre); PINS(bim); PINS(cre); PINS(cim); PINS(logdt); PINS(dskip); PINS(gate); PINS(out); PINS(ws);
    const rsrc_t rsX = mkrs(x, SEQ * ROWB), rsWS = mkrs(ws, WS_END);
    const unsigned opiece = WS_O + blk * 65536 + (th * 16 + 2 * j + hd) * 2048;

    const rsrc_t rsWQ = mkrs(wq, DM * ROWB);
    const int pwq = sl ^ (((r8 >> 1) & 1) << 2);
    const unsigned vw = r8 * ROWB + 112 * j + (pwq << 4);
#define WQD(i0, i1) do { if (pwq != 7) { UNROLL for (int i = (i0); i < (i1); ++i) { const int p = i < 18 ? 3 * i + w - 1 : min(53 + w, 55); dma(rsWQ, vw, p * 8 * ROWB, l0 + L_WQ + p * 1024); } } } while (0)
#define BUMP() do { if (b == 0) atomic_inc(rsWS, lane < 16 ? WS_X + lane * 128 : OOB); } while (0)
    if (w < 4) {
        const int qrow = 32 * th + r32;
        const unsigned rob = L_RO + w * 2048 + l16, lsb = L_LS + (w * 32 + r32) * 16;
        lp a00, a01, a02, a03, a1, ro0, ro1, ro2, rop, lsp, lw0, lw1, lw2, lw3, qs0, qs1, qs2, qs3;
        unsigned kco, kc = 0u;
        u32x4 sel0, sel1, pw0, pw1, kf[4][2], vf[4][2];
        f32x16 o[4];
#define QPREP() do { const unsigned a0o = L_X + qrow * 128 + ((hi ^ xswz(qrow)) << 4); \
        a00 = lbase(L, a0o); a01 = lbase(L, a0o ^ 32); a02 = lbase(L, a0o ^ 64); a03 = lbase(L, a0o ^ 96); \
        a1 = lbase(L, L_WQ + (8 * hi + tq) * 128 + ((56 * hd + 32 * (G & 1) + 8 * tp) ^ (((tq >> 1) & 1) << 6))); \
        kco = (w == 0 && lane < 16) ? WS_KVCNT + (b & 7) * 2048 + lane * 128 : OOB; PIN(kco); \
        ro0 = lbase(L, rob ^ 2048); ro1 = lbase(L, (rob ^ 4096) + 8192); ro2 = lbase(L, (rob ^ 6144) + 16384); rop = lbase(L, rob); lsp = lbase(L, lsb); \
        lw0 = lbase(L, hi ? (lsb ^ 512) + w * 4 : L_DUMMY); lw1 = lbase(L, hi ? (lsb ^ 1024) + w * 4 : L_DUMMY); lw2 = lbase(L, hi ? (lsb ^ 1536) + w * 4 : L_DUMMY); lw3 = lbase(L, hi ? lsb + w * 4 : L_DUMMY); \
        qs0 = lbase(L, L_QSH + (w ^ 1) * 2048 + l16); qs1 = lbase(L, L_QSH + (w ^ 2) * 2048 + l16); qs2 = lbase(L, L_QSH + (w ^ 3) * 2048 + l16); qs3 = lbase(L, L_QSH + w * 2048 + l16); \
        const int js = 4 * ((r32 >> 3) & 1) + (r32 & 3); const unsigned one = ((r32 >> 2) & 1) == hi ? 0x3F80u << (16 * (js & 1)) : 0u; \
        UNROLL for (int e = 0; e < 4; ++e) { const unsigned v = (js >> 1) == e ? one : 0u; sel0[e] = r32 < 16 ? v : 0u; sel1[e] = r32 < 16 ? 0u : v; } PIN(sel0); PIN(sel1); \
        o[0] = o[1] = o[2] = o[3] = (f32x16){}; PIN(o[0]); PIN(o[1]); PIN(o[2]); PIN(o[3]); \
        pw1 = (u32x4){0u, 0u, 0u, 0u}; vf[3][1] = pw1; PIN(pw1); PIN(vf[3][1]); } while (0)
        __builtin_amdgcn_s_setprio(2);
        if (w == 0) {
            const unsigned xoff = lane == 0 ? WS_X + (b & 15) * 128 : OOB;
            const rsrc_t rsWK = mkrs(kind ? wv : wk, DM * 56);
            const unsigned vxa = vx ^ (xswz(16 * tg) << 4), vxb = (vx ^ (xswz(16 * tg + 8) << 4)) + 8 * ROWB, xs = (blk * 64 + tg * 16) * ROWB;
            unsigned x0 = 0u, x1 = 0u;
            asm volatile("buffer_atomic_add %0, %1, %2, 0 offen sc0 sc1" : "+v"(x0) : "v"(xoff), "s"(rsWS) : "memory");
            STAMP(36);
            UNROLL for (int c = 0; c < 7; ++c) {
                dma(rsX, vxa, xs + c * 128, l0 + L_X + c * 8192 + tg * 2048); dma(rsX, vxb, xs + c * 128, l0 + L_X + c * 8192 + tg * 2048 + 1024);
                UNROLL for (int n = (7 * c + 1) / 2; n < (7 * c + 8) / 2; ++n) dma(rsWK, l16, n * 1024, l0 + L_WK + n * 1024);
            }
            asm volatile("s_barrier" ::: "memory");
            STAMP(14);
            __builtin_amdgcn_raw_buffer_store_b64((u32x2){~0u, ~0u}, rsWS, lane < 56 ? lane * 8 : OOB, WS_E + (blk * DM + 56 * j) * 8, SC1);
            const int cg0 = kind ? tp : 4 * (tp >> 1) + (tp & 1), cg1 = kind ? 4 + tp : 4 * (tp >> 1) + 2 + (tp & 1);
            const lp aw0 = lbase(L, L_WK + (8 * G + tq) * 56 + cg0 * 8), aw1 = lbase(L, L_WK + (8 * G + tq) * 56 + cg1 * 8);
            const unsigned axo = L_X + (16 * tg + r16) * 128 + ((G ^ xswz(16 * tg + r16)) << 4);
            const lp ax0 = lbase(L, axo), ax1 = lbase(L, axo ^ 64);
            const int sg = tg & 1;
            const unsigned tb = WS_KV + (2 * blk + (tg >> 1)) * 4096 + (kind ? 2048 + sg * 1024 : 0);
            unsigned kva = kind ? ((G >> 1) * 32 + r16) * 16 + (G & 1) * 8 : (G * 32 + 16 * sg + ((r16 & 3) | ((r16 & 4) << 1) | ((r16 & 8) >> 1))) * 16; PIN(kva);
            unsigned kvb = kind ? kva + 256 : kva + 8; PIN(kvb);
            const bool spare = kind ? r16 >= 12 : G == 3;
            unsigned sp2 = (kind && r16 != 12) ? 0u : 0x3F803F80u, sp3 = kind ? sp2 : 0x00003F80u; PIN(sp2); PIN(sp3);
            f32x4 d0 = {0.f, 0.f, 0.f, 0.f}, d1 = d0;
            bf16x8 xf[14], w0[14], w1[14];
#define KVR(c) do { wait_vm<1 + 39 - 2 * ((c) + 1) - (7 * (c) + 8) / 2>(); UNROLL for (int ks = 2 * (c); ks < 2 * (c) + 2; ++ks) { xf[ks] = ld128(((ks & 1) ? ax1 : ax0) + (c) * 8192); \
                        w0[ks] = ldtr2(aw0 + ks * 1792, aw0 + ks * 1792 + 224); w1[ks] = ldtr2(aw1 + ks * 1792, aw1 + ks * 1792 + 224); } SBAR(); } while (0)
#define KVM(c, K) do { UNROLL for (int ks = 2 * (c); ks < 2 * (c) + 2; ++ks) { \
                        if (K) { d0 = MFMA16(xf[ks], w0[ks], d0); d1 = MFMA16(xf[ks], w1[ks], d1); } else { d0 = MFMA16(w0[ks], xf[ks], d0); d1 = MFMA16(w1[ks], xf[ks], d1); } } SBAR(); } while (0)
#define KVP(K) do { KVR(0); STAMP(1); KVR(1); KVM(0, K); KVR(2); KVM(1, K); KVR(3); KVM(2, K); KVR(4); KVM(3, K); KVR(5); KVM(4, K); KVR(6); STAMP(32); KVM(5, K); KVM(6, K); } while (0)
            if (kind) KVP(1); else KVP(0);
            __builtin_amdgcn_raw_buffer_store_b64((u32x2){cvt2(d0[0], d0[1]), cvt2(d0[2], d0[3])}, rsWS, kva, tb, SC1);
            __builtin_amdgcn_raw_buffer_store_b64((u32x2){spare ? sp2 : cvt2(d1[0], d1[1]), spare ? sp3 : cvt2(d1[2], d1[3])}, rsWS, kvb, tb, SC1);
            asm volatile("buffer_atomic_add %0, %1, %2, 0 offen sc0 sc1" : "+v"(x1) : "v"(xoff), "s"(rsWS) : "memory");
            ladd(L, L_KD, 1u); ladd(L, L_XA, 1u);
            STAMP(12);
            QPREP();
            asm volatile("s_waitcnt vmcnt(0)" : "+v"(x0), "+v"(x1) :: "memory");
            STAMP(35);
            const unsigned s0 = __builtin_amdgcn_readfirstlane(x0); unsigned s1 = __builtin_amdgcn_readfirstlane(x1);
            _Pragma("unroll 1") for (unsigned spin = 0; s1 == s0 && spin < SPIN_CAP; ++spin) s1 = __builtin_amdgcn_readfirstlane(atomic_read(rsWS, xoff));
            atomic_inc(rsWS, lane < 8 ? WS_KVCNT + lane * 2048 + (b & 15) * 128 : OOB);
            BUMP();
            STAMP(13);
        } else {
            unsigned g0, g1, g2;
            const int gk = min((w - 1) * 192, 256) + lane;
            const uint16_t* gp = gate + gk; PIN(gp);
            const int ga = 2 * (w - 1) + (w - 1 >= tg ? 2 : 0);
            unsigned vxa = vx ^ (xswz(8 * ga) << 4), vxb = (vx ^ (xswz(8 * ga + 8) << 4)) + 8 * ROWB; const unsigned xs = (blk * 64 + 8 * ga) * ROWB; PIN(vxa); PIN(vxb);
            if (w == 1 && lane < 16) *(LAS unsigned*)(L + L_KD + lane * 4) = 0u;
            lds_bar();
            UNROLL for (int c = 0; c < 7; ++c) { dma(rsX, vxa, xs + c * 128, l0 + L_X + c * 8192 + ga * 1024); dma(rsX, vxb, xs + c * 128, l0 + L_X + c * 8192 + ga * 1024 + 1024); }
            asm volatile("global_load_ushort %0, %3, off\n\tglobal_load_ushort %1, %3, off offset:128\n\tglobal_load_ushort %2, %3, off offset:256"
                         : "=&v"(g0), "=&v"(g1), "=&v"(g2) : "v"(gp) : "memory");
            WQD(0, 6);
            QPREP();
            wait_vm<9>();
            lcount(L, L_XA, lane);
            WQD(6, 19);
            STAMP(14);
            asm volatile("s_waitcnt vmcnt(9)" : "+v"(g0), "+v"(g1), "+v"(g2) :: "memory");
            const lp gl = L + (L_WQ + gk * 128 + (112 ^ (((gk >> 1) & 1) << 6)));
            *(LAS uint16_t*)(gl) = (uint16_t)g0; *(LAS uint16_t*)(gl + 8192) = (uint16_t)g1; *(LAS uint16_t*)(gl + 16384) = (uint16_t)g2;
            lcount(L, L_XF, lane);
            STAMP(1);
        }
        lwait(L, L_XA, 4u); lwait(L, L_XF, 3u);
        STAMP(2);
        f32x16 qa = {}; bool kvok;
        { bf16x8 wf[28], xq[28];
          UNROLL for (int ks = 0; ks < 33; ++ks) {
            if (ks == WQ2) { STAMP(44); wait_vm<0>(); lcount(L, L_XG, lane); STAMP(45); }
            if (ks == 15) { lwait(L, L_XG, 4u); STAMP(46); }
            if (ks < 28) { wf[ks] = ldtr2(a1 + ks * 2048, a1 + ks * 2048 + 512); xq[ks] = ld128(((ks & 3) == 0 ? a00 : (ks & 3) == 1 ? a01 : (ks & 3) == 2 ? a02 : a03) + (ks >> 2) * 8192); }
            if (ks >= 5) qa = MFMA32(wf[ks - 5], xq[ks - 5], qa);
            if (ks == KPOLL) asm volatile("buffer_atomic_add %0, %1, %2, 0 offen sc0 sc1" : "+v"(kc) : "v"(kco), "s"(rsWS) : "memory");
            if (ks == KCHK) { asm volatile("s_waitcnt vmcnt(0)" : "+v"(kc) :: "memory"); kvok = __builtin_amdgcn_ballot_w64(kc != 16 && lane < 16) == 0; ladd(L, L_KR, w == 0 && kvok ? 1u : 0u); }
            if (ks == 28) ladd(L, L_QD, 1u);
            SBAR(); } }
        const unsigned kv0 = WS_KV + w * 65536;
        unsigned kvs = kv0;
#define LD2(A0, A1, off) asm volatile("buffer_load_dwordx4 %0, %2, %3, %4 offen offset:%5 sc1\n\tbuffer_load_dwordx4 %1, %2, %3, %4 offen offset:%6 sc1" \
                                      : "=&v"(A0), "=&v"(A1) : "v"(l16), "s"(rsWS), "s"(kvs), "i"(off), "i"((off) + 1024) : "memory")
#define WAIT2(N, A, B) asm volatile("s_waitcnt vmcnt(%2)" : "+v"(A), "+v"(B) : "i"(N) : "memory")
        STAMP(47);
#define KVOPEN() UNROLL for (int n = 0; n < 3; ++n) { if (n) kvs += 4096; LD2(kf[n][0], kf[n][1], 0); LD2(vf[n][0], vf[n][1], 2048); }
        const bool early = lread(L, L_KR) != 0u;
        if (early) { KVOPEN(); }
        STAMP(48);
        lwait(L, L_QD, 4u);
        asm volatile("s_nop 11" : "+v"(qa));
        const f32x2 cs = {0.18898223650461361f * 1.4426950408889634f, 0.f};
        u32x4 q0[4], q1[4];
        UNROLL for (int e = 0; e < 4; ++e) { const f32x2 a = pk_scale(cs, (f32x2){qa[2 * e], qa[2 * e + 1]}), c = pk_scale(cs, (f32x2){qa[8 + 2 * e], qa[9 + 2 * e]});
            q0[3][e] = cvt2(a[0], a[1]); q1[3][e] = cvt2(c[0], c[1]); }
        if (hi) { q1[3][2] = 0u; q1[3][3] = 0u; }
        *(LAS u32x4*)(qs3) = q0[3]; *(LAS u32x4*)(qs3 + 1024) = q1[3];
        if (hd == 1 && hi) *(LAS float*)(L + L_G + qrow * 4) = __builtin_amdgcn_rcpf(1.0f + fexp(-qa[12]));
        STAMP(3);
        lcount(L, L_QF, lane);
        if (w == 0 && !kvok) {
            _Pragma("unroll 1") for (unsigned spin = 0; spin < SPIN_CAP; ++spin) {
                STAMP(49); BUMP(); if (__builtin_amdgcn_ballot_w64(atomic_read(rsWS, kco) != 16 && lane < 16) == 0) break; }
            lcount(L, L_KR, lane);
        }
        if (!early) { lwait(L, L_KR, 1u); KVOPEN(); }
        STAMP(4);

        f32x16 sA, sB;
        lwait(L, L_QF, 4u);
        STAMP(5);
        q0[0] = *(LAS const u32x4*)(qs0); q1[0] = *(LAS const u32x4*)(qs0 + 1024); q0[1] = *(LAS const u32x4*)(qs1); q1[1] = *(LAS const u32x4*)(qs1 + 1024);
        q0[2] = *(LAS const u32x4*)(qs2); q1[2] = *(LAS const u32x4*)(qs2 + 1024);
        const float gt = *(LAS const float*)(L + L_G + qrow * 4);
        ladd(L, L_SF, 1u);
#define GIVE(O, RP, LP) do { *(LAS u32x4*)(RP) = pack8(O, 0); *(LAS u32x4*)((RP) + 1024) = pack8(O, 8); *(LAS float*)(LP) = O[12]; } while (0)
        WAIT2(10, kf[0][0], kf[0][1]);
        __builtin_amdgcn_s_setprio(0);
        STAMP(6);
        sA = MFMA32(asbf(kf[0][0]), asbf(q0[0]), (f32x16){}); sA = MFMA32(asbf(kf[0][1]), asbf(q1[0]), sA);
#define EXP(A, i) A[i] = __builtin_amdgcn_exp2f(A[i])
#define EX4(A, i) do { EXP(A, i); EXP(A, i + 1); EXP(A, i + 2); EXP(A, i + 3); PIN(A); } while (0)
#define UNIT(A, B, KN0, KN1, qn, q, qp, V0, VP1, NEXT, H0, H1, H3) do { SBAR(); H0; \
        if (NEXT) { B = MFMA32(asbf(KN0), asbf(q0[qn]), (f32x16){}); PIN(B); } SBAR(); \
        EX4(A, 0); SBAR(); \
        o[qp] = MFMA32(asbf(VP1), asbf(pw1), o[qp]); PIN(o[qp]); SBAR(); H1; \
        pw0[0] = cvt2(A[0], A[1]); pw0[1] = cvt2(A[2], A[3]); PIN(pw0); SBAR(); EX4(A, 4); SBAR(); \
        if (NEXT) { B = MFMA32(asbf(KN1), asbf(q1[qn]), B); PIN(B); } SBAR(); \
        pw0[2] = cvt2(A[4], A[5]); pw0[3] = cvt2(A[6], A[7]); PIN(pw0); SBAR(); EX4(A, 8); SBAR(); H3; \
        o[q] = MFMA32(asbf(V0), asbf(pw0), o[q]); PIN(o[q]); SBAR(); \
        { float e12 = __builtin_amdgcn_exp2f(A[12]), e13 = __builtin_amdgcn_exp2f(A[13]), e14 = __builtin_amdgcn_exp2f(A[14]), e15 = __builtin_amdgcn_exp2f(A[15]); \
          asm volatile("" : "+v"(e12), "+v"(e13), "+v"(e14), "+v"(e15)); SBAR(); \
          pw1[0] = cvt2(A[8], A[9]); pw1[1] = cvt2(A[10], A[11]); SBAR(); pw1[2] = cvt2(e12, e13); pw1[3] = cvt2(e14, e15); PIN(pw1); } SBAR(); } while (0)
#define TRIP(c, n, p, LD, NV, NK, NEXT) do { \
        UNIT(sA, sB, kf[c][0], kf[c][1], 1, 0, 3, vf[c][0], vf[p][1], true, {}, { if (LD) { kvs += 4096; LD2(kf[p][0], kf[p][1], 0); } }, WAIT2(NV, vf[c][0], vf[c][1])); \
        UNIT(sB, sA, kf[c][0], kf[c][1], 2, 1, 0, vf[c][0], vf[c][1], true, {}, { if (LD) LD2(vf[p][0], vf[p][1], 2048); }, {}); \
        UNIT(sA, sB, kf[c][0], kf[c][1], 3, 2, 1, vf[c][0], vf[c][1], true, {}, {}, {}); \
        UNIT(sB, sA, kf[n][0], kf[n][1], 0, 3, 2, vf[c][0], vf[c][1], NEXT, { if (NEXT) WAIT2(NK, kf[n][0], kf[n][1]); }, {}, {}); } while (0)
        _Pragma("unroll 1") for (int it = 0; it < 3; ++it) {
            TRIP(0, 1, 3, true, 10, 10, true); TRIP(1, 2, 0, true, 10, 10, true); TRIP(2, 3, 1, true, 10, 10, true); TRIP(3, 0, 2, true, 10, 10, true);
            STAMPV(17 + it);
        }
        TRIP(0, 1, 3, true, 10, 10, true);
        ladd(L, L_QL, 1u);
        TRIP(1, 2, 0, false, 8, 6, true); TRIP(2, 3, 1, false, 4, 2, true);
        ladd(L, L_QE, 1u);
        lwait(L, L_UF, 8u);
        UNIT(sA, sB, kf[3][0], kf[3][1], 1, 0, 3, vf[3][0], vf[2][1], true, {}, {}, WAIT2(0, vf[3][0], vf[3][1]));
        UNIT(sB, sA, kf[3][0], kf[3][1], 2, 1, 0, vf[3][0], vf[3][1], true, {}, {}, {});
        UNIT(sA, sB, kf[3][0], kf[3][1], 3, 2, 1, vf[3][0], vf[3][1], true, {}, GIVE(o[0], ro0, lw0), {});
        UNIT(sB, sA, kf[0][0], kf[0][1], 0, 3, 2, vf[3][0], vf[3][1], false, {}, GIVE(o[1], ro1, lw1), {});
        STAMPV(20);
        o[3] = MFMA32(asbf(vf[3][1]), asbf(pw1), o[3]); PIN(o[3]); SBAR();
        GIVE(o[2], ro2, lw2); SBAR();
        STAMP(7);

        float l3 = o[3][12];
        *(LAS float*)(lw3) = l3;
        {   const unsigned u0 = __float_as_uint(o[0][12]), u1 = __float_as_uint(o[1][12]), u2 = __float_as_uint(o[2][12]), u3 = __float_as_uint(l3);
            const unsigned lo = min(min(u0, u1), min(u2, u3)), up = max(max(u0, u1), max(u2, u3));
            const bool bad = __builtin_amdgcn_ballot_w64(hi && (lo < 0x1F800000u || up > 0x5D800000u)) != 0;
            ladd(L, L_GV, bad ? 0x10001u : 1u); }
        unsigned gv = lread(L, L_GV);
        if (__builtin_expect((gv & 0xffffu) < 4u, 0)) { _Pragma("unroll 1") for (unsigned spin = 0; (gv & 0xffffu) < 4u && spin < (1u << 20); ++spin) gv = lread(L, L_GV); }
        asm volatile("" ::: "memory");
        STAMP(15);
        bf16x8 f[6]; f32x4 m0;
        UNROLL for (int t = 0; t < 6; ++t) f[t] = ld128(rop + (t >> 1) * 8192 + (t & 1) * 1024);
        m0 = *(LAS const f32x4*)(lsp);
        SBAR();
        if (__builtin_expect(gv > 0xffffu, 0)) {
            auto qbar = [&](unsigned n) __attribute__((always_inline)) { asm volatile("s_waitcnt lgkmcnt(0)" ::: "memory"); ladd(L, L_FB, 1u); lwait(L, L_FB, n); };
            float mx[4] = {-INFINITY, -INFINITY, -INFINITY, -INFINITY};
            qbar(4u);
            _Pragma("unroll 1") for (int t = 0; t < 16; ++t) {
                const bf16x8 a = asbf(__builtin_amdgcn_raw_buffer_load_b128(rsWS, l16, kv0 + t * 4096, SC1));
                const bf16x8 c = asbf(__builtin_amdgcn_raw_buffer_load_b128(rsWS, l16, kv0 + t * 4096 + 1024, SC1));
                UNROLL for (int q = 0; q < 4; ++q) { f32x16 s = MFMA32(a, asbf(q0[q]), (f32x16){}); s = MFMA32(c, asbf(q1[q]), s); mx[q] = fmaxf(mx[q], rowmax16(s)); SBAR(); }
            }
            *(LAS float*)(lw0) = mx[0]; *(LAS float*)(lw1) = mx[1]; *(LAS float*)(lw2) = mx[2]; *(LAS float*)(lw3) = mx[3];
            qbar(8u);
            UNROLL for (int q = 0; q < 4; ++q) { const f32x4 n0 = *(LAS const f32x4*)(L + (lsb ^ (((q + 1) & 3) * 512)));
                set_shift(q1[q], fmaxf(fmaxf(n0[0], n0[1]), fmaxf(n0[2], n0[3])), hi); }
            qbar(12u);
            UNROLL for (int q = 0; q < 4; ++q) {
                f32x16 acc = {};
                _Pragma("unroll 1") for (int t = 0; t < 16; ++t) {
                    const bf16x8 a = asbf(__builtin_amdgcn_raw_buffer_load_b128(rsWS, l16, kv0 + t * 4096, SC1));
                    const bf16x8 c = asbf(__builtin_amdgcn_raw_buffer_load_b128(rsWS, l16, kv0 + t * 4096 + 1024, SC1));
                    const bf16x8 d = asbf(__builtin_amdgcn_raw_buffer_load_b128(rsWS, l16, kv0 + t * 4096 + 2048, SC1));
                    const bf16x8 e = asbf(__builtin_amdgcn_raw_buffer_load_b128(rsWS, l16, kv0 + t * 4096 + 3072, SC1));
                    f32x16 s = MFMA32(a, asbf(q0[q]), (f32x16){}); s = MFMA32(c, asbf(q1[q]), s);
                    UNROLL for (int r = 0; r < 16; ++r) s[r] = __builtin_amdgcn_exp2f(s[r]);
                    acc = MFMA32(d, asbf(pack8(s, 0)), acc); acc = MFMA32(e, asbf(pack8(s, 8)), acc);
                }
                if (q == 0) { GIVE(acc, ro0, lw0); } else if (q == 1) { GIVE(acc, ro1, lw1); } else if (q == 2) { GIVE(acc, ro2, lw2); } else { *(LAS float*)(lw3) = acc[12]; o[3] = acc; }
            }
            qbar(16u);
            UNROLL for (int t = 0; t < 6; ++t) f[t] = ld128(rop + (t >> 1) * 8192 + (t & 1) * 1024);
            m0 = *(LAS const f32x4*)(lsp);
        }
        __builtin_amdgcn_s_setprio(1);
        f32x16 t = o[3];
        UNROLL for (int k = 0; k < 6; ++k) t = MFMA32(asbf((k & 1) ? sel1 : sel0), f[k], t);
        {
            float s0 = m0[0] + m0[1], s2 = m0[2] + m0[3]; PIN(s0); PIN(s2);
            f32x2 sc; sc[0] = sc[1] = gt * __builtin_amdgcn_rcpf(s0 + s2); PIN(sc);
            u32x4 oa, ob;
            UNROLL for (int e = 0; e < 4; ++e) { const f32x2 a = sc * (f32x2){t[2 * e], t[2 * e + 1]}; oa[e] = nosent(cvt2(a[0], a[1])); }
            st128(oa, rsWS, l16, opiece, SC1);
            SBAR();
            UNROLL for (int e = 0; e < 4; ++e) { const f32x2 a = sc * (f32x2){t[8 + 2 * e], t[9 + 2 * e]}; ob[e] = nosent(cvt2(a[0], a[1])); }
            if (hi) { ob[2] = 0u; ob[3] = 0u; }
            st128(ob, rsWS, l16, opiece + 1024, SC1);
        }
        __builtin_amdgcn_s_setprio(0);
        STAMP(8);
        const int tok = blk * 64 + qrow, col = 56 * j + 32 * hd + 4 * hi;
        const rsrc_t rsD = mkrs(dskip, ROWB), rsO = mkrs(out, SEQ * DM * 4);
        if (ODRAIN) wait_vm<0>();
        ladd(L, L_OD, 1u);
        f32x4 r[4]; u32x2 xv[4], dv[4];
        UNROLL for (int e = 0; e < 4; ++e) { xv[e] = __builtin_amdgcn_raw_buffer_load_b64(rsX, tok * ROWB + col * 2 + e * 16, 0, 0); dv[e] = __builtin_amdgcn_raw_buffer_load_b64(rsD, col * 2 + e * 16, 0, 0); }
        SBAR();
        UNROLL for (int e = 0; e < 4; ++e) {
            float t0 = bf2f(xv[e][0]) * bf2f(dv[e][0]), t1 = bfhi(xv[e][0]) * bfhi(dv[e][0]), t2 = bf2f(xv[e][1]) * bf2f(dv[e][1]), t3 = bfhi(xv[e][1]) * bfhi(dv[e][1]);
            PIN(t0); PIN(t1); PIN(t2); PIN(t3); t0 *= 1.0f - gt; t1 *= 1.0f - gt; t2 *= 1.0f - gt; t3 *= 1.0f - gt; PIN(t0); PIN(t1); PIN(t2); PIN(t3);
            r[e] = (f32x4){t0, t1, t2, t3}; }
        const lp rp = lbase(L, L_RED + hd * 32768 + th * 16384 + l16);
        unsigned oo = (tok * DM + col) * 4, o3 = hd ? OOB : oo + 96; PIN(oo); PIN(o3);
        lds_bar();
        STAMP(10);
        UNROLL for (int e = 0; e < 4; ++e) {
            r[e] += (*(LAS const f32x4*)(rp + e * 1024) + *(LAS const f32x4*)(rp + 4096 + e * 1024)) + (*(LAS const f32x4*)(rp + 8192 + e * 1024) + *(LAS const f32x4*)(rp + 12288 + e * 1024));
            st128(__builtin_bit_cast(u32x4, r[e]), rsO, e < 3 ? oo + e * 32 : o3, 0, 0); }
    } else {
        const int u = w - 4, st = min(16 * u + r16, 55);
        const unsigned eoff = (G == 0 && 16 * u + r16 < 56) ? st * 8 : OOB;
        const rsrc_t rsBR = mkrs(bre, DM * ROWB), rsBI = mkrs(bim, DM * ROWB);
        const unsigned vb0_ = (56 * j + 16 * u + r8) * ROWB + ((sl ^ (r8 >> 1)) << 4), vb1_ = u < 3 ? (vb0_ ^ 64) + 8 * ROWB : OOB;
#define DST2(a) (u < 3 ? (a) : l0 + L_DUMMY)
        unsigned p0, p1, p2;
        const uint16_t *a0 = lre + 56 * j + st, *a1 = lim + 56 * j + st, *a2 = logdt + 56 * j + st; PIN(a0); PIN(a1); PIN(a2);
#define TOUCH(ptr, bytes, vo) do { const rsrc_t r_ = mkrs(ptr, bytes); \
            asm volatile("s_mov_b32 m0, %0\n\ts_nop 0\n\tbuffer_load_dword %1, %2, 0 offen lds" :: "s"(l0 + L_DUMMY), "v"(vo), "s"(r_) : "memory"); } while (0)
        if (PFE == 1) { if (u < 3) TOUCH(u == 0 ? wq : u == 1 ? bre : bim, DM * ROWB, lane < 25 ? (b * 25 + lane) * 64 : OOB); else TOUCH(x, SEQ * ROWB, lane < 56 ? (blk * 448 + j * 56 + lane) * 128 : OOB); }
        const bool master = b == 0 && w == 7;
        if (master) {
            __builtin_amdgcn_raw_buffer_store_b32(0u, rsWS, lane * 128, 0, SC1);
            __builtin_amdgcn_raw_buffer_store_b32(0u, rsWS, lane * 128 + 8192, 0, SC1);
            __builtin_amdgcn_raw_buffer_store_b32(0u, rsWS, lane < 32 ? lane * 128 + 16384 : OOB, 0, SC1);
        }
        asm volatile("s_barrier" ::: "memory");
        if (master) { wait_vm<0>(); BUMP(); }
        if (PFE == 0) { if (u < 3) TOUCH(u == 0 ? wq : u == 1 ? bre : bim, DM * ROWB, lane < 25 ? (b * 25 + lane) * 64 : OOB); }
        asm volatile("global_load_ushort %0, %3, off\n\tglobal_load_ushort %1, %4, off\n\tglobal_load_ushort %2, %5, off"
                     : "=&v"(p0), "=&v"(p1), "=&v"(p2) : "v"(a0), "v"(a1), "v"(a2) : "memory");
#define BOFF(c) ((c) % 3 == 0 ? L_B0 : L_WK + ((c) % 3 - 1) * 14336)
#define BD(c) do { dma(rsBR, vb0_, (c) * 128, l0 + BOFF(c) + u * 2048); dma(rsBI, vb0_, (c) * 128, l0 + BOFF(c) + 7168 + u * 2048); \
                   dma(rsBR, vb1_, (c) * 128, DST2(l0 + BOFF(c) + u * 2048 + 1024)); dma(rsBI, vb1_, (c) * 128, DST2(l0 + BOFF(c) + 7168 + u * 2048 + 1024)); } while (0)
        STAMP(14);
        asm volatile("s_waitcnt vmcnt(0)" : "+v"(p0), "+v"(p1), "+v"(p2) :: "memory");
        if (master) BUMP();
        f32x2 lam, coef; cf lam8, lam16, lam64, wlo, whi, zG;
        {   const float lr = bf2f(p0), li = bf2f(p1), dt = bfround(fexp(bf2f(p2)));
            const float er = fexp(dt * lr), rev = __builtin_amdgcn_fractf(dt * li * 0.15915494309189535f);
            const cf l1 = {er * __builtin_amdgcn_cosf(rev), er * __builtin_amdgcn_sinf(rev)};
            const float inv = __builtin_amdgcn_rcpf(lr * lr + li * li);
            const cf cq = cmul((cf){l1.r - 1.0f, l1.i}, (cf){lr * inv, -li * inv});
            lam = (f32x2){l1.r, l1.i}; coef = (f32x2){cq.r, cq.i};
            const cf l2 = cmul(l1, l1), l4 = cmul(l2, l2); lam8 = cmul(l4, l4); lam16 = cmul(lam8, lam8);
            const cf l32 = cmul(lam16, lam16); lam64 = cmul(l32, l32);
            const cf l128 = cmul(lam64, lam64), l256 = cmul(l128, l128), l512 = cmul(l256, l256), l1024 = cmul(l512, l512), one = {1.f, 0.f};
            wlo = cmul((G & 1) ? lam16 : one, (G & 2) ? l32 : one); whi = cmul(wlo, lam8);
            zG = cmul((G & 1) ? l512 : one, (G & 2) ? l1024 : one);
            asm volatile("" : "+v"(lam), "+v"(coef), "+v"(lam8.r), "+v"(lam8.i), "+v"(lam16.r), "+v"(lam16.i), "+v"(lam64.r), "+v"(lam64.i),
                              "+v"(wlo.r), "+v"(wlo.i), "+v"(whi.r), "+v"(whi.i), "+v"(zG.r), "+v"(zG.i) :: "memory"); }
        const int rr = u < 3 ? r16 : r16 & 7;
        const unsigned bro = u * 2048 + rr * 128 + ((G ^ (rr >> 1)) << 4);
        const lp br0 = lbase(L, L_WK + bro), br1 = lbase(L, L_WK + (bro ^ 64)), br2 = lbase(L, L_B0 + bro), br3 = lbase(L, L_B0 + (bro ^ 64));
        lp at[4][2];
        UNROLL for (int m = 0; m < 4; ++m) { const int row = 16 * (r16 >> 2) + 2 * m + ((r16 & 3) >> 1) + 8 * (r16 & 1); const unsigned a = L_X + row * 128 + ((G ^ xswz(row)) << 4);
            at[m][0] = lbase(L, a); at[m][1] = lbase(L, a ^ 64); }
        if (master) { _Pragma("unroll 1") for (unsigned spin = 0; lread(L, L_KD) < 1u && spin < SPIN_CAP; ++spin) { BUMP(); __builtin_amdgcn_s_sleep(8); } BUMP(); wait_vm<0>(); }
        if (B0EARLY) BD(0);
        lwait(L, L_KD, 1u);
        STAMP(1);
        if (!B0EARLY) BD(0);
        BD(1); BD(2);
        f32x4 ur[4] = {}, ui[4] = {};
        bf16x8 fb[2][4], fx[2][8];
#define BP(c, k) ((c) % 3 == 0 ? ((k) ? br3 : br2) : ((k) ? br1 : br0) + ((c) % 3 - 1) * 14336)
#define BRD(c) do { wait_vm<(c) < 5 ? 8 : 4 * (6 - (c))>(); fb[(c) & 1][0] = ld128(BP(c, 0)); fb[(c) & 1][1] = ld128(BP(c, 0) + 7168); fb[(c) & 1][2] = ld128(BP(c, 1)); fb[(c) & 1][3] = ld128(BP(c, 1) + 7168); \
                    if ((c) < 4) { asm volatile("s_waitcnt lgkmcnt(0)" ::: "memory"); BD((c) + 3); } } while (0)
#define XRD(c, i) fx[(c) & 1][i] = ld128(at[(i) & 3][(i) >> 2] + (c) * 8192)
#define UCH(c) UNROLL for (int i = 0; i < 8; ++i) { \
            if (i == 0 && (c) < 6) BRD((c) < 6 ? (c) + 1 : 6); \
            ur[i & 3] = MFMA16(fx[(c) & 1][i], fb[(c) & 1][2 * (i >> 2)], ur[i & 3]); ui[i & 3] = MFMA16(fx[(c) & 1][i], fb[(c) & 1][2 * (i >> 2) + 1], ui[i & 3]); \
            if ((c) < 6) XRD((c) + 1, i); \
            SBAR(); }
        BRD(0);
        lwait(L, L_XA, 4u);
        STAMP(2);
        UNROLL for (int i = 0; i < 8; ++i) XRD(0, i);
        SBAR();
        UCH(0) UCH(1) UCH(2)
        if (u < 3) TOUCH(u == 0 ? wo : u == 1 ? cre : cim, DM * ROWB, lane < 25 ? (b * 25 + lane) * 64 : OOB);
        __builtin_amdgcn_s_setprio(UPRIO);
        UCH(3) UCH(4) UCH(5) UCH(6)
        lcount(L, L_UF, lane);
        asm volatile("s_nop 7" : "+v"(ur[0]), "+v"(ur[1]), "+v"(ur[2]), "+v"(ur[3]), "+v"(ui[0]), "+v"(ui[1]), "+v"(ui[2]), "+v"(ui[3]));
        STAMP(3);
        f32x2 hr, hm;
        UNROLL for (int p = 0; p < 8; ++p) {
            f32x2 tr, ti; pk_cmul(tr, ti, coef, PAIR(ur, p), PAIR(ui, p));
            ur[p >> 1][2 * (p & 1)] = tr[0]; ur[p >> 1][2 * (p & 1) + 1] = tr[1]; ui[p >> 1][2 * (p & 1)] = ti[0]; ui[p >> 1][2 * (p & 1) + 1] = ti[1];
            if (p == 0) { hr = tr; hm = ti; } else { f32x2 nr, ni; pk_cfma(nr, ni, lam, hr, hm, tr, ti); hr = nr; hm = ni; }
        }
        const cf elo = {hr[0], hm[0]}, e16 = cfma(lam8, elo, (cf){hr[1], hm[1]});
        cf e0, e1, e2, e3; bcast4(e16.r, e0.r, e1.r, e2.r, e3.r); bcast4(e16.i, e0.i, e1.i, e2.i, e3.i);
        const cf t2 = cfma(lam16, e0, e1), t3 = cfma(lam16, t2, e2), eb = cfma(lam16, t3, e3);
        lwait(L, L_KR, 1u); lwait(L, L_QF, 4u);
        __builtin_amdgcn_raw_buffer_store_b64((u32x2){nosent(__float_as_uint(eb.r)), nosent(__float_as_uint(eb.i))}, rsWS, eoff, WS_E + (blk * DM + 56 * j) * 8, SC1);
        const cf pg = {G == 0 ? 0.f : G == 1 ? e0.r : G == 2 ? t2.r : t3.r, G == 0 ? 0.f : G == 1 ? e0.i : G == 2 ? t2.i : t3.i}, ph = cfma(lam8, pg, elo);
        STAMP(4);
        asm volatile("s_mov_b32 m0, %0\n\ts_nop 0\n\tbuffer_load_dword %1, %2, %3 offen sc1 lds"
                     :: "s"(l0 + L_DUMMY), "v"(u == 0 ? lane * 128 : OOB), "s"(rsWS), "s"(WS_KV + (b >> 3) * 8192) : "memory");
        st128((u32x4){~0u, ~0u, ~0u, ~0u}, rsWS, l16, opiece, SC1);
        st128((u32x4){~0u, ~0u, ~0u, ~0u}, rsWS, l16, opiece + 1024, SC1);
        const float gt = *(LAS const float*)(L + L_G + (32 * th + r32) * 4), go = *(LAS const float*)(L + L_G + (32 * (th ^ 1) + r32) * 4);
        lwait(L, L_UF, 4u);
        {   const rsrc_t rsCR = mkrs(cre, DM * ROWB), rsCI = mkrs(cim, DM * ROWB);
            const unsigned vc = (56 * j + 8 * u + r8) * ROWB + ((sl ^ ((4 * u + (r8 >> 1)) & 7)) << 4), vc2 = u < 3 ? vc + 32 * ROWB : OOB;
            UNROLL for (int cc = 0; cc < 7; ++cc) {
                dma(rsCR, vc, cc * 128, l0 + L_C + cc * 7168 + u * 1024); PACE(); dma(rsCI, vc, cc * 128, l0 + L_CIM + cc * 7168 + u * 1024); PACE();
                dma(rsCR, vc2, cc * 128, DST2(l0 + L_C + cc * 7168 + (u + 4) * 1024)); PACE(); dma(rsCI, vc2, cc * 128, DST2(l0 + L_CIM + cc * 7168 + (u + 4) * 1024)); PACE(); } }
        const rsrc_t rsE = mkrs(ws + WS_E + 56 * j * 8, 32 * DM * 8 - 56 * j * 8);
        const int ebase = (blk - 1 - 8 * G) * (DM * 8) + st * 8;
        const unsigned hoff = lane == 0 ? WS_HCNT + blk * 128 : OOB;
        STAMP(38);
        {
            u32x2 ev[8];
            _Pragma("unroll 1") for (unsigned spin = 0; spin < SPIN_CAP; ++spin) {
                asm volatile("" ::: "memory");
                UNROLL for (int m = 0; m < 8; ++m) ev[m] = __builtin_amdgcn_raw_buffer_load_b64(rsE, ebase - m * (DM * 8), 0, SC1);
                unsigned mx = 0;
                UNROLL for (int m = 0; m < 8; ++m) mx = max(mx, max(ev[m][0], ev[m][1]));
                if (__builtin_amdgcn_ballot_w64(mx == ~0u) == 0) break;
                __builtin_amdgcn_s_sleep(8);
            }
            STAMP(39);
            cf part = {__uint_as_float(ev[7][0]), __uint_as_float(ev[7][1])};
            UNROLL for (int m = 6; m >= 0; --m) part = cfma(lam64, part, (cf){__uint_as_float(ev[m][0]), __uint_as_float(ev[m][1])});
            part = cmul(zG, part);
            if (ebase < 0) part = (cf){0.f, 0.f};
            part.r = sum4(part.r); part.i = sum4(part.i);
            const cf ilo = cfma(wlo, part, pg), ihi = cfma(whi, part, ph);
            hr = (f32x2){ilo.r, ihi.r}; hm = (f32x2){ilo.i, ihi.i};
            u32x4 pr0, pr1, pi0, pi1;
            UNROLL for (int p = 0; p < 8; ++p) {
                f32x2 nr, ni; pk_cfma(nr, ni, lam, hr, hm, PAIR(ur, p), PAIR(ui, p)); hr = nr; hm = ni;
                unsigned cr, ci; asm("v_cvt_pk_bf16_f32 %0, %2, %3\n\tv_cvt_pk_bf16_f32 %1, -%4, -%5" : "=&v"(cr), "=&v"(ci) : "v"(hr[0]), "v"(hr[1]), "v"(hm[0]), "v"(hm[1]));
                if (p < 4) { pr0[p & 3] = cr; pi0[p & 3] = ci; } else { pr1[p & 3] = cr; pi1[p & 3] = ci; }
            }
            const unsigned sb = L_HS + u * 4096, wa = sb + r16 * 128 + (((2 * G) ^ (r16 & 7)) << 4);
            const int tok = (lane & 48) + (r16 >> 1) + 8 * (lane & 1);
            const unsigned gha = (tok >> 5) * 57344 + (tok & 31) * 16, ghb = u < 3 ? gha + 1024 : OOB, gs = WS_H + blk * 114688 + (7 * j + 2 * u) * 1024;
            const unsigned rao = sb + tq * 128 + (((2 * G + (tp >> 1)) ^ tq) << 4) + 8 * (tp & 1);
            const lp wa0 = lbase(L, wa), wa1 = lbase(L, wa ^ 16), ra = lbase(L, rao), rb = lbase(L, (rao ^ 64) + 512);
            *(LAS u32x4*)(wa0) = pr0; *(LAS u32x4*)(wa1) = pr1; *(LAS u32x4*)(wa0 + 2048) = pi0; *(LAS u32x4*)(wa1 + 2048) = pi1;
            asm volatile("" ::: "memory");
            UNROLL for (int pl = 0; pl < 2; ++pl) {
                const bf16x8 c0 = ldtr2(ra + pl * 2048, rb + pl * 2048), c1 = ldtr2(ra + pl * 2048 + 1024, rb + pl * 2048 + 1024);
                st128(__builtin_bit_cast(u32x4, c0), rsWS, gha + pl * 512, gs, SC1);
                st128(__builtin_bit_cast(u32x4, c1), rsWS, ghb + pl * 512, gs, SC1);
            }
        }
        STAMP(5);
        lcount(L, L_SF, lane);
        wait_vm<0>();
        atomic_inc(rsWS, hoff);
        lwait(L, L_SF, 8u);
        { const rsrc_t rsWO = mkrs(wo, DM * ROWB);
          UNROLL for (int n = 0; n < 14; ++n) { dma(rsWO, vw, (u * 14 + n) * 8 * ROWB, l0 + L_WO + (u * 14 + n) * 1024); PACE(); } }
        STAMP(40);
        _Pragma("unroll 1") for (unsigned spin = 0; __builtin_amdgcn_readfirstlane(atomic_read(rsWS, hoff)) != 32 && spin < SPIN_CAP; ++spin) __builtin_amdgcn_s_sleep(8);
        lcount(L, L_WF, lane);
        const unsigned hb = WS_H + blk * 114688 + u * 1024;
        u32x4 h0[14], h1[14];
        const unsigned cx = (32 * (G & 1) + 8 * tp) ^ (((tq >> 1) & 1) << 6), nb = L_WO + (112 * u + tq) * 128, wa_ = nb + hi * 512;
        const lp wa0_ = lbase(L, wa_ + cx), wa1_ = lbase(L, wa_ + (cx ^ 64)), nb0 = lbase(L, nb + cx), nb1 = lbase(L, nb + (cx ^ 64));
        lwait(L, L_WF, 4u);
        bf16x8 wf0[8], wf1[8];
        UNROLL for (int k = 0; k < 8; ++k) { const unsigned ro = (28 * (k >> 1) + 16 * (k & 1)) * 128;
            wf0[k] = ldtr2(wa0_ + ro, ((k & 1) ? nb0 : wa0_) + ro + 1024); wf1[k] = ldtr2(wa1_ + ro, ((k & 1) ? nb1 : wa1_) + ro + 1024); }
        lcount(L, L_UF, lane);
        STAMP(42);
        UNROLL for (int m = 0; m < 14; ++m) { h0[m] = __builtin_amdgcn_raw_buffer_load_b128(rsWS, l16, hb + th * 57344 + m * 4096, SC1); PACE(); }
        lwait_idle(L, L_QL, 4u);
        UNROLL for (int m = 0; m < 14; ++m) h1[m] = __builtin_amdgcn_raw_buffer_load_b128(rsWS, l16, hb + (th ^ 1) * 57344 + m * 4096, SC1);
        const int rbc = min(32 + r32, 55);
        const unsigned cao = hi * L_CIM + r32 * 128 + ((u ^ ((r32 >> 1) & 7)) << 4), cbo = hi * L_CIM + rbc * 128 + ((u ^ ((rbc >> 1) & 7)) << 4);
        const lp ca0 = lbase(L, cao), ca1 = lbase(L, cao ^ 64), cb0 = lbase(L, cbo), cb1 = lbase(L, cbo ^ 64);
        const lp rp = lbase(L, L_RED + th * 16384 + u * 4096 + l16), rq = lbase(L, L_RED + (th ^ 1) * 16384 + u * 4096 + l16);
        const unsigned oa = WS_O + blk * 65536 + (th * 16 + 4 * u) * 2048, ob = WS_O + blk * 65536 + ((th ^ 1) * 16 + 4 * u) * 2048;
        f32x2 s2 = {2.0f * (1.0f - gt), 0.f}, s3 = {2.0f * (1.0f - go), 0.f}; PIN(s2); PIN(s3);
        STAMP(41);
        __builtin_amdgcn_s_setprio(0);
        lwait_idle(L, L_QE, 4u);
        __builtin_amdgcn_s_sleep(YSLEEP);
        STAMP(15);
        f32x16 y[4]; y[0] = y[1] = y[2] = y[3] = (f32x16){};
        u32x4 of[16];
#define OLD(k0) UNROLL for (int k = (k0); k < (k0) + 8; ++k) of[k] = __builtin_amdgcn_raw_buffer_load_b128(rsWS, l16 + (k & 3) * 1024, (k < 8 ? oa : ob) + ((k >> 2) & 1) * 4096, SC1)
        {   bf16x8 fa[28], fc[28];
            UNROLL for (int m = -YD; m < 28; ++m) {
                if (m + YD < 28) { const int n = (m + YD) % 14; fa[m + YD] = ld128(((n & 1) ? ca1 : ca0) + (n >> 1) * 7168); fc[m + YD] = ld128(((n & 1) ? cb1 : cb0) + (n >> 1) * 7168); }
                if (m >= 0 && m < 14) { y[0] = MFMA32(fa[m], asbf(h0[m]), y[0]); y[1] = MFMA32(fc[m], asbf(h0[m]), y[1]); }
                if (m >= 14) { y[2] = MFMA32(fa[m], asbf(h1[m - 14]), y[2]); y[3] = MFMA32(fc[m], asbf(h1[m - 14]), y[3]); }
                if (m >= 16 && m < 24) { UNROLL for (int k = 0; k < 2; ++k) { const f32x2 a = pk_scale(s2, (f32x2){y[k][2 * (m - 16)], y[k][2 * (m - 16) + 1]}); y[k][2 * (m - 16)] = a[0]; y[k][2 * (m - 16) + 1] = a[1]; } PIN(y[0]); PIN(y[1]); }
                if (m == 26) ladd(L, L_YF, 1u);
                SBAR();
            } }
        STAMP(9);
        lwait(L, L_YF, 4u);
        asm volatile("s_nop 11" : "+v"(y[2]), "+v"(y[3]));
        UNROLL for (int k = 2; k < 4; ++k) UNROLL for (int e = 0; e < 8; ++e) { const f32x2 a = pk_scale(s3, (f32x2){y[k][2 * e], y[k][2 * e + 1]}); y[k][2 * e] = a[0]; y[k][2 * e + 1] = a[1]; }
        asm volatile("s_nop 1" : "+v"(y[2]), "+v"(y[3]));
        lwait(L, L_OD, ODN);
        if (OSLEEP) __builtin_amdgcn_s_sleep(OSLEEP);
        STAMP(43);
        OLD(0); OLD(8);
#define OCHK(k0) do { unsigned mx = 0; UNROLL for (int k = (k0); k < (k0) + 8; ++k) mx = max(max(mx, max(of[k][0], of[k][1])), max(of[k][2], of[k][3])); \
        if (__builtin_expect(__builtin_amdgcn_ballot_w64(mx == ~0u) != 0, 0)) { _Pragma("unroll 1") for (unsigned spin = 0; spin < SPIN_CAP; ++spin) { \
            asm volatile("" ::: "memory");                              \
            OLD(k0); mx = 0; UNROLL for (int k = (k0); k < (k0) + 8; ++k) mx = max(max(mx, max(of[k][0], of[k][1])), max(of[k][2], of[k][3])); \
            if (__builtin_amdgcn_ballot_w64(mx == ~0u) == 0) break; } } } while (0)
        SBAR();
        OCHK(0);
        STAMP(10);
#define REDW(p, Y, e) *(LAS f32x4*)((p) + (e) * 1024) = (f32x4){Y[4 * (e)], Y[4 * (e) + 1], Y[4 * (e) + 2], Y[4 * (e) + 3]}
        UNROLL for (int k = 0; k < 8; ++k) y[0] = MFMA32(wf0[k], asbf(of[k]), y[0]);
        PIN(y[0]); SBAR();
        UNROLL for (int k = 0; k < 8; ++k) { y[1] = MFMA32(wf1[k], asbf(of[k]), y[1]); SBAR(); if (k >= 3 && k < 7) REDW(rp, y[0], k - 3); SBAR(); }
        OCHK(8);
        UNROLL for (int k = 0; k < 8; ++k) { y[2] = MFMA32(wf0[k], asbf(of[8 + k]), y[2]); SBAR(); if (k >= 3 && k < 7) REDW(rp + 32768, y[1], k - 3); SBAR(); }
        UNROLL for (int k = 0; k < 8; ++k) { y[3] = MFMA32(wf1[k], asbf(of[8 + k]), y[3]); SBAR(); if (k >= 3 && k < 7) REDW(rq, y[2], k - 3); SBAR(); }
        UNROLL for (int e = 0; e < 4; ++e) REDW(rq + 32768, y[3], e);
        lds_bar();
    }
    STAMP(11);
}

extern "C" void kernel_launch(void* const* d_in, const int* in_sizes, int n_in, void* d_out, int out_size, void* d_ws, size_t ws_size,
                              hipStream_t stream) {
    const uint16_t* const* in = (const uint16_t* const*)d_in;
    gated_mix_kernel<<<256, 512, 0, stream>>>(in[0], in[1], in[2], in[3], in[4], in[6], in[7], in[8], in[9], in[10], in[11], in[12], in[13], in[14],
                                              (float*)d_out, (char*)d_ws);
}
```

```cpp
#include <hip/hip_runtime.h>
#include <hip/hip_bf16.h>
#include <stdint.h>

typedef __bf16 bf16x8 __attribute__((ext_vector_type(8)));
typedef __bf16 bf16x2 __attribute__((ext_vector_type(2)));
typedef float f32x16 __attribute__((ext_vector_type(16)));
typedef float f32x4 __attribute__((ext_vector_type(4)));
typedef float f32x2 __attribute__((ext_vector_type(2)));
typedef unsigned u32x4 __attribute__((ext_vector_type(4)));
typedef unsigned u32x2 __attribute__((ext_vector_type(2)));
typedef short s16x4 __attribute__((ext_vector_type(4)));
typedef __amdgpu_buffer_rsrc_t rsrc_t;
#define LAS __attribute__((address_space(3)))
#define DI __device__ __forceinline__
#define SBAR() __builtin_amdgcn_sched_barrier(0)
#define PIN(x) asm volatile("" : "+v"(x))
#define PINS(x) asm volatile("" : "+s"(x))
#define MFMA32(a, b, c) __builtin_amdgcn_mfma_f32_32x32x16_bf16((a), (b), (c), 0, 0, 0)
#define MFMA16(a, b, c) __builtin_amdgcn_mfma_f32_16x16x32_bf16((a), (b), (c), 0, 0, 0)
#define UNROLL _Pragma("unroll")
#define PACE() asm volatile("s_sleep %0" :: "i"(CSLEEP) : "memory")
#define SC1 16

#define CSLEEP 2
#define YSLEEP 8
#define CPREF 32768
#define WQ2 8
#define YD 3
#define OSLEEP 6
#define PACKED 1
#define ODN 4u
#define PFC 0
#define B0EARLY 0
#define KCHK 29
#define PFE 0
#define ODRAIN 0
#define ELDN 15
#define QD_ 7
#define OM 28
#define BRI 3
#define TCW 0
#define LSLEEP 0
#define REGB 1
#define QREP 2
#define KPOLL 16
#define UPRIO 1
#define SPRIO 0
#define OM0 16
#define OM1 22
#define MAP42 0

constexpr int SEQ = 2048, DM = 448, ROWB = DM * 2;
constexpr unsigned WS_KVCNT = 0;
constexpr unsigned WS_HCNT = 16384;
constexpr unsigned WS_X = 20480;
constexpr unsigned OOB = 0x7FFFFFF0u;
constexpr unsigned WS_KV = 65536;
constexpr unsigned WS_E = WS_KV + 262144;
constexpr unsigned WS_H = WS_E + 131072;
constexpr unsigned WS_O = WS_H + 32 * 114688;
constexpr unsigned WS_END = WS_O + 32 * 65536;
constexpr unsigned WS_STAMP = WS_END;

constexpr unsigned L_X = 0, L_WK = 57344, L_WQ = 86016, L_B0 = 143360, L_G = 160768;
constexpr unsigned L_KD = 161024, L_XA = 161028, L_XF = 161060, L_QD = 161032, L_QF = 161036, L_UF = 161040, L_WF = 161044, L_KR = 161048, L_YF = 161052, L_XG = 161064, L_GV = 161068, L_QE = 161072, L_SF = 161076, L_QL = 161080, L_OD = 161056, L_FB = 161084;
constexpr unsigned L_QSH = 122880, L_HS = 106496;
constexpr unsigned L_C = 0, L_CIM = 50176, L_WO = 100352;
constexpr unsigned L_RO = 131072, L_LS = 155648;
constexpr unsigned L_RED = 0, L_DUMMY = 157696;
constexpr unsigned LDS_BYTES = 163840;
constexpr unsigned SPIN_CAP = 1u << 14;

DI unsigned cvt2(float a, float b) { f32x2 v = {a, b}; return __builtin_bit_cast(unsigned, __builtin_convertvector(v, bf16x2)); }
DI float bf2f(unsigned h) { return __uint_as_float(h << 16); }
DI float bfhi(unsigned h) { return __uint_as_float(h & 0xffff0000u); }
DI float bfround(float x) { return __uint_as_float(cvt2(x, 0.f) << 16); }
DI float fexp(float x) { return __builtin_amdgcn_exp2f(x * 1.4426950408889634f); }
DI rsrc_t mkrs(const void* p, unsigned bytes) { return __builtin_amdgcn_make_buffer_rsrc((void*)p, (short)0, (int)bytes, 0x00020000); }
DI void dma(rsrc_t rs, unsigned voff, unsigned soff, unsigned lds) {
    asm volatile("s_mov_b32 m0, %0\n\ts_nop 0\n\tbuffer_load_dwordx4 %1, %2, %3 offen lds" :: "s"(lds), "v"(voff), "s"(rs), "s"(soff) : "memory"); }
template <int N> DI void wait_bar() { asm volatile("s_waitcnt vmcnt(%0) lgkmcnt(0)\n\ts_barrier" :: "i"(N) : "memory"); }
template <int N> DI void wait_vm() { asm volatile("s_waitcnt vmcnt(%0)" :: "i"(N) : "memory"); }
DI void lds_bar() { asm volatile("s_waitcnt lgkmcnt(0)\n\ts_barrier" ::: "memory"); }
typedef LAS char* lp;
DI lp lbase(LAS char* L, unsigned off) { lp p = L + off; PIN(p); return p; }
DI bf16x8 ld128(lp p) { return __builtin_bit_cast(bf16x8, *(LAS const u32x4*)p); }
DI bf16x8 ldtr2(lp p0, lp p1) {
    s16x4 a = __builtin_amdgcn_ds_read_tr16_b64_v4i16((LAS s16x4*)p0), b = __builtin_amdgcn_ds_read_tr16_b64_v4i16((LAS s16x4*)p1);
    return __builtin_bit_cast(bf16x8, __builtin_shufflevector(a, b, 0, 1, 2, 3, 4, 5, 6, 7)); }
DI bf16x8 asbf(u32x4 v) { return __builtin_bit_cast(bf16x8, v); }
DI void st128(u32x4 v, rsrc_t rs, unsigned voff, unsigned soff, int aux) {
    if (aux) __builtin_amdgcn_raw_buffer_store_b128(v, rs, voff, soff, SC1); else __builtin_amdgcn_raw_buffer_store_b128(v, rs, voff, soff, 0);
    asm volatile("s_nop 1" :: "v"(v)); }
DI unsigned nosent(unsigned v) { return v < 0xFFFFFFFEu ? v : 0xFFFFFFFEu; }
DI unsigned atomic_read(rsrc_t rs, unsigned voff) { return (unsigned)__builtin_amdgcn_raw_ptr_buffer_atomic_add_i32(0, rs, (int)voff, 0, SC1); }
DI void atomic_inc(rsrc_t rs, unsigned voff) { asm volatile("buffer_atomic_add %0, %1, %2, 0 offen sc1" :: "v"(1u), "v"(voff), "s"(rs) : "memory"); }
struct cf { float r, i; };
DI cf cmul(cf a, cf b) { cf o;
    asm("v_mul_f32 %0, %3, %5\n\tv_mul_f32 %1, %3, %4\n\tv_fma_f32 %0, %2, %4, -%0\n\tv_fma_f32 %1, %2, %5, %1" : "=&v"(o.r), "=&v"(o.i) : "v"(a.r), "v"(a.i), "v"(b.r), "v"(b.i)); return o; }
DI cf cfma(cf a, cf b, cf c) { cf o;
    asm("v_fma_f32 %0, -%3, %5, %6\n\tv_fma_f32 %1, %3, %4, %7\n\tv_fma_f32 %0, %2, %4, %0\n\tv_fma_f32 %1, %2, %5, %1"
        : "=&v"(o.r), "=&v"(o.i) : "v"(a.r), "v"(a.i), "v"(b.r), "v"(b.i), "v"(c.r), "v"(c.i)); return o; }
DI void pk_cfma(f32x2& nr, f32x2& ni, f32x2 l, f32x2 hr, f32x2 hm, f32x2 a, f32x2 c) {
    asm("v_pk_fma_f32 %0, %2, %3, %5 op_sel_hi:[0,1,1]\n\tv_pk_fma_f32 %1, %2, %4, %6 op_sel_hi:[0,1,1]\n\t"
        "v_pk_fma_f32 %0, %2, %4, %0 op_sel:[1,0,0] neg_lo:[1,0,0] neg_hi:[1,0,0]\n\tv_pk_fma_f32 %1, %2, %3, %1 op_sel:[1,0,0]"
        : "=&v"(nr), "=&v"(ni) : "v"(l), "v"(hr), "v"(hm), "v"(a), "v"(c)); }
DI void pk_cmul(f32x2& nr, f32x2& ni, f32x2 l, f32x2 hr, f32x2 hm) {
    asm("v_pk_mul_f32 %0, %2, %3 op_sel_hi:[0,1]\n\tv_pk_mul_f32 %1, %2, %4 op_sel_hi:[0,1]\n\t"
        "v_pk_fma_f32 %0, %2, %4, %0 op_sel:[1,0,0] neg_lo:[1,0,0] neg_hi:[1,0,0]\n\tv_pk_fma_f32 %1, %2, %3, %1 op_sel:[1,0,0]"
        : "=&v"(nr), "=&v"(ni) : "v"(l), "v"(hr), "v"(hm)); }
DI f32x2 pk_scale(f32x2 s, f32x2 a) { f32x2 r; asm("v_pk_mul_f32 %0, %1, %2 op_sel_hi:[0,1]" : "=v"(r) : "v"(s), "v"(a)); return r; }
DI float sum4(float v) {
    auto a = __builtin_amdgcn_permlane16_swap(__float_as_uint(v), __float_as_uint(v), false, false); v = __uint_as_float(a[0]) + __uint_as_float(a[1]);
    auto c = __builtin_amdgcn_permlane32_swap(__float_as_uint(v), __float_as_uint(v), false, false); return __uint_as_float(c[0]) + __uint_as_float(c[1]); }
DI void bcast4(float v, float& r0, float& r1, float& r2, float& r3) {
    auto a = __builtin_amdgcn_permlane16_swap(__float_as_uint(v), __float_as_uint(v), false, false);
    auto e = __builtin_amdgcn_permlane32_swap(a[0], a[0], false, false); auto o = __builtin_amdgcn_permlane32_swap(a[1], a[1], false, false);
    r0 = __uint_as_float(e[0]); r2 = __uint_as_float(e[1]); r1 = __uint_as_float(o[0]); r3 = __uint_as_float(o[1]); }
DI float rowmax16(const f32x16& s) {
    float a = fmaxf(fmaxf(s[0], s[1]), s[2]), b = fmaxf(fmaxf(s[3], s[4]), s[5]);
    a = fmaxf(fmaxf(a, s[6]), s[7]); b = fmaxf(fmaxf(b, s[8]), s[9]); a = fmaxf(fmaxf(a, s[10]), s[11]); b = fmaxf(fmaxf(b, s[12]), s[13]);
    a = fmaxf(fmaxf(a, s[14]), s[15]); a = fmaxf(a, b);
    auto rr = __builtin_amdgcn_permlane32_swap(__float_as_uint(a), __float_as_uint(a), false, false);
    return fmaxf(__uint_as_float(rr[0]), __uint_as_float(rr[1])); }
DI void set_shift(u32x4& q1, float c, int hi) {
    const float c1 = bfround(c), c2 = bfround(c - c1), c3 = c - c1 - c2;
    if (hi) { q1[2] = cvt2(-c1, -c2); q1[3] = cvt2(-c3, 0.f); } }
DI void ladd(LAS char* L, unsigned off, unsigned v) {
    asm volatile("s_mov_b64 exec, 1\n\tds_add_u32 %0, %1\n\ts_mov_b64 exec, -1" :: "v"(L + off), "v"(v) : "memory"); }
DI void lcount(LAS char* L, unsigned off, int) { ladd(L, off, 1u); }
DI unsigned lread(LAS char* L, unsigned off) { return __builtin_amdgcn_readfirstlane(*(volatile LAS unsigned*)(L + off)); }
DI void lwait(LAS char* L, unsigned off, unsigned n) {
    if (__builtin_expect(lread(L, off) < n, 0)) { _Pragma("unroll 1") for (unsigned spin = 0; lread(L, off) < n && spin < (1u << 20); ++spin) { if (LSLEEP) __builtin_amdgcn_s_sleep(LSLEEP); } }
    asm volatile("" ::: "memory"); }
DI void lwait_idle(LAS char* L, unsigned off, unsigned n) {
    _Pragma("unroll 1") for (unsigned spin = 0; lread(L, off) < n && spin < (1u << 18); ++spin) __builtin_amdgcn_s_sleep(2);
    asm volatile("" ::: "memory"); }
DI int xswz(int row) { return ((row >> 1) & 7) ^ ((row & 16) ? 7 : 0) ^ ((row & 32) ? 6 : 0); }
DI u32x4 pack8(const f32x16& a, int e) { return (u32x4){cvt2(a[e], a[e + 1]), cvt2(a[e + 2], a[e + 3]), cvt2(a[e + 4], a[e + 5]), cvt2(a[e + 6], a[e + 7])}; }
#define PAIR(v, p) (f32x2){v[(p) >> 1][2 * ((p) & 1)], v[(p) >> 1][2 * ((p) & 1) + 1]}

#define STAMP(k)
#define STAMPV(k)

__global__ void __launch_bounds__(512, 2)
gated_mix_kernel(const uint16_t* x, const uint16_t* wq, const uint16_t* wk, const uint16_t* wv, const uint16_t* wo, const uint16_t* lre, const uint16_t* lim,
                 const uint16_t* bre, const uint16_t* bim, const uint16_t* cre, const uint16_t* cim, const uint16_t* logdt, const uint16_t* dskip,
                 const uint16_t* gate, float* out, char* ws) {
    __shared__ __attribute__((aligned(16))) char smem[LDS_BYTES];
    LAS char* L = (LAS char*)smem;
    const unsigned l0 = (unsigned)(uintptr_t)L;
    const int lane = threadIdx.x & 63, w = __builtin_amdgcn_readfirstlane(threadIdx.x >> 6);
    const int b = blockIdx.x;
    STAMP(0);
    {   unsigned long long pc; asm volatile("s_getpc_b64 %0" : "=s"(pc));
        const rsrc_t rsC = mkrs((const void*)(pc & ~255ull), CPREF);
        asm volatile("s_mov_b32 m0, %0\n\ts_nop 4\n\tbuffer_load_dword %1, %2, 0 offen lds" :: "s"(l0 + L_DUMMY), "v"(threadIdx.x * 64), "s"(rsC) : "memory"); }
    const int blk = (b & 7) * 4 + (b >> 6), j = (b >> 3) & 7;
    const int th = w & 1, hd = (w >> 1) & 1;
    const int r32 = lane & 31, hi = lane >> 5, r16 = lane & 15, G = lane >> 4;
    const int tq = r16 >> 2, tp = lane & 3;
    const int sl = lane & 7, r8 = lane >> 3;
    const unsigned l16 = lane * 16;
    const int kind = j & 1, tg = j >> 1;
    unsigned vx = r8 * ROWB + ((sl ^ (r8 >> 1)) << 4); PIN(vx);
    PINS(x); PINS(wq); PINS(wk); PINS(wv); PINS(wo); PINS(lre); PINS(lim); PINS(bre); PINS(bim); PINS(cre); PINS(cim); PINS(logdt); PINS(dskip); PINS(gate); PINS(out); PINS(ws);
    const rsrc_t rsX = mkrs(x, SEQ * ROWB), rsWS = mkrs(ws, WS_END);
    const unsigned opiece = WS_O + blk * 65536 + (th * 16 + 2 * j + hd) * 2048;

    const rsrc_t rsWQ = mkrs(wq, DM * ROWB);
    const int pwq = sl ^ (((r8 >> 1) & 1) << 2);
    const unsigned vw = r8 * ROWB + 112 * j + (pwq << 4);
#define WQD(i0, i1) do { if (pwq != 7) { UNROLL for (int i = (i0); i < (i1); ++i) { const int p = i < 18 ? 3 * i + w - 1 : min(53 + w, 55); dma(rsWQ, vw, p * 8 * ROWB, l0 + L_WQ + p * 1024); } } } while (0)
#define BUMP() do { if (b == 0) atomic_inc(rsWS, lane < 16 ? WS_X + lane * 128 : OOB); } while (0)
    if (w < 4) {
        const int qrow = 32 * th + r32;
        const unsigned rob = L_RO + w * 2048 + l16, lsb = L_LS + (w * 32 + r32) * 16;
        lp a00, a01, a02, a03, a1, ro0, ro1, ro2, rop, lsp, lw0, lw1, lw2, lw3, qs0, qs1, qs2, qs3;
        unsigned kco, kc = 0u;
        u32x4 sel0, sel1, pw0, pw1, kf[4][2], vf[4][2];
        f32x16 o[4];
#define QPREP() do { const unsigned a0o = L_X + qrow * 128 + ((hi ^ xswz(qrow)) << 4); \
        a00 = lbase(L, a0o); a01 = lbase(L, a0o ^ 32); a02 = lbase(L, a0o ^ 64); a03 = lbase(L, a0o ^ 96); \
        a1 = lbase(L, L_WQ + (8 * hi + tq) * 128 + ((56 * hd + 32 * (G & 1) + 8 * tp) ^ (((tq >> 1) & 1) << 6))); \
        kco = lane < 16 ? WS_KVCNT + (b & 7) * 2048 + lane * 128 : OOB; PIN(kco); \
        ro0 = lbase(L, rob ^ 2048); ro1 = lbase(L, (rob ^ 4096) + 8192); ro2 = lbase(L, (rob ^ 6144) + 16384); rop = lbase(L, rob); lsp = lbase(L, lsb); \
        lw0 = lbase(L, hi ? (lsb ^ 512) + w * 4 : L_DUMMY); lw1 = lbase(L, hi ? (lsb ^ 1024) + w * 4 : L_DUMMY); lw2 = lbase(L, hi ? (lsb ^ 1536) + w * 4 : L_DUMMY); lw3 = lbase(L, hi ? lsb + w * 4 : L_DUMMY); \
        qs0 = lbase(L, L_QSH + (w ^ 1) * 2048 + l16); qs1 = lbase(L, L_QSH + (w ^ 2) * 2048 + l16); qs2 = lbase(L, L_QSH + (w ^ 3) * 2048 + l16); qs3 = lbase(L, L_QSH + w * 2048 + l16); \
        const int js = 4 * ((r32 >> 3) & 1) + (r32 & 3); const unsigned one = ((r32 >> 2) & 1) == hi ? 0x3F80u << (16 * (js & 1)) : 0u; \
        UNROLL for (int e = 0; e < 4; ++e) { const unsigned v = (js >> 1) == e ? one : 0u; sel0[e] = r32 < 16 ? v : 0u; sel1[e] = r32 < 16 ? 0u : v; } PIN(sel0); PIN(sel1); \
        o[0] = o[1] = o[2] = o[3] = (f32x16){}; PIN(o[0]); PIN(o[1]); PIN(o[2]); PIN(o[3]); \
        pw1 = (u32x4){0u, 0u, 0u, 0u}; vf[3][1] = pw1; PIN(pw1); PIN(vf[3][1]); } while (0)
        __builtin_amdgcn_s_setprio(2);
        if (w == 0) {
            const unsigned xoff = lane == 0 ? WS_X + (b & 15) * 128 : OOB;
            const rsrc_t rsWK = mkrs(kind ? wv : wk, DM * 56);
            const unsigned vxa = vx ^ (xswz(16 * tg) << 4), vxb = (vx ^ (xswz(16 * tg + 8) << 4)) + 8 * ROWB, xs = (blk * 64 + tg * 16) * ROWB;
            unsigned x0 = 0u, x1 = 0u;
            asm volatile("buffer_atomic_add %0, %1, %2, 0 offen sc0 sc1" : "+v"(x0) : "v"(xoff), "s"(rsWS) : "memory");
            STAMP(36);
            UNROLL for (int c = 0; c < 7; ++c) {
                dma(rsX, vxa, xs + c * 128, l0 + L_X + c * 8192 + tg * 2048); dma(rsX, vxb, xs + c * 128, l0 + L_X + c * 8192 + tg * 2048 + 1024);
                UNROLL for (int n = (7 * c + 1) / 2; n < (7 * c + 8) / 2; ++n) dma(rsWK, l16, n * 1024, l0 + L_WK + n * 1024);
            }
            asm volatile("s_barrier" ::: "memory");
            STAMP(14);
            __builtin_amdgcn_raw_buffer_store_b64((u32x2){~0u, ~0u}, rsWS, lane < 56 ? lane * 8 : OOB, WS_E + (blk * DM + 56 * j) * 8, SC1);
            const int cg0 = kind ? tp : 4 * (tp >> 1) + (tp & 1), cg1 = kind ? 4 + tp : 4 * (tp >> 1) + 2 + (tp & 1);
            const lp aw0 = lbase(L, L_WK + (8 * G + tq) * 56 + cg0 * 8), aw1 = lbase(L, L_WK + (8 * G + tq) * 56 + cg1 * 8);
            const unsigned axo = L_X + (16 * tg + r16) * 128 + ((G ^ xswz(16 * tg + r16)) << 4);
            const lp ax0 = lbase(L, axo), ax1 = lbase(L, axo ^ 64);
            const int sg = tg & 1;
            const unsigned tb = WS_KV + (2 * blk + (tg >> 1)) * 4096 + (kind ? 2048 + sg * 1024 : 0);
            unsigned kva = kind ? ((G >> 1) * 32 + r16) * 16 + (G & 1) * 8 : (G * 32 + 16 * sg + ((r16 & 3) | ((r16 & 4) << 1) | ((r16 & 8) >> 1))) * 16; PIN(kva);
            unsigned kvb = kind ? kva + 256 : kva + 8; PIN(kvb);
            const bool spare = kind ? r16 >= 12 : G == 3;
            unsigned sp2 = (kind && r16 != 12) ? 0u : 0x3F803F80u, sp3 = kind ? sp2 : 0x00003F80u; PIN(sp2); PIN(sp3);
            f32x4 d0 = {0.f, 0.f, 0.f, 0.f}, d1 = d0;
            bf16x8 xf[14], w0[14], w1[14];
#define KVR(c) do { wait_vm<1 + 39 - 2 * ((c) + 1) - (7 * (c) + 8) / 2>(); UNROLL for (int ks = 2 * (c); ks < 2 * (c) + 2; ++ks) { xf[ks] = ld128(((ks & 1) ? ax1 : ax0) + (c) * 8192); \
                        w0[ks] = ldtr2(aw0 + ks * 1792, aw0 + ks * 1792 + 224); w1[ks] = ldtr2(aw1 + ks * 1792, aw1 + ks * 1792 + 224); } SBAR(); } while (0)
#define KVM(c, K) do { UNROLL for (int ks = 2 * (c); ks < 2 * (c) + 2; ++ks) { \
                        if (K) { d0 = MFMA16(xf[ks], w0[ks], d0); d1 = MFMA16(xf[ks], w1[ks], d1); } else { d0 = MFMA16(w0[ks], xf[ks], d0); d1 = MFMA16(w1[ks], xf[ks], d1); } } SBAR(); } while (0)
#define KVP(K) do { KVR(0); STAMP(1); KVR(1); KVM(0, K); KVR(2); KVM(1, K); KVR(3); KVM(2, K); KVR(4); KVM(3, K); KVR(5); KVM(4, K); KVR(6); STAMP(32); KVM(5, K); KVM(6, K); } while (0)
            if (kind) KVP(1); else KVP(0);
            __builtin_amdgcn_raw_buffer_store_b64((u32x2){cvt2(d0[0], d0[1]), cvt2(d0[2], d0[3])}, rsWS, kva, tb, SC1);
            __builtin_amdgcn_raw_buffer_store_b64((u32x2){spare ? sp2 : cvt2(d1[0], d1[1]), spare ? sp3 : cvt2(d1[2], d1[3])}, rsWS, kvb, tb, SC1);
            asm volatile("buffer_atomic_add %0, %1, %2, 0 offen sc0 sc1" : "+v"(x1) : "v"(xoff), "s"(rsWS) : "memory");
            ladd(L, L_KD, 1u); ladd(L, L_XA, 1u); ladd(L, L_XF, 1u);
            STAMP(12);
            QPREP();
            asm volatile("s_waitcnt vmcnt(0)" : "+v"(x0), "+v"(x1) :: "memory");
            STAMP(35);
            const unsigned s0 = __builtin_amdgcn_readfirstlane(x0); unsigned s1 = __builtin_amdgcn_readfirstlane(x1);
            _Pragma("unroll 1") for (unsigned spin = 0; s1 == s0 && spin < SPIN_CAP; ++spin) s1 = __builtin_amdgcn_readfirstlane(atomic_read(rsWS, xoff));
            atomic_inc(rsWS, lane < 8 ? WS_KVCNT + lane * 2048 + (b & 15) * 128 : OOB);
            BUMP();
            STAMP(13);
        } else {
            unsigned g0, g1, g2;
            const int gk = min((w - 1) * 192, 256) + lane;
            const uint16_t* gp = gate + gk; PIN(gp);
            const int ga = 2 * (w - 1) + (w - 1 >= tg ? 2 : 0);
            unsigned vxa = vx ^ (xswz(8 * ga) << 4), vxb = (vx ^ (xswz(8 * ga + 8) << 4)) + 8 * ROWB; const unsigned xs = (blk * 64 + 8 * ga) * ROWB; PIN(vxa); PIN(vxb);
            if (w == 1 && lane < 16) *(LAS unsigned*)(L + L_KD + lane * 4) = 0u;
            lds_bar();
            UNROLL for (int c = 0; c < 7; ++c) { dma(rsX, vxa, xs + c * 128, l0 + L_X + c * 8192 + ga * 1024); dma(rsX, vxb, xs + c * 128, l0 + L_X + c * 8192 + ga * 1024 + 1024); }
            asm volatile("global_load_ushort %0, %3, off\n\tglobal_load_ushort %1, %3, off offset:128\n\tglobal_load_ushort %2, %3, off offset:256"
                         : "=&v"(g0), "=&v"(g1), "=&v"(g2) : "v"(gp) : "memory");
            WQD(0, 6);
            QPREP();
            wait_vm<9>();
            lcount(L, L_XA, lane);
            WQD(6, 19);
            STAMP(14);
            asm volatile("s_waitcnt vmcnt(9)" : "+v"(g0), "+v"(g1), "+v"(g2) :: "memory");
            const lp gl = L + (L_WQ + gk * 128 + (112 ^ (((gk >> 1) & 1) << 6)));
            *(LAS uint16_t*)(gl) = (uint16_t)g0; *(LAS uint16_t*)(gl + 8192) = (uint16_t)g1; *(LAS uint16_t*)(gl + 16384) = (uint16_t)g2;
            lcount(L, L_XF, lane);
            STAMP(1);
        }
        lwait(L, L_XF, 4u);
        STAMP(2);
        f32x16 qa = {};
        { bf16x8 wf[28], xq[28];
          UNROLL for (int ks = 0; ks < 28 + QD_; ++ks) {
            if (ks == WQ2) { STAMP(44); wait_vm<0>(); lcount(L, L_XG, lane); STAMP(45); }
            if (ks == 15) { lwait(L, L_XG, 4u); STAMP(46); }
            if (ks < 28) { wf[ks] = ldtr2(a1 + ks * 2048, a1 + ks * 2048 + 512); xq[ks] = ld128(((ks & 3) == 0 ? a00 : (ks & 3) == 1 ? a01 : (ks & 3) == 2 ? a02 : a03) + (ks >> 2) * 8192); }
            if (ks >= QD_) qa = MFMA32(wf[ks - QD_], xq[ks - QD_], qa);
            if (ks == KPOLL && w == 0) asm volatile("buffer_atomic_add %0, %1, %2, 0 offen sc0 sc1" : "+v"(kc) : "v"(kco), "s"(rsWS) : "memory");
            if (ks == KCHK && w == 0) {
                asm volatile("s_waitcnt vmcnt(0)" : "+v"(kc) :: "memory");
                if (__builtin_expect(__builtin_amdgcn_ballot_w64(kc != 16 && lane < 16) != 0, 0)) { _Pragma("unroll 1") for (unsigned spin = 0; spin < SPIN_CAP; ++spin) {
                    BUMP(); if (__builtin_amdgcn_ballot_w64(atomic_read(rsWS, kco) != 16 && lane < 16) == 0) break; } }
                ladd(L, L_KR, 1u); }
            if (ks == 28) ladd(L, L_QD, 1u);
            SBAR(); } }
        const unsigned kv0 = WS_KV + w * 65536;
        const char* kvp = ws + kv0;
#define LD1(A0, off) asm volatile("global_load_dwordx4 %0, %1, %2 offset:%3 sc1" : "=&v"(A0) : "v"(l16), "s"(kvp), "i"(off) : "memory")
#define LD2(A0, A1, off) do { LD1(A0, off); LD1(A1, (off) + 1024); } while (0)
#define WAIT2(N, A, B) asm volatile("s_waitcnt vmcnt(%2)" : "+v"(A), "+v"(B) : "i"(N) : "memory")
        STAMP(47);
        lwait(L, L_KR, 1u);
        LD2(kf[0][0], kf[0][1], 0); LD2(vf[0][0], vf[0][1], 2048);
        STAMP(48);
        lwait(L, L_QD, 4u);
        asm volatile("s_nop 11" : "+v"(qa));
        const f32x2 cs = {0.18898223650461361f * 1.4426950408889634f, 0.f};
        u32x4 q0[4], q1[4];
        UNROLL for (int e = 0; e < 4; ++e) { const f32x2 a = pk_scale(cs, (f32x2){qa[2 * e], qa[2 * e + 1]}), c = pk_scale(cs, (f32x2){qa[8 + 2 * e], qa[9 + 2 * e]});
            q0[3][e] = cvt2(a[0], a[1]); q1[3][e] = cvt2(c[0], c[1]); }
        if (hi) { q1[3][2] = 0u; q1[3][3] = 0u; }
        *(LAS u32x4*)(qs3) = q0[3]; *(LAS u32x4*)(qs3 + 1024) = q1[3];
        if (hd == 1 && hi) *(LAS float*)(L + L_G + qrow * 4) = __builtin_amdgcn_rcpf(1.0f + fexp(-qa[12]));
        STAMP(3);
        lcount(L, L_QF, lane);
        UNROLL for (int n = 1; n < 3; ++n) { kvp += 4096; LD2(kf[n][0], kf[n][1], 0); LD2(vf[n][0], vf[n][1], 2048); }
        STAMP(4);

        f32x16 sA, sB;
        lwait(L, L_QF, 4u);
        STAMP(5);
        q0[0] = *(LAS const u32x4*)(qs0); q1[0] = *(LAS const u32x4*)(qs0 + 1024); q0[1] = *(LAS const u32x4*)(qs1); q1[1] = *(LAS const u32x4*)(qs1 + 1024);
        q0[2] = *(LAS const u32x4*)(qs2); q1[2] = *(LAS const u32x4*)(qs2 + 1024);
        const float gt = *(LAS const float*)(L + L_G + qrow * 4);
        ladd(L, L_SF, 1u);
#define GIVE(O, RP, LP) do { *(LAS u32x4*)(RP) = pack8(O, 0); *(LAS u32x4*)((RP) + 1024) = pack8(O, 8); *(LAS float*)(LP) = O[12]; } while (0)
        WAIT2(10, kf[0][0], kf[0][1]);
        __builtin_amdgcn_s_setprio(0);
        STAMP(6);
        sA = MFMA32(asbf(kf[0][0]), asbf(q0[0]), (f32x16){}); sA = MFMA32(asbf(kf[0][1]), asbf(q1[0]), sA);
#define EXP(A, i) A[i] = __builtin_amdgcn_exp2f(A[i])
#define EX4(A, i) do { EXP(A, i); EXP(A, i + 1); EXP(A, i + 2); EXP(A, i + 3); PIN(A); } while (0)
#define UNIT(A, B, KN0, KN1, qn, q, qp, V0, VP1, NEXT, H0, H1, H2, H3) do { SBAR(); H0; \
        if (NEXT) { B = MFMA32(asbf(KN0), asbf(q0[qn]), (f32x16){}); PIN(B); } SBAR(); \
        EX4(A, 0); SBAR(); \
        o[qp] = MFMA32(asbf(VP1), asbf(pw1), o[qp]); PIN(o[qp]); SBAR(); H1; \
        pw0[0] = cvt2(A[0], A[1]); pw0[1] = cvt2(A[2], A[3]); PIN(pw0); SBAR(); EX4(A, 4); SBAR(); \
        if (NEXT) { B = MFMA32(asbf(KN1), asbf(q1[qn]), B); PIN(B); } SBAR(); H2; SBAR(); \
        pw0[2] = cvt2(A[4], A[5]); pw0[3] = cvt2(A[6], A[7]); PIN(pw0); SBAR(); EX4(A, 8); SBAR(); H3; \
        o[q] = MFMA32(asbf(V0), asbf(pw0), o[q]); PIN(o[q]); SBAR(); \
        { float e12 = __builtin_amdgcn_exp2f(A[12]), e13 = __builtin_amdgcn_exp2f(A[13]), e14 = __builtin_amdgcn_exp2f(A[14]), e15 = __builtin_amdgcn_exp2f(A[15]); \
          asm volatile("" : "+v"(e12), "+v"(e13), "+v"(e14), "+v"(e15)); SBAR(); \
          pw1[0] = cvt2(A[8], A[9]); pw1[1] = cvt2(A[10], A[11]); SBAR(); pw1[2] = cvt2(e12, e13); pw1[3] = cvt2(e14, e15); PIN(pw1); } SBAR(); } while (0)
#define TRIP(c, n, p, LD, NV, NK) do { \
        UNIT(sA, sB, kf[c][0], kf[c][1], 1, 0, 3, vf[c][0], vf[p][1], true, WAIT2(NV, vf[c][0], vf[c][1]), { if (LD) { kvp += 4096; LD1(kf[p][0], 0); } }, { if (LD) LD1(kf[p][1], 1024); }, {}); \
        UNIT(sB, sA, kf[c][0], kf[c][1], 2, 1, 0, vf[c][0], vf[c][1], true, {}, { if (LD) LD1(vf[p][0], 2048); }, { if (LD) LD1(vf[p][1], 3072); }, {}); \
        UNIT(sA, sB, kf[c][0], kf[c][1], 3, 2, 1, vf[c][0], vf[c][1], true, {}, {}, WAIT2(NK, kf[n][0], kf[n][1]), {}); \
        UNIT(sB, sA, kf[n][0], kf[n][1], 0, 3, 2, vf[c][0], vf[c][1], true, {}, {}, {}, {}); } while (0)
        _Pragma("unroll 1") for (int it = 0; it < 3; ++it) {
            TRIP(0, 1, 3, true, 8, 10); TRIP(1, 2, 0, true, 8, 10); TRIP(2, 3, 1, true, 8, 10); TRIP(3, 0, 2, true, 8, 10);
            STAMPV(17 + it);
        }
        TRIP(0, 1, 3, true, 8, 10);
        ladd(L, L_QL, 1u);
        TRIP(1, 2, 0, false, 8, 6); TRIP(2, 3, 1, false, 4, 2);
        ladd(L, L_QE, 1u);
        lwait(L, L_UF, 8u);
        UNIT(sA, sB, kf[3][0], kf[3][1], 1, 0, 3, vf[3][0], vf[2][1], true, WAIT2(0, vf[3][0], vf[3][1]), {}, {}, {});
        UNIT(sB, sA, kf[3][0], kf[3][1], 2, 1, 0, vf[3][0], vf[3][1], true, {}, {}, {}, {});
        UNIT(sA, sB, kf[3][0], kf[3][1], 3, 2, 1, vf[3][0], vf[3][1], true, {}, GIVE(o[0], ro0, lw0), {}, {});
        UNIT(sB, sA, kf[0][0], kf[0][1], 0, 3, 2, vf[3][0], vf[3][1], false, {}, GIVE(o[1], ro1, lw1), {}, {});
        STAMPV(20);
        o[3] = MFMA32(asbf(vf[3][1]), asbf(pw1), o[3]); PIN(o[3]); SBAR();
        GIVE(o[2], ro2, lw2); SBAR();
        STAMP(7);

        float l3 = o[3][12];
        *(LAS float*)(lw3) = l3;
        {   const unsigned u0 = __float_as_uint(o[0][12]), u1 = __float_as_uint(o[1][12]), u2 = __float_as_uint(o[2][12]), u3 = __float_as_uint(l3);
            const unsigned lo = min(min(u0, u1), min(u2, u3)), up = max(max(u0, u1), max(u2, u3));
            const bool bad = __builtin_amdgcn_ballot_w64(hi && (lo < 0x1F800000u || up > 0x5D800000u)) != 0;
            ladd(L, L_GV, bad ? 0x10001u : 1u); }
        unsigned gv = lread(L, L_GV);
        if (__builtin_expect((gv & 0xffffu) < 4u, 0)) { _Pragma("unroll 1") for (unsigned spin = 0; (gv & 0xffffu) < 4u && spin < (1u << 20); ++spin) gv = lread(L, L_GV); }
        asm volatile("" ::: "memory");
        STAMP(15);
        bf16x8 f[6]; f32x4 m0;
        UNROLL for (int t = 0; t < 6; ++t) f[t] = ld128(rop + (t >> 1) * 8192 + (t & 1) * 1024);
        m0 = *(LAS const f32x4*)(lsp);
        SBAR();
        if (__builtin_expect(gv > 0xffffu, 0)) {
            auto qbar = [&](unsigned n) __attribute__((always_inline)) { asm volatile("s_waitcnt lgkmcnt(0)" ::: "memory"); ladd(L, L_FB, 1u); lwait(L, L_FB, n); };
            float mx[4] = {-INFINITY, -INFINITY, -INFINITY, -INFINITY};
            qbar(4u);
            _Pragma("unroll 1") for (int t = 0; t < 16; ++t) {
                const bf16x8 a = asbf(__builtin_amdgcn_raw_buffer_load_b128(rsWS, l16, kv0 + t * 4096, SC1));
                const bf16x8 c = asbf(__builtin_amdgcn_raw_buffer_load_b128(rsWS, l16, kv0 + t * 4096 + 1024, SC1));
                UNROLL for (int q = 0; q < 4; ++q) { f32x16 s = MFMA32(a, asbf(q0[q]), (f32x16){}); s = MFMA32(c, asbf(q1[q]), s); mx[q] = fmaxf(mx[q], rowmax16(s)); SBAR(); }
            }
            *(LAS float*)(lw0) = mx[0]; *(LAS float*)(lw1) = mx[1]; *(LAS float*)(lw2) = mx[2]; *(LAS float*)(lw3) = mx[3];
            qbar(8u);
            UNROLL for (int q = 0; q < 4; ++q) { const f32x4 n0 = *(LAS const f32x4*)(L + (lsb ^ (((q + 1) & 3) * 512)));
                set_shift(q1[q], fmaxf(fmaxf(n0[0], n0[1]), fmaxf(n0[2], n0[3])), hi); }
            qbar(12u);
            UNROLL for (int q = 0; q < 4; ++q) {
                f32x16 acc = {};
                _Pragma("unroll 1") for (int t = 0; t < 16; ++t) {
                    const bf16x8 a = asbf(__builtin_amdgcn_raw_buffer_load_b128(rsWS, l16, kv0 + t * 4096, SC1));
                    const bf16x8 c = asbf(__builtin_amdgcn_raw_buffer_load_b128(rsWS, l16, kv0 + t * 4096 + 1024, SC1));
                    const bf16x8 d = asbf(__builtin_amdgcn_raw_buffer_load_b128(rsWS, l16, kv0 + t * 4096 + 2048, SC1));
                    const bf16x8 e = asbf(__builtin_amdgcn_raw_buffer_load_b128(rsWS, l16, kv0 + t * 4096 + 3072, SC1));
                    f32x16 s = MFMA32(a, asbf(q0[q]), (f32x16){}); s = MFMA32(c, asbf(q1[q]), s);
                    UNROLL for (int r = 0; r < 16; ++r) s[r] = __builtin_amdgcn_exp2f(s[r]);
                    acc = MFMA32(d, asbf(pack8(s, 0)), acc); acc = MFMA32(e, asbf(pack8(s, 8)), acc);
                }
                if (q == 0) { GIVE(acc, ro0, lw0); } else if (q == 1) { GIVE(acc, ro1, lw1); } else if (q == 2) { GIVE(acc, ro2, lw2); } else { *(LAS float*)(lw3) = acc[12]; o[3] = acc; }
            }
            qbar(16u);
            UNROLL for (int t = 0; t < 6; ++t) f[t] = ld128(rop + (t >> 1) * 8192 + (t & 1) * 1024);
            m0 = *(LAS const f32x4*)(lsp);
        }
        __builtin_amdgcn_s_setprio(1);
        f32x16 t = o[3];
        UNROLL for (int k = 0; k < 6; ++k) t = MFMA32(asbf((k & 1) ? sel1 : sel0), f[k], t);
        {
            float s0 = m0[0] + m0[1], s2 = m0[2] + m0[3]; PIN(s0); PIN(s2);
            f32x2 sc; sc[0] = sc[1] = gt * __builtin_amdgcn_rcpf(s0 + s2); PIN(sc);
            u32x4 oa, ob;
            UNROLL for (int e = 0; e < 4; ++e) { const f32x2 a = sc * (f32x2){t[2 * e], t[2 * e + 1]}; oa[e] = nosent(cvt2(a[0], a[1])); }
            st128(oa, rsWS, l16, opiece, SC1);
            SBAR();
            UNROLL for (int e = 0; e < 4; ++e) { const f32x2 a = sc * (f32x2){t[8 + 2 * e], t[9 + 2 * e]}; ob[e] = nosent(cvt2(a[0], a[1])); }
            if (hi) { ob[2] = 0u; ob[3] = 0u; }
            st128(ob, rsWS, l16, opiece + 1024, SC1);
        }
        __builtin_amdgcn_s_setprio(0);
        STAMP(8);
        const int tok = blk * 64 + qrow, col = 56 * j + 32 * hd + 4 * hi;
        const rsrc_t rsD = mkrs(dskip, ROWB), rsO = mkrs(out, SEQ * DM * 4);
        if (ODRAIN) wait_vm<0>();
        ladd(L, L_OD, 1u);
        f32x4 r[4]; u32x2 xv[4], dv[4];
        UNROLL for (int e = 0; e < 4; ++e) { xv[e] = __builtin_amdgcn_raw_buffer_load_b64(rsX, tok * ROWB + col * 2 + e * 16, 0, 0); dv[e] = __builtin_amdgcn_raw_buffer_load_b64(rsD, col * 2 + e * 16, 0, 0); }
        SBAR();
        UNROLL for (int e = 0; e < 4; ++e) {
            float t0 = bf2f(xv[e][0]) * bf2f(dv[e][0]), t1 = bfhi(xv[e][0]) * bfhi(dv[e][0]), t2 = bf2f(xv[e][1]) * bf2f(dv[e][1]), t3 = bfhi(xv[e][1]) * bfhi(dv[e][1]);
            PIN(t0); PIN(t1); PIN(t2); PIN(t3); t0 *= 1.0f - gt; t1 *= 1.0f - gt; t2 *= 1.0f - gt; t3 *= 1.0f - gt; PIN(t0); PIN(t1); PIN(t2); PIN(t3);
            r[e] = (f32x4){t0, t1, t2, t3}; }
        const lp rp = lbase(L, L_RED + hd * 32768 + th * 16384 + l16);
        unsigned oo = (tok * DM + col) * 4, o3 = hd ? OOB : oo + 96; PIN(oo); PIN(o3);
        lds_bar();
        STAMP(10);
        UNROLL for (int e = 0; e < 4; ++e) {
            r[e] += (*(LAS const f32x4*)(rp + e * 1024) + *(LAS const f32x4*)(rp + 4096 + e * 1024)) + (*(LAS const f32x4*)(rp + 8192 + e * 1024) + *(LAS const f32x4*)(rp + 12288 + e * 1024));
            st128(__builtin_bit_cast(u32x4, r[e]), rsO, e < 3 ? oo + e * 32 : o3, 0, 0); }
    } else {
        const int u = w - 4, st = min(16 * u + r16, 55);
        const unsigned eoff = (G == 0 && 16 * u + r16 < 56) ? st * 8 : OOB;
        const rsrc_t rsBR = mkrs(bre, DM * ROWB), rsBI = mkrs(bim, DM * ROWB);
        const unsigned vb0_ = (56 * j + 16 * u + r8) * ROWB + ((sl ^ (r8 >> 1)) << 4), vb1_ = u < 3 ? (vb0_ ^ 64) + 8 * ROWB : OOB;
#define DST2(a) (u < 3 ? (a) : l0 + L_DUMMY)
        unsigned p0, p1, p2;
        const uint16_t *a0 = lre + 56 * j + st, *a1 = lim + 56 * j + st, *a2 = logdt + 56 * j + st; PIN(a0); PIN(a1); PIN(a2);
#define TOUCH(ptr, bytes, vo) do { const rsrc_t r_ = mkrs(ptr, bytes); \
            asm volatile("s_mov_b32 m0, %0\n\ts_nop 0\n\tbuffer_load_dword %1, %2, 0 offen lds" :: "s"(l0 + L_DUMMY), "v"(vo), "s"(r_) : "memory"); } while (0)
        if (PFE == 1) { if (u < 3) TOUCH(u == 0 ? wq : u == 1 ? bre : bim, DM * ROWB, lane < 25 ? (b * 25 + lane) * 64 : OOB); else TOUCH(x, SEQ * ROWB, lane < 56 ? (blk * 448 + j * 56 + lane) * 128 : OOB); }
        const bool master = b == 0 && w == 7;
        if (master) {
            __builtin_amdgcn_raw_buffer_store_b32(0u, rsWS, lane * 128, 0, SC1);
            __builtin_amdgcn_raw_buffer_store_b32(0u, rsWS, lane * 128 + 8192, 0, SC1);
            __builtin_amdgcn_raw_buffer_store_b32(0u, rsWS, lane < 32 ? lane * 128 + 16384 : OOB, 0, SC1);
        }
        asm volatile("s_barrier" ::: "memory");
        if (master) { wait_vm<0>(); BUMP(); }
        if (PFE == 2 && u < 3) {
            const int r = b >> 3;
            TOUCH(u == 0 ? wq : u == 1 ? bre : bim, DM * ROWB, lane < 49 ? (r * 98 + lane) * 128 : OOB);
            TOUCH(u == 0 ? wq : u == 1 ? bre : bim, DM * ROWB, lane < 49 ? (r * 98 + 49 + lane) * 128 : OOB);
        }
        if (PFE == 0) { if (u < 3) TOUCH(u == 0 ? wq : u == 1 ? bre : bim, DM * ROWB, lane < 25 ? (b * 25 + lane) * 64 : OOB); }
        asm volatile("global_load_ushort %0, %3, off\n\tglobal_load_ushort %1, %4, off\n\tglobal_load_ushort %2, %5, off"
                     : "=&v"(p0), "=&v"(p1), "=&v"(p2) : "v"(a0), "v"(a1), "v"(a2) : "memory");
#define BOFF(c) ((c) % 3 == 0 ? L_B0 : L_WK + ((c) % 3 - 1) * 14336)
#define BD(c) do { dma(rsBR, vb0_, (c) * 128, l0 + BOFF(c) + u * 2048); dma(rsBI, vb0_, (c) * 128, l0 + BOFF(c) + 7168 + u * 2048); \
                   dma(rsBR, vb1_, (c) * 128, DST2(l0 + BOFF(c) + u * 2048 + 1024)); dma(rsBI, vb1_, (c) * 128, DST2(l0 + BOFF(c) + 7168 + u * 2048 + 1024)); } while (0)
        STAMP(14);
        asm volatile("s_waitcnt vmcnt(0)" : "+v"(p0), "+v"(p1), "+v"(p2) :: "memory");
        if (master) BUMP();
        f32x2 lam, coef; cf lam8, lam16, lam64, wlo, whi, zG;
        {   const float lr = bf2f(p0), li = bf2f(p1), dt = bfround(fexp(bf2f(p2)));
            const float er = fexp(dt * lr), rev = __builtin_amdgcn_fractf(dt * li * 0.15915494309189535f);
            const cf l1 = {er * __builtin_amdgcn_cosf(rev), er * __builtin_amdgcn_sinf(rev)};
            const float inv = __builtin_amdgcn_rcpf(lr * lr + li * li);
            const cf cq = cmul((cf){l1.r - 1.0f, l1.i}, (cf){lr * inv, -li * inv});
            lam = (f32x2){l1.r, l1.i}; coef = (f32x2){cq.r, cq.i};
            const cf l2 = cmul(l1, l1), l4 = cmul(l2, l2); lam8 = cmul(l4, l4); lam16 = cmul(lam8, lam8);
            const cf l32 = cmul(lam16, lam16); lam64 = cmul(l32, l32);
            const cf l128 = cmul(lam64, lam64), l256 = cmul(l128, l128), l512 = cmul(l256, l256), l1024 = cmul(l512, l512), one = {1.f, 0.f};
            wlo = cmul((G & 1) ? lam16 : one, (G & 2) ? l32 : one); whi = cmul(wlo, lam8);
            zG = cmul((G & 1) ? l512 : one, (G & 2) ? l1024 : one);
            asm volatile("" : "+v"(lam), "+v"(coef), "+v"(lam8.r), "+v"(lam8.i), "+v"(lam16.r), "+v"(lam16.i), "+v"(lam64.r), "+v"(lam64.i),
                              "+v"(wlo.r), "+v"(wlo.i), "+v"(whi.r), "+v"(whi.i), "+v"(zG.r), "+v"(zG.i) :: "memory"); }
        const int rr = u < 3 ? r16 : r16 & 7;
        const unsigned bro = u * 2048 + rr * 128 + ((G ^ (rr >> 1)) << 4);
        const lp br0 = lbase(L, L_WK + bro), br1 = lbase(L, L_WK + (bro ^ 64)), br2 = lbase(L, L_B0 + bro), br3 = lbase(L, L_B0 + (bro ^ 64));
        lp at[4][2];
        UNROLL for (int m = 0; m < 4; ++m) { const int row = 16 * (r16 >> 2) + 2 * m + ((r16 & 3) >> 1) + 8 * (r16 & 1); const unsigned a = L_X + row * 128 + ((G ^ xswz(row)) << 4);
            at[m][0] = lbase(L, a); at[m][1] = lbase(L, a ^ 64); }
        if (master) { _Pragma("unroll 1") for (unsigned spin = 0; lread(L, L_KD) < 1u && spin < SPIN_CAP; ++spin) { BUMP(); __builtin_amdgcn_s_sleep(8); } BUMP(); wait_vm<0>(); }
        if (B0EARLY) BD(0);
        lwait(L, L_KD, 1u);
        STAMP(1);
        if (!B0EARLY) BD(0);
        BD(1); BD(2);
        u32x4 bq[4][4];
        UNROLL for (int c = 0; c < 4; ++c)
            asm volatile("buffer_load_dwordx4 %0, %4, %6, %8 offen\n\tbuffer_load_dwordx4 %1, %4, %7, %8 offen\n\tbuffer_load_dwordx4 %2, %5, %6, %8 offen\n\tbuffer_load_dwordx4 %3, %5, %7, %8 offen"
                         : "=&v"(bq[c][0]), "=&v"(bq[c][1]), "=&v"(bq[c][2]), "=&v"(bq[c][3]) : "v"(vb0_), "v"(vb1_), "s"(rsBR), "s"(rsBI), "s"((c + 3) * 128) : "memory");
        const lp bw0 = lbase(L, L_B0 + u * 2048 + l16), bw1 = lbase(L, u < 3 ? L_B0 + u * 2048 + 1024 + l16 : L_DUMMY + l16), bw2 = lbase(L, u < 3 ? L_B0 + 7168 + u * 2048 + 1024 + l16 : L_DUMMY + l16);
        f32x4 ur[4] = {}, ui[4] = {};
        bf16x8 fb[2][4], fx[2][8];
#define BP(c, k) (((c) == 0 || (c) >= 3) ? ((k) ? br3 : br2) : ((k) ? br1 : br0) + ((c) - 1) * 14336)
#define BRD(c) do { if ((c) < 3) wait_vm<16 + 4 * (2 - ((c) < 3 ? (c) : 2))>(); else { \
                        asm volatile("s_waitcnt vmcnt(%4)" : "+v"(bq[(c) < 3 ? 0 : (c) - 3][0]), "+v"(bq[(c) < 3 ? 0 : (c) - 3][1]), "+v"(bq[(c) < 3 ? 0 : (c) - 3][2]), "+v"(bq[(c) < 3 ? 0 : (c) - 3][3]) : "i"(4 * (6 - (c))) : "memory"); \
                        *(LAS u32x4*)(bw0) = bq[(c) < 3 ? 0 : (c) - 3][0]; *(LAS u32x4*)(bw0 + 7168) = bq[(c) < 3 ? 0 : (c) - 3][1]; *(LAS u32x4*)(bw1) = bq[(c) < 3 ? 0 : (c) - 3][2]; *(LAS u32x4*)(bw2) = bq[(c) < 3 ? 0 : (c) - 3][3]; \
                        asm volatile("" ::: "memory"); }                \
                    fb[(c) & 1][0] = ld128(BP(c, 0)); fb[(c) & 1][1] = ld128(BP(c, 0) + 7168); fb[(c) & 1][2] = ld128(BP(c, 1)); fb[(c) & 1][3] = ld128(BP(c, 1) + 7168); asm volatile("" ::: "memory"); } while (0)
#define XRD(c, i) fx[(c) & 1][i] = ld128(at[(i) & 3][(i) >> 2] + (c) * 8192)
#define UCH(c) UNROLL for (int i = 0; i < 8; ++i) { \
            if (i == BRI && (c) < 6) BRD((c) < 6 ? (c) + 1 : 6); \
            ur[i & 3] = MFMA16(fx[(c) & 1][i], fb[(c) & 1][2 * (i >> 2)], ur[i & 3]); ui[i & 3] = MFMA16(fx[(c) & 1][i], fb[(c) & 1][2 * (i >> 2) + 1], ui[i & 3]); \
            if ((c) < 6) XRD((c) + 1, i); \
            SBAR(); }
        BRD(0);
        lwait(L, L_XA, 4u);
        STAMP(2);
        UNROLL for (int i = 0; i < 8; ++i) XRD(0, i);
        SBAR();
        UCH(0) UCH(1) UCH(2)
        __builtin_amdgcn_s_setprio(UPRIO);
        UCH(3) UCH(4) UCH(5) UCH(6)
        if (TCW == 0) {
        if (u < 3) TOUCH(u == 0 ? wo : u == 1 ? cre : cim, DM * ROWB, lane < 25 ? (b * 25 + lane) * 64 : OOB);
        }
        lcount(L, L_UF, lane);
        asm volatile("s_nop 7" : "+v"(ur[0]), "+v"(ur[1]), "+v"(ur[2]), "+v"(ur[3]), "+v"(ui[0]), "+v"(ui[1]), "+v"(ui[2]), "+v"(ui[3]));
        STAMP(3);
        f32x2 hr, hm;
        UNROLL for (int p = 0; p < 8; ++p) {
            f32x2 tr, ti; pk_cmul(tr, ti, coef, PAIR(ur, p), PAIR(ui, p));
            ur[p >> 1][2 * (p & 1)] = tr[0]; ur[p >> 1][2 * (p & 1) + 1] = tr[1]; ui[p >> 1][2 * (p & 1)] = ti[0]; ui[p >> 1][2 * (p & 1) + 1] = ti[1];
            if (p == 0) { hr = tr; hm = ti; } else { f32x2 nr, ni; pk_cfma(nr, ni, lam, hr, hm, tr, ti); hr = nr; hm = ni; }
        }
        const cf elo = {hr[0], hm[0]}, e16 = cfma(lam8, elo, (cf){hr[1], hm[1]});
        cf e0, e1, e2, e3; bcast4(e16.r, e0.r, e1.r, e2.r, e3.r); bcast4(e16.i, e0.i, e1.i, e2.i, e3.i);
        const cf t2 = cfma(lam16, e0, e1), t3 = cfma(lam16, t2, e2), eb = cfma(lam16, t3, e3);
        lwait(L, L_KR, 1u); lwait(L, L_QF, 4u);
        __builtin_amdgcn_raw_buffer_store_b64((u32x2){nosent(__float_as_uint(eb.r)), nosent(__float_as_uint(eb.i))}, rsWS, eoff, WS_E + (blk * DM + 56 * j) * 8, SC1);
        if (TCW == 1) {
        if (u < 3) TOUCH(u == 0 ? wo : u == 1 ? cre : cim, DM * ROWB, lane < 25 ? (b * 25 + lane) * 64 : OOB);
        }
        const cf pg = {G == 0 ? 0.f : G == 1 ? e0.r : G == 2 ? t2.r : t3.r, G == 0 ? 0.f : G == 1 ? e0.i : G == 2 ? t2.i : t3.i}, ph = cfma(lam8, pg, elo);
        STAMP(4);
        asm volatile("s_mov_b32 m0, %0\n\ts_nop 0\n\tbuffer_load_dword %1, %2, %3 offen sc1 lds"
                     :: "s"(l0 + L_DUMMY), "v"(u == 0 ? lane * 128 : OOB), "s"(rsWS), "s"(WS_KV + (b >> 3) * 8192) : "memory");
        st128((u32x4){~0u, ~0u, ~0u, ~0u}, rsWS, l16, opiece, SC1);
        st128((u32x4){~0u, ~0u, ~0u, ~0u}, rsWS, l16, opiece + 1024, SC1);
        const float gt = *(LAS const float*)(L + L_G + (32 * th + r32) * 4), go = *(LAS const float*)(L + L_G + (32 * (th ^ 1) + r32) * 4);
        lwait(L, L_UF, 4u);
        const rsrc_t rsCR = mkrs(cre, DM * ROWB), rsCI = mkrs(cim, DM * ROWB);
        const unsigned vc = (56 * j + 8 * u + r8) * ROWB + ((sl ^ ((4 * u + (r8 >> 1)) & 7)) << 4), vc2 = u < 3 ? vc + 32 * ROWB : OOB;
#define CD(n) do { const int cc_ = (n) >> 2; \
            if (((n) & 3) == 0) dma(rsCR, vc, cc_ * 128, l0 + L_C + cc_ * 7168 + u * 1024); else if (((n) & 3) == 1) dma(rsCI, vc, cc_ * 128, l0 + L_CIM + cc_ * 7168 + u * 1024); \
            else if (((n) & 3) == 2) dma(rsCR, vc2, cc_ * 128, DST2(l0 + L_C + cc_ * 7168 + (u + 4) * 1024)); else dma(rsCI, vc2, cc_ * 128, DST2(l0 + L_CIM + cc_ * 7168 + (u + 4) * 1024)); } while (0)
        const rsrc_t rsE = mkrs(ws + WS_E + 56 * j * 8, 32 * DM * 8 - 56 * j * 8);
        const int ebase = (blk - 1 - 8 * G) * (DM * 8) + st * 8;
        const unsigned hoff = lane == 0 ? WS_HCNT + blk * 128 : OOB;
        UNROLL for (int n = 0; n < ELDN; ++n) { CD(n); PACE(); }
        STAMP(38);
        {
            u32x2 ev[8];
            UNROLL for (int m = 0; m < 8; m += 2)
                asm volatile("buffer_load_dwordx2 %0, %2, %4, 0 offen sc1\n\tbuffer_load_dwordx2 %1, %3, %4, 0 offen sc1"
                             : "=&v"(ev[m]), "=&v"(ev[m + 1]) : "v"(ebase - m * (DM * 8)), "v"(ebase - (m + 1) * (DM * 8)), "s"(rsE) : "memory");
            UNROLL for (int n = ELDN; n < ELDN + 5; ++n) { PACE(); CD(n); }
            asm volatile("s_waitcnt vmcnt(5)" : "+v"(ev[0]), "+v"(ev[1]), "+v"(ev[2]), "+v"(ev[3]), "+v"(ev[4]), "+v"(ev[5]), "+v"(ev[6]), "+v"(ev[7]) :: "memory");
            _Pragma("unroll 1") for (unsigned spin = 0; spin < SPIN_CAP; ++spin) {
                unsigned mx = 0;
                UNROLL for (int m = 0; m < 8; ++m) mx = max(mx, max(ev[m][0], ev[m][1]));
                if (__builtin_expect(__builtin_amdgcn_ballot_w64(mx == ~0u) == 0, 1)) break;
                __builtin_amdgcn_s_sleep(8);
                asm volatile("" ::: "memory");
                UNROLL for (int m = 0; m < 8; ++m) ev[m] = __builtin_amdgcn_raw_buffer_load_b64(rsE, ebase - m * (DM * 8), 0, SC1);
            }
            STAMP(39);
            cf part = {__uint_as_float(ev[7][0]), __uint_as_float(ev[7][1])};
            UNROLL for (int m = 6; m >= 0; --m) part = cfma(lam64, part, (cf){__uint_as_float(ev[m][0]), __uint_as_float(ev[m][1])});
            part = cmul(zG, part);
            if (ebase < 0) part = (cf){0.f, 0.f};
            PIN(part.r); SBAR(); CD(ELDN + 5); SBAR();
            part.r = sum4(part.r); part.i = sum4(part.i);
            const cf ilo = cfma(wlo, part, pg), ihi = cfma(whi, part, ph);
            hr = (f32x2){ilo.r, ihi.r}; hm = (f32x2){ilo.i, ihi.i};
            u32x4 pr0, pr1, pi0, pi1;
            UNROLL for (int p = 0; p < 8; ++p) {
                f32x2 nr, ni; pk_cfma(nr, ni, lam, hr, hm, PAIR(ur, p), PAIR(ui, p)); hr = nr; hm = ni;
                unsigned cr, ci; asm("v_cvt_pk_bf16_f32 %0, %2, %3\n\tv_cvt_pk_bf16_f32 %1, -%4, -%5" : "=&v"(cr), "=&v"(ci) : "v"(hr[0]), "v"(hr[1]), "v"(hm[0]), "v"(hm[1]));
                if (p < 4) { pr0[p & 3] = cr; pi0[p & 3] = ci; } else { pr1[p & 3] = cr; pi1[p & 3] = ci; }
                if (p == 1 || p == 5) { PIN(hr); SBAR(); CD(ELDN + 6 + (p >> 2)); SBAR(); }
            }
            const unsigned sb = L_HS + u * 4096, wa = sb + r16 * 128 + (((2 * G) ^ (r16 & 7)) << 4);
            const int tok = (lane & 48) + (r16 >> 1) + 8 * (lane & 1);
            const unsigned gha = (tok >> 5) * 57344 + (tok & 31) * 16, ghb = u < 3 ? gha + 1024 : OOB, gs = WS_H + blk * 114688 + (7 * j + 2 * u) * 1024;
            const unsigned rao = sb + tq * 128 + (((2 * G + (tp >> 1)) ^ tq) << 4) + 8 * (tp & 1);
            const lp wa0 = lbase(L, wa), wa1 = lbase(L, wa ^ 16), ra = lbase(L, rao), rb = lbase(L, (rao ^ 64) + 512);
            *(LAS u32x4*)(wa0) = pr0; *(LAS u32x4*)(wa1) = pr1; *(LAS u32x4*)(wa0 + 2048) = pi0; *(LAS u32x4*)(wa1 + 2048) = pi1;
            asm volatile("" ::: "memory");
            UNROLL for (int pl = 0; pl < 2; ++pl) {
                const bf16x8 c0 = ldtr2(ra + pl * 2048, rb + pl * 2048), c1 = ldtr2(ra + pl * 2048 + 1024, rb + pl * 2048 + 1024);
                st128(__builtin_bit_cast(u32x4, c0), rsWS, gha + pl * 512, gs, SC1);
                st128(__builtin_bit_cast(u32x4, c1), rsWS, ghb + pl * 512, gs, SC1);
            }
        }
        STAMP(5);
        lcount(L, L_SF, lane);
        UNROLL for (int n = ELDN + 8; n < 28; ++n) { PACE(); CD(n); }
        wait_vm<28 - ELDN - 8>();
        atomic_inc(rsWS, hoff);
        lwait(L, L_SF, 8u);
        { const rsrc_t rsWO = mkrs(wo, DM * ROWB);
          UNROLL for (int n = 0; n < 14; ++n) { dma(rsWO, vw, (u * 14 + n) * 8 * ROWB, l0 + L_WO + (u * 14 + n) * 1024); PACE(); } }
        STAMP(40);
        _Pragma("unroll 1") for (unsigned spin = 0; __builtin_amdgcn_readfirstlane(atomic_read(rsWS, hoff)) != 32 && spin < SPIN_CAP; ++spin) __builtin_amdgcn_s_sleep(8);
        lcount(L, L_WF, lane);
        const unsigned hb = WS_H + blk * 114688 + u * 1024;
        u32x4 h0[14], h1[14];
        const unsigned cx = (32 * (G & 1) + 8 * tp) ^ (((tq >> 1) & 1) << 6), nb = L_WO + (112 * u + tq) * 128, wa_ = nb + hi * 512;
        const lp wa0_ = lbase(L, wa_ + cx), wa1_ = lbase(L, wa_ + (cx ^ 64)), nb0 = lbase(L, nb + cx), nb1 = lbase(L, nb + (cx ^ 64));
        lwait(L, L_WF, 4u);
        bf16x8 wf0[8], wf1[8];
        UNROLL for (int k = 0; k < 8; ++k) { const unsigned ro = (28 * (k >> 1) + 16 * (k & 1)) * 128;
            wf0[k] = ldtr2(wa0_ + ro, ((k & 1) ? nb0 : wa0_) + ro + 1024); wf1[k] = ldtr2(wa1_ + ro, ((k & 1) ? nb1 : wa1_) + ro + 1024); }
        lcount(L, L_UF, lane);
        STAMP(42);
        UNROLL for (int m = 0; m < 14; ++m) { h0[m] = __builtin_amdgcn_raw_buffer_load_b128(rsWS, l16, hb + th * 57344 + m * 4096, SC1); PACE(); }
        lwait_idle(L, L_QL, 4u);
        UNROLL for (int m = 0; m < 14; ++m) h1[m] = __builtin_amdgcn_raw_buffer_load_b128(rsWS, l16, hb + (th ^ 1) * 57344 + m * 4096, SC1);
        const int rbc = min(32 + r32, 55);
        const unsigned cao = hi * L_CIM + r32 * 128 + ((u ^ ((r32 >> 1) & 7)) << 4), cbo = hi * L_CIM + rbc * 128 + ((u ^ ((rbc >> 1) & 7)) << 4);
        const lp ca0 = lbase(L, cao), ca1 = lbase(L, cao ^ 64), cb0 = lbase(L, cbo), cb1 = lbase(L, cbo ^ 64);
        const lp rp = lbase(L, L_RED + th * 16384 + u * 4096 + l16), rq = lbase(L, L_RED + (th ^ 1) * 16384 + u * 4096 + l16);
        const unsigned oa = WS_O + blk * 65536 + (th * 16 + 4 * u) * 2048, ob = WS_O + blk * 65536 + ((th ^ 1) * 16 + 4 * u) * 2048;
        f32x2 s2 = {2.0f * (1.0f - gt), 0.f}, s3 = {2.0f * (1.0f - go), 0.f}; PIN(s2); PIN(s3);
        STAMP(41);
        __builtin_amdgcn_s_setprio(0);
        lwait_idle(L, L_QE, 4u);
        __builtin_amdgcn_s_sleep(YSLEEP);
        STAMP(15);
        f32x16 y[4]; y[0] = y[1] = y[2] = y[3] = (f32x16){};
        u32x4 of[16];
#define OLD(k0) UNROLL for (int k = (k0); k < (k0) + 8; ++k) of[k] = __builtin_amdgcn_raw_buffer_load_b128(rsWS, l16 + (k & 3) * 1024, (k < 8 ? oa : ob) + ((k >> 2) & 1) * 4096, SC1)
        {   bf16x8 fa[28], fc[28];
            UNROLL for (int m = -YD; m < 28; ++m) {
                if (m + YD < 28) { const int n = (m + YD) % 14; fa[m + YD] = ld128(((n & 1) ? ca1 : ca0) + (n >> 1) * 7168); fc[m + YD] = ld128(((n & 1) ? cb1 : cb0) + (n >> 1) * 7168); }
                if (m >= 0 && m < 14) { y[0] = MFMA32(fa[m], asbf(h0[m]), y[0]); y[1] = MFMA32(fc[m], asbf(h0[m]), y[1]); }
                if (m >= 14) { y[2] = MFMA32(fa[m], asbf(h1[m - 14]), y[2]); y[3] = MFMA32(fc[m], asbf(h1[m - 14]), y[3]); }
                if (m >= 16 && m < 24) { UNROLL for (int k = 0; k < 2; ++k) { const f32x2 a = pk_scale(s2, (f32x2){y[k][2 * (m - 16)], y[k][2 * (m - 16) + 1]}); y[k][2 * (m - 16)] = a[0]; y[k][2 * (m - 16) + 1] = a[1]; } PIN(y[0]); PIN(y[1]); }
                if (OM < 28 && m == OM) { lwait(L, L_OD, ODN); if (OSLEEP) __builtin_amdgcn_s_sleep(OSLEEP); STAMP(43); OLD(0); }
                if (OM < 28 && m == OM + 2) { OLD(8); }
                if (m == 26) ladd(L, L_YF, 1u);
                SBAR();
            } }
        STAMP(9);
        lwait(L, L_YF, 4u);
        asm volatile("s_nop 11" : "+v"(y[2]), "+v"(y[3]));
        UNROLL for (int k = 2; k < 4; ++k) UNROLL for (int e = 0; e < 8; ++e) { const f32x2 a = pk_scale(s3, (f32x2){y[k][2 * e], y[k][2 * e + 1]}); y[k][2 * e] = a[0]; y[k][2 * e + 1] = a[1]; }
        asm volatile("s_nop 1" : "+v"(y[2]), "+v"(y[3]));
        if (OM >= 28) { lwait(L, L_OD, ODN); if (OSLEEP) __builtin_amdgcn_s_sleep(OSLEEP); STAMP(43); OLD(0); OLD(8); }
#define OMAX(mx, k) mx = max(max(mx, max(of[k][0], of[k][1])), max(of[k][2], of[k][3]))
#define OFIX(mx, k0) do { if (__builtin_expect(__builtin_amdgcn_ballot_w64(mx == ~0u) != 0, 0)) { _Pragma("unroll 1") for (unsigned spin = 0; spin < SPIN_CAP; ++spin) { \
            asm volatile("" ::: "memory");                              \
            OLD(k0); mx = 0; UNROLL for (int k = (k0); k < (k0) + 8; ++k) OMAX(mx, k); \
            if (__builtin_amdgcn_ballot_w64(mx == ~0u) == 0) break; } } } while (0)
        SBAR();
        unsigned mxa = 0, mxb = 0;
        UNROLL for (int k = 0; k < 8; ++k) OMAX(mxa, k);
        OFIX(mxa, 0);
        STAMP(10);
#define REDW(p, Y, e) *(LAS f32x4*)((p) + (e) * 1024) = (f32x4){Y[4 * (e)], Y[4 * (e) + 1], Y[4 * (e) + 2], Y[4 * (e) + 3]}
        UNROLL for (int k = 0; k < 8; ++k) { y[0] = MFMA32(wf0[k], asbf(of[k]), y[0]); SBAR(); OMAX(mxb, 8 + k); PIN(mxb); SBAR(); }
        UNROLL for (int k = 0; k < 8; ++k) { y[1] = MFMA32(wf1[k], asbf(of[k]), y[1]); SBAR(); if (k >= 3 && k < 7) REDW(rp, y[0], k - 3); SBAR(); }
        OFIX(mxb, 8);
        UNROLL for (int k = 0; k < 8; ++k) { y[2] = MFMA32(wf0[k], asbf(of[8 + k]), y[2]); SBAR(); if (k >= 3 && k < 7) REDW(rp + 32768, y[1], k - 3); SBAR(); }
        UNROLL for (int k = 0; k < 8; ++k) { y[3] = MFMA32(wf1[k], asbf(of[8 + k]), y[3]); SBAR(); if (k >= 3 && k < 7) REDW(rq, y[2], k - 3); SBAR(); }
        UNROLL for (int e = 0; e < 4; ++e) REDW(rq + 32768, y[3], e);
        lds_bar();
    }
    STAMP(11);
}

extern "C" void kernel_launch(void* const* d_in, const int* in_sizes, int n_in, void* d_out, int out_size, void* d_ws, size_t ws_size,
                              hipStream_t stream) {
    const uint16_t* const* in = (const uint16_t* const*)d_in;
    gated_mix_kernel<<<256, 512, 0, stream>>>(in[0], in[1], in[2], in[3], in[4], in[6], in[7], in[8], in[9], in[10], in[11], in[12], in[13], in[14],
                                              (float*)d_out, (char*)d_ws);
}
```

```cpp
#include <hip/hip_runtime.h>
#include <hip/hip_bf16.h>
#include <stdint.h>

typedef __bf16 bf16x8 __attribute__((ext_vector_type(8)));
typedef __bf16 bf16x2 __attribute__((ext_vector_type(2)));
typedef float f32x16 __attribute__((ext_vector_type(16)));
typedef float f32x4 __attribute__((ext_vector_type(4)));
typedef float f32x2 __attribute__((ext_vector_type(2)));
typedef unsigned u32x4 __attribute__((ext_vector_type(4)));
typedef unsigned u32x2 __attribute__((ext_vector_type(2)));
typedef short s16x4 __attribute__((ext_vector_type(4)));
typedef __amdgpu_buffer_rsrc_t rsrc_t;
#define LAS __attribute__((address_space(3)))
#define DI __device__ __forceinline__
#define SBAR() __builtin_amdgcn_sched_barrier(0)
#define PIN(x) asm volatile("" : "+v"(x))
#define PINS(x) asm volatile("" : "+s"(x))
#define MFMA32(a, b, c) __builtin_amdgcn_mfma_f32_32x32x16_bf16((a), (b), (c), 0, 0, 0)
#define MFMA16(a, b, c) __builtin_amdgcn_mfma_f32_16x16x32_bf16((a), (b), (c), 0, 0, 0)
#define UNROLL _Pragma("unroll")
#define PACE() asm volatile("s_sleep %0" :: "i"(CSLEEP) : "memory")
#define SC1 16

#define CSLEEP 3
#define YSLEEP 1
#define CPREF 32768
#define WQ2 8
#define YD 3
#define OSLEEP 6
#define PACKED 1
#define ODN 4u
#define PFC 0
#define B0EARLY 0
#define KCHK 29
#define PFE 0
#define ODRAIN 0
#define ELDN 12
#define QD_ 7
#define OM 28
#define BRI 7
#define TCW 0
#define LSLEEP 0
#define REGB 1
#define HP 9
#define YPRIO 0
#define AMF 1
#define QREP 2
#define KPOLL 16
#define UPRIO 1
#define SPRIO 0
#define OM0 16
#define OM1 22
#define MAP42 0

constexpr int SEQ = 2048, DM = 448, ROWB = DM * 2;
constexpr unsigned WS_KVCNT = 0;
constexpr unsigned WS_HCNT = 16384;
constexpr unsigned WS_X = 20480;
constexpr unsigned OOB = 0x7FFFFFF0u;
constexpr unsigned WS_KV = 65536;
constexpr unsigned WS_E = WS_KV + 262144;
constexpr unsigned WS_H = WS_E + 131072;
constexpr unsigned WS_O = WS_H + 32 * 114688;
constexpr unsigned WS_END = WS_O + 32 * 65536;
constexpr unsigned WS_STAMP = WS_END;

constexpr unsigned L_X = 0, L_WK = 57344, L_WQ = 86016, L_B0 = 143360, L_G = 160768;
constexpr unsigned L_KD = 161024, L_XA = 161028, L_XF = 161060, L_QD = 161032, L_QF = 161036, L_UF = 161040, L_WF = 161044, L_KR = 161048, L_YF = 161052, L_XG = 161064, L_GV = 161068, L_QE = 161072, L_SF = 161076, L_QL = 161080, L_OD = 161056, L_FB = 161084;
constexpr unsigned L_QSH = 122880, L_HS = 106496;
constexpr unsigned L_C = 0, L_CIM = 50176, L_WO = 100352;
constexpr unsigned L_RO = 131072, L_LS = 155648;
constexpr unsigned L_RED = 0, L_DUMMY = 157696;
constexpr unsigned LDS_BYTES = 163840;
constexpr unsigned SPIN_CAP = 1u << 14;

DI unsigned cvt2(float a, float b) { f32x2 v = {a, b}; return __builtin_bit_cast(unsigned, __builtin_convertvector(v, bf16x2)); }
DI float bf2f(unsigned h) { return __uint_as_float(h << 16); }
DI float bfhi(unsigned h) { return __uint_as_float(h & 0xffff0000u); }
DI float bfround(float x) { return __uint_as_float(cvt2(x, 0.f) << 16); }
DI float fexp(float x) { return __builtin_amdgcn_exp2f(x * 1.4426950408889634f); }
DI rsrc_t mkrs(const void* p, unsigned bytes) { return __builtin_amdgcn_make_buffer_rsrc((void*)p, (short)0, (int)bytes, 0x00020000); }
DI void dma(rsrc_t rs, unsigned voff, unsigned soff, unsigned lds) {
    asm volatile("s_mov_b32 m0, %0\n\ts_nop 0\n\tbuffer_load_dwordx4 %1, %2, %3 offen lds" :: "s"(lds), "v"(voff), "s"(rs), "s"(soff) : "memory"); }
template <int N> DI void wait_bar() { asm volatile("s_waitcnt vmcnt(%0) lgkmcnt(0)\n\ts_barrier" :: "i"(N) : "memory"); }
template <int N> DI void wait_vm() { asm volatile("s_waitcnt vmcnt(%0)" :: "i"(N) : "memory"); }
DI void lds_bar() { asm volatile("s_waitcnt lgkmcnt(0)\n\ts_barrier" ::: "memory"); }
typedef LAS char* lp;
DI lp lbase(LAS char* L, unsigned off) { lp p = L + off; PIN(p); return p; }
DI bf16x8 ld128(lp p) { return __builtin_bit_cast(bf16x8, *(LAS const u32x4*)p); }
DI bf16x8 ldtr2(lp p0, lp p1) {
    s16x4 a = __builtin_amdgcn_ds_read_tr16_b64_v4i16((LAS s16x4*)p0), b = __builtin_amdgcn_ds_read_tr16_b64_v4i16((LAS s16x4*)p1);
    return __builtin_bit_cast(bf16x8, __builtin_shufflevector(a, b, 0, 1, 2, 3, 4, 5, 6, 7)); }
DI bf16x8 asbf(u32x4 v) { return __builtin_bit_cast(bf16x8, v); }
DI void st128(u32x4 v, rsrc_t rs, unsigned voff, unsigned soff, int aux) {
    if (aux) __builtin_amdgcn_raw_buffer_store_b128(v, rs, voff, soff, SC1); else __builtin_amdgcn_raw_buffer_store_b128(v, rs, voff, soff, 0);
    asm volatile("s_nop 1" :: "v"(v)); }
DI unsigned nosent(unsigned v) { return v < 0xFFFFFFFEu ? v : 0xFFFFFFFEu; }
DI unsigned atomic_read(rsrc_t rs, unsigned voff) { return (unsigned)__builtin_amdgcn_raw_ptr_buffer_atomic_add_i32(0, rs, (int)voff, 0, SC1); }
DI void atomic_inc(rsrc_t rs, unsigned voff) { asm volatile("buffer_atomic_add %0, %1, %2, 0 offen sc1" :: "v"(1u), "v"(voff), "s"(rs) : "memory"); }
struct cf { float r, i; };
DI cf cmul(cf a, cf b) { cf o;
    asm("v_mul_f32 %0, %3, %5\n\tv_mul_f32 %1, %3, %4\n\tv_fma_f32 %0, %2, %4, -%0\n\tv_fma_f32 %1, %2, %5, %1" : "=&v"(o.r), "=&v"(o.i) : "v"(a.r), "v"(a.i), "v"(b.r), "v"(b.i)); return o; }
DI cf cfma(cf a, cf b, cf c) { cf o;
    asm("v_fma_f32 %0, -%3, %5, %6\n\tv_fma_f32 %1, %3, %4, %7\n\tv_fma_f32 %0, %2, %4, %0\n\tv_fma_f32 %1, %2, %5, %1"
        : "=&v"(o.r), "=&v"(o.i) : "v"(a.r), "v"(a.i), "v"(b.r), "v"(b.i), "v"(c.r), "v"(c.i)); return o; }
DI void pk_cfma(f32x2& nr, f32x2& ni, f32x2 l, f32x2 hr, f32x2 hm, f32x2 a, f32x2 c) {
    asm("v_pk_fma_f32 %0, %2, %3, %5 op_sel_hi:[0,1,1]\n\tv_pk_fma_f32 %1, %2, %4, %6 op_sel_hi:[0,1,1]\n\t"
        "v_pk_fma_f32 %0, %2, %4, %0 op_sel:[1,0,0] neg_lo:[1,0,0] neg_hi:[1,0,0]\n\tv_pk_fma_f32 %1, %2, %3, %1 op_sel:[1,0,0]"
        : "=&v"(nr), "=&v"(ni) : "v"(l), "v"(hr), "v"(hm), "v"(a), "v"(c)); }
DI void pk_cmul(f32x2& nr, f32x2& ni, f32x2 l, f32x2 hr, f32x2 hm) {
    asm("v_pk_mul_f32 %0, %2, %3 op_sel_hi:[0,1]\n\tv_pk_mul_f32 %1, %2, %4 op_sel_hi:[0,1]\n\t"
        "v_pk_fma_f32 %0, %2, %4, %0 op_sel:[1,0,0] neg_lo:[1,0,0] neg_hi:[1,0,0]\n\tv_pk_fma_f32 %1, %2, %3, %1 op_sel:[1,0,0]"
        : "=&v"(nr), "=&v"(ni) : "v"(l), "v"(hr), "v"(hm)); }
DI f32x2 pk_scale(f32x2 s, f32x2 a) { f32x2 r; asm("v_pk_mul_f32 %0, %1, %2 op_sel_hi:[0,1]" : "=v"(r) : "v"(s), "v"(a)); return r; }
DI float sum4(float v) {
    auto a = __builtin_amdgcn_permlane16_swap(__float_as_uint(v), __float_as_uint(v), false, false); v = __uint_as_float(a[0]) + __uint_as_float(a[1]);
    auto c = __builtin_amdgcn_permlane32_swap(__float_as_uint(v), __float_as_uint(v), false, false); return __uint_as_float(c[0]) + __uint_as_float(c[1]); }
DI void bcast4(float v, float& r0, float& r1, float& r2, float& r3) {
    auto a = __builtin_amdgcn_permlane16_swap(__float_as_uint(v), __float_as_uint(v), false, false);
    auto e = __builtin_amdgcn_permlane32_swap(a[0], a[0], false, false); auto o = __builtin_amdgcn_permlane32_swap(a[1], a[1], false, false);
    r0 = __uint_as_float(e[0]); r2 = __uint_as_float(e[1]); r1 = __uint_as_float(o[0]); r3 = __uint_as_float(o[1]); }
DI float rowmax16(const f32x16& s) {
    float a = fmaxf(fmaxf(s[0], s[1]), s[2]), b = fmaxf(fmaxf(s[3], s[4]), s[5]);
    a = fmaxf(fmaxf(a, s[6]), s[7]); b = fmaxf(fmaxf(b, s[8]), s[9]); a = fmaxf(fmaxf(a, s[10]), s[11]); b = fmaxf(fmaxf(b, s[12]), s[13]);
    a = fmaxf(fmaxf(a, s[14]), s[15]); a = fmaxf(a, b);
    auto rr = __builtin_amdgcn_permlane32_swap(__float_as_uint(a), __float_as_uint(a), false, false);
    return fmaxf(__uint_as_float(rr[0]), __uint_as_float(rr[1])); }
DI void set_shift(u32x4& q1, float c, int hi) {
    const float c1 = bfround(c), c2 = bfround(c - c1), c3 = c - c1 - c2;
    if (hi) { q1[2] = cvt2(-c1, -c2); q1[3] = cvt2(-c3, 0.f); } }
DI void ladd(LAS char* L, unsigned off, unsigned v) {
    asm volatile("s_mov_b64 exec, 1\n\tds_add_u32 %0, %1\n\ts_mov_b64 exec, -1" :: "v"(L + off), "v"(v) : "memory"); }
DI void lcount(LAS char* L, unsigned off, int) { ladd(L, off, 1u); }
DI unsigned lread(LAS char* L, unsigned off) { return __builtin_amdgcn_readfirstlane(*(volatile LAS unsigned*)(L + off)); }
DI void lwait(LAS char* L, unsigned off, unsigned n) {
    if (__builtin_expect(lread(L, off) < n, 0)) { _Pragma("unroll 1") for (unsigned spin = 0; lread(L, off) < n && spin < (1u << 20); ++spin) { if (LSLEEP) __builtin_amdgcn_s_sleep(LSLEEP); } }
    asm volatile("" ::: "memory"); }
DI void lwait_idle(LAS char* L, unsigned off, unsigned n) {
    _Pragma("unroll 1") for (unsigned spin = 0; lread(L, off) < n && spin < (1u << 18); ++spin) __builtin_amdgcn_s_sleep(2);
    asm volatile("" ::: "memory"); }
DI int xswz(int row) { return (row >> 1) & 7; }
DI u32x4 pack8(const f32x16& a, int e) { return (u32x4){cvt2(a[e], a[e + 1]), cvt2(a[e + 2], a[e + 3]), cvt2(a[e + 4], a[e + 5]), cvt2(a[e + 6], a[e + 7])}; }
#define PAIR(v, p) (f32x2){v[2 * (p)], v[2 * (p) + 1]}

#define STAMP(k)
#define STAMPV(k)

__global__ void __launch_bounds__(512, 2)
gated_mix_kernel(const uint16_t* x, const uint16_t* wq, const uint16_t* wk, const uint16_t* wv, const uint16_t* wo, const uint16_t* lre, const uint16_t* lim,
                 const uint16_t* bre, const uint16_t* bim, const uint16_t* cre, const uint16_t* cim, const uint16_t* logdt, const uint16_t* dskip,
                 const uint16_t* gate, float* out, char* ws) {
    __shared__ __attribute__((aligned(16))) char smem[LDS_BYTES];
    LAS char* L = (LAS char*)smem;
    const unsigned l0 = (unsigned)(uintptr_t)L;
    const int lane = threadIdx.x & 63, w = __builtin_amdgcn_readfirstlane(threadIdx.x >> 6);
    const int b = blockIdx.x;
    STAMP(0);
    {   unsigned long long pc; asm volatile("s_getpc_b64 %0" : "=s"(pc));
        const rsrc_t rsC = mkrs((const void*)(pc & ~255ull), CPREF);
        asm volatile("s_mov_b32 m0, %0\n\ts_nop 4\n\tbuffer_load_dword %1, %2, 0 offen lds" :: "s"(l0 + L_DUMMY), "v"(threadIdx.x * 64), "s"(rsC) : "memory"); }
    const int blk = (b & 7) * 4 + (b >> 6), j = (b >> 3) & 7;
    const int th = w & 1, hd = (w >> 1) & 1;
    const int r32 = lane & 31, hi = lane >> 5, r16 = lane & 15, G = lane >> 4;
    const int tq = r16 >> 2, tp = lane & 3;
    const int sl = lane & 7, r8 = lane >> 3;
    const unsigned l16 = lane * 16;
    const int kind = j & 1, tg = j >> 1;
    unsigned vx = r8 * ROWB + ((sl ^ (r8 >> 1)) << 4); PIN(vx);
    PINS(x); PINS(wq); PINS(wk); PINS(wv); PINS(wo); PINS(lre); PINS(lim); PINS(bre); PINS(bim); PINS(cre); PINS(cim); PINS(logdt); PINS(dskip); PINS(gate); PINS(out); PINS(ws);
    const rsrc_t rsX = mkrs(x, SEQ * ROWB), rsWS = mkrs(ws, WS_END);
    const unsigned opiece = WS_O + blk * 65536 + (th * 16 + 2 * j + hd) * 2048;

    const rsrc_t rsWQ = mkrs(wq, DM * ROWB);
    const int pwq = sl ^ (((r8 >> 1) & 1) << 2);
    const unsigned vw = r8 * ROWB + 112 * j + (pwq << 4);
#define WQD(i0, i1) do { if (pwq != 7) { UNROLL for (int i = (i0); i < (i1); ++i) { const int p = i < 18 ? 3 * i + w - 1 : min(53 + w, 55); dma(rsWQ, vw, p * 8 * ROWB, l0 + L_WQ + p * 1024); } } } while (0)
#define BUMP() do { if (b == 0) atomic_inc(rsWS, lane < 16 ? WS_X + lane * 128 : OOB); } while (0)
    if (w < 4) {
        const int qrow = 32 * th + r32;
        const unsigned rob = L_RO + w * 2048 + l16, lsb = L_LS + (w * 32 + r32) * 16;
        lp a00, a01, a02, a03, a1, ro0, ro1, ro2, rop, lsp, lw0, lw1, lw2, lw3, qs0, qs1, qs2, qs3;
        unsigned kco, kc = 0u;
        u32x4 sel0, sel1, pw0, pw1, kf[4][2], vf[4][2];
        f32x16 o[4];
#define QPREP() do { const unsigned a0o = L_X + qrow * 128 + ((hi ^ xswz(qrow)) << 4); \
        a00 = lbase(L, a0o); a01 = lbase(L, a0o ^ 32); a02 = lbase(L, a0o ^ 64); a03 = lbase(L, a0o ^ 96); \
        a1 = lbase(L, L_WQ + (8 * hi + tq) * 128 + ((56 * hd + 32 * (G & 1) + 8 * tp) ^ (((tq >> 1) & 1) << 6))); \
        kco = lane < 16 ? WS_KVCNT + (b & 7) * 2048 + lane * 128 : OOB; PIN(kco); \
        ro0 = lbase(L, rob ^ 2048); ro1 = lbase(L, (rob ^ 4096) + 8192); ro2 = lbase(L, (rob ^ 6144) + 16384); rop = lbase(L, rob); lsp = lbase(L, lsb); \
        lw0 = lbase(L, hi ? (lsb ^ 512) + w * 4 : L_DUMMY); lw1 = lbase(L, hi ? (lsb ^ 1024) + w * 4 : L_DUMMY); lw2 = lbase(L, hi ? (lsb ^ 1536) + w * 4 : L_DUMMY); lw3 = lbase(L, hi ? lsb + w * 4 : L_DUMMY); \
        qs0 = lbase(L, L_QSH + (w ^ 1) * 2048 + l16); qs1 = lbase(L, L_QSH + (w ^ 2) * 2048 + l16); qs2 = lbase(L, L_QSH + (w ^ 3) * 2048 + l16); qs3 = lbase(L, L_QSH + w * 2048 + l16); \
        const int js = 4 * ((r32 >> 3) & 1) + (r32 & 3); const unsigned one = ((r32 >> 2) & 1) == hi ? 0x3F80u << (16 * (js & 1)) : 0u; \
        UNROLL for (int e = 0; e < 4; ++e) { const unsigned v = (js >> 1) == e ? one : 0u; sel0[e] = r32 < 16 ? v : 0u; sel1[e] = r32 < 16 ? 0u : v; } PIN(sel0); PIN(sel1); \
        o[0] = o[1] = o[2] = o[3] = (f32x16){}; PIN(o[0]); PIN(o[1]); PIN(o[2]); PIN(o[3]); \
        pw1 = (u32x4){0u, 0u, 0u, 0u}; vf[3][1] = pw1; PIN(pw1); PIN(vf[3][1]); } while (0)
        __builtin_amdgcn_s_setprio(2);
        if (w == 0) {
            const unsigned xoff = lane == 0 ? WS_X + (b & 15) * 128 : OOB;
            const rsrc_t rsWK = mkrs(kind ? wv : wk, DM * 56);
            const unsigned vxa = vx ^ (xswz(16 * tg) << 4), vxb = (vx ^ (xswz(16 * tg + 8) << 4)) + 8 * ROWB, xs = (blk * 64 + tg * 16) * ROWB;
            unsigned x0 = 0u, x1 = 0u;
            asm volatile("buffer_atomic_add %0, %1, %2, 0 offen sc0 sc1" : "+v"(x0) : "v"(xoff), "s"(rsWS) : "memory");
            STAMP(36);
            UNROLL for (int c = 0; c < 7; ++c) {
                dma(rsX, vxa, xs + c * 128, l0 + L_X + c * 8192 + tg * 2048); dma(rsX, vxb, xs + c * 128, l0 + L_X + c * 8192 + tg * 2048 + 1024);
                UNROLL for (int n = (7 * c + 1) / 2; n < (7 * c + 8) / 2; ++n) dma(rsWK, l16, n * 1024, l0 + L_WK + n * 1024);
            }
            asm volatile("s_barrier" ::: "memory");
            STAMP(14);
            __builtin_amdgcn_raw_buffer_store_b64((u32x2){~0u, ~0u}, rsWS, lane < 56 ? lane * 8 : OOB, WS_E + (blk * DM + 56 * j) * 8, SC1);
            const int cg0 = kind ? tp : 4 * (tp >> 1) + (tp & 1), cg1 = kind ? 4 + tp : 4 * (tp >> 1) + 2 + (tp & 1);
            const lp aw0 = lbase(L, L_WK + (8 * G + tq) * 56 + cg0 * 8), aw1 = lbase(L, L_WK + (8 * G + tq) * 56 + cg1 * 8);
            const unsigned axo = L_X + (16 * tg + r16) * 128 + ((G ^ xswz(16 * tg + r16)) << 4);
            const lp ax0 = lbase(L, axo), ax1 = lbase(L, axo ^ 64);
            const int sg = tg & 1;
            const unsigned tb = WS_KV + (2 * blk + (tg >> 1)) * 4096 + (kind ? 2048 + sg * 1024 : 0);
            unsigned kva = kind ? ((G >> 1) * 32 + r16) * 16 + (G & 1) * 8 : (G * 32 + 16 * sg + ((r16 & 3) | ((r16 & 4) << 1) | ((r16 & 8) >> 1))) * 16; PIN(kva);
            unsigned kvb = kind ? kva + 256 : kva + 8; PIN(kvb);
            const bool spare = kind ? r16 >= 12 : G == 3;
            unsigned sp2 = (kind && r16 != 12) ? 0u : 0x3F803F80u, sp3 = kind ? sp2 : 0x00003F80u; PIN(sp2); PIN(sp3);
            f32x4 d0 = {0.f, 0.f, 0.f, 0.f}, d1 = d0;
            bf16x8 xf[14], w0[14], w1[14];
#define KVR(c) do { wait_vm<1 + 39 - 2 * ((c) + 1) - (7 * (c) + 8) / 2>(); UNROLL for (int ks = 2 * (c); ks < 2 * (c) + 2; ++ks) { xf[ks] = ld128(((ks & 1) ? ax1 : ax0) + (c) * 8192); \
                        w0[ks] = ldtr2(aw0 + ks * 1792, aw0 + ks * 1792 + 224); w1[ks] = ldtr2(aw1 + ks * 1792, aw1 + ks * 1792 + 224); } SBAR(); } while (0)
#define KVM(c, K) do { UNROLL for (int ks = 2 * (c); ks < 2 * (c) + 2; ++ks) { \
                        if (K) { d0 = MFMA16(xf[ks], w0[ks], d0); d1 = MFMA16(xf[ks], w1[ks], d1); } else { d0 = MFMA16(w0[ks], xf[ks], d0); d1 = MFMA16(w1[ks], xf[ks], d1); } } SBAR(); } while (0)
#define KVP(K) do { KVR(0); STAMP(1); KVR(1); KVM(0, K); KVR(2); KVM(1, K); KVR(3); KVM(2, K); KVR(4); KVM(3, K); KVR(5); KVM(4, K); KVR(6); STAMP(32); KVM(5, K); KVM(6, K); } while (0)
            if (kind) KVP(1); else KVP(0);
            __builtin_amdgcn_raw_buffer_store_b64((u32x2){cvt2(d0[0], d0[1]), cvt2(d0[2], d0[3])}, rsWS, kva, tb, SC1);
            __builtin_amdgcn_raw_buffer_store_b64((u32x2){spare ? sp2 : cvt2(d1[0], d1[1]), spare ? sp3 : cvt2(d1[2], d1[3])}, rsWS, kvb, tb, SC1);
            asm volatile("buffer_atomic_add %0, %1, %2, 0 offen sc0 sc1" : "+v"(x1) : "v"(xoff), "s"(rsWS) : "memory");
            ladd(L, L_KD, 1u); ladd(L, L_XA, 1u); ladd(L, L_XF, 1u);
            STAMP(12);
            QPREP();
            asm volatile("s_waitcnt vmcnt(0)" : "+v"(x0), "+v"(x1) :: "memory");
            STAMP(35);
            const unsigned s0 = __builtin_amdgcn_readfirstlane(x0); unsigned s1 = __builtin_amdgcn_readfirstlane(x1);
            _Pragma("unroll 1") for (unsigned spin = 0; s1 == s0 && spin < SPIN_CAP; ++spin) s1 = __builtin_amdgcn_readfirstlane(atomic_read(rsWS, xoff));
            atomic_inc(rsWS, lane < 8 ? WS_KVCNT + lane * 2048 + (b & 15) * 128 : OOB);
            BUMP();
            STAMP(13);
        } else {
            unsigned g0, g1, g2;
            const int gk = min((w - 1) * 192, 256) + lane;
            const uint16_t* gp = gate + gk; PIN(gp);
            const int ga = 2 * (w - 1) + (w - 1 >= tg ? 2 : 0);
            unsigned vxa = vx ^ (xswz(8 * ga) << 4), vxb = (vx ^ (xswz(8 * ga + 8) << 4)) + 8 * ROWB; const unsigned xs = (blk * 64 + 8 * ga) * ROWB; PIN(vxa); PIN(vxb);
            if (w == 1 && lane < 16) *(LAS unsigned*)(L + L_KD + lane * 4) = 0u;
            lds_bar();
            UNROLL for (int c = 0; c < 7; ++c) { dma(rsX, vxa, xs + c * 128, l0 + L_X + c * 8192 + ga * 1024); dma(rsX, vxb, xs + c * 128, l0 + L_X + c * 8192 + ga * 1024 + 1024); }
            asm volatile("global_load_ushort %0, %3, off\n\tglobal_load_ushort %1, %3, off offset:128\n\tglobal_load_ushort %2, %3, off offset:256"
                         : "=&v"(g0), "=&v"(g1), "=&v"(g2) : "v"(gp) : "memory");
            WQD(0, 6);
            QPREP();
            wait_vm<9>();
            lcount(L, L_XA, lane);
            WQD(6, 19);
            STAMP(14);
            asm volatile("s_waitcnt vmcnt(9)" : "+v"(g0), "+v"(g1), "+v"(g2) :: "memory");
            const lp gl = L + (L_WQ + gk * 128 + (112 ^ (((gk >> 1) & 1) << 6)));
            *(LAS uint16_t*)(gl) = (uint16_t)g0; *(LAS uint16_t*)(gl + 8192) = (uint16_t)g1; *(LAS uint16_t*)(gl + 16384) = (uint16_t)g2;
            lcount(L, L_XF, lane);
            STAMP(1);
        }
        lwait(L, L_XF, 4u);
        STAMP(2);
        f32x16 qa = {};
        { bf16x8 wf[28], xq[28];
          UNROLL for (int ks = 0; ks < 28 + QD_; ++ks) {
            if (ks == WQ2) { STAMP(44); wait_vm<0>(); lcount(L, L_XG, lane); STAMP(45); }
            if (ks == 15) { lwait(L, L_XG, 4u); STAMP(46); }
            if (ks < 28) { wf[ks] = ldtr2(a1 + ks * 2048, a1 + ks * 2048 + 512); xq[ks] = ld128(((ks & 3) == 0 ? a00 : (ks & 3) == 1 ? a01 : (ks & 3) == 2 ? a02 : a03) + (ks >> 2) * 8192); }
            if (ks >= QD_) qa = MFMA32(wf[ks - QD_], xq[ks - QD_], qa);
            if (ks == KPOLL && w == 0) asm volatile("buffer_atomic_add %0, %1, %2, 0 offen sc0 sc1" : "+v"(kc) : "v"(kco), "s"(rsWS) : "memory");
            if (ks == KCHK && w == 0) {
                asm volatile("s_waitcnt vmcnt(0)" : "+v"(kc) :: "memory");
                if (__builtin_expect(__builtin_amdgcn_ballot_w64(kc != 16 && lane < 16) != 0, 0)) { _Pragma("unroll 1") for (unsigned spin = 0; spin < SPIN_CAP; ++spin) {
                    BUMP(); if (__builtin_amdgcn_ballot_w64(atomic_read(rsWS, kco) != 16 && lane < 16) == 0) break; } }
                ladd(L, L_KR, 1u); }
            if (ks == 28) ladd(L, L_QD, 1u);
            SBAR(); } }
        const unsigned kv0 = WS_KV + w * 65536;
        const char* kvp = ws + kv0;
#define LD1(A0, off) asm volatile("global_load_dwordx4 %0, %1, %2 offset:%3 sc1" : "=&v"(A0) : "v"(l16), "s"(kvp), "i"(off) : "memory")
#define LD2(A0, A1, off) do { LD1(A0, off); LD1(A1, (off) + 1024); } while (0)
#define WAIT2(N, A, B) asm volatile("s_waitcnt vmcnt(%2)" : "+v"(A), "+v"(B) : "i"(N) : "memory")
        STAMP(47);
        lwait(L, L_KR, 1u);
        LD2(kf[0][0], kf[0][1], 0); LD2(vf[0][0], vf[0][1], 2048);
        STAMP(48);
        lwait(L, L_QD, 4u);
        asm volatile("s_nop 11" : "+v"(qa));
        const f32x2 cs = {0.18898223650461361f * 1.4426950408889634f, 0.f};
        u32x4 q0[4], q1[4];
        UNROLL for (int e = 0; e < 4; ++e) { const f32x2 a = pk_scale(cs, (f32x2){qa[2 * e], qa[2 * e + 1]}), c = pk_scale(cs, (f32x2){qa[8 + 2 * e], qa[9 + 2 * e]});
            q0[3][e] = cvt2(a[0], a[1]); q1[3][e] = cvt2(c[0], c[1]); }
        if (hi) { q1[3][2] = 0u; q1[3][3] = 0u; }
        *(LAS u32x4*)(qs3) = q0[3]; *(LAS u32x4*)(qs3 + 1024) = q1[3];
        if (hd == 1 && hi) *(LAS float*)(L + L_G + qrow * 4) = __builtin_amdgcn_rcpf(1.0f + fexp(-qa[12]));
        STAMP(3);
        lcount(L, L_QF, lane);
        UNROLL for (int n = 1; n < 3; ++n) { kvp += 4096; LD2(kf[n][0], kf[n][1], 0); LD2(vf[n][0], vf[n][1], 2048); }
        STAMP(4);

        f32x16 sA, sB;
        lwait(L, L_QF, 4u);
        STAMP(5);
        q0[0] = *(LAS const u32x4*)(qs0); q1[0] = *(LAS const u32x4*)(qs0 + 1024); q0[1] = *(LAS const u32x4*)(qs1); q1[1] = *(LAS const u32x4*)(qs1 + 1024);
        q0[2] = *(LAS const u32x4*)(qs2); q1[2] = *(LAS const u32x4*)(qs2 + 1024);
        const float gt = *(LAS const float*)(L + L_G + qrow * 4);
        ladd(L, L_SF, 1u);
#define GIVE(O, RP, LP) do { *(LAS u32x4*)(RP) = pack8(O, 0); *(LAS u32x4*)((RP) + 1024) = pack8(O, 8); *(LAS float*)(LP) = O[12]; } while (0)
        WAIT2(10, kf[0][0], kf[0][1]);
        __builtin_amdgcn_s_setprio(0);
        STAMP(6);
        sA = MFMA32(asbf(kf[0][0]), asbf(q0[0]), (f32x16){}); sA = MFMA32(asbf(kf[0][1]), asbf(q1[0]), sA);
#define EXP(A, i) A[i] = __builtin_amdgcn_exp2f(A[i])
#define EX4(A, i) do { EXP(A, i); EXP(A, i + 1); EXP(A, i + 2); EXP(A, i + 3); PIN(A); } while (0)
#define UNIT(A, B, KN0, KN1, qn, q, qp, V0, VP1, NEXT, H0, H1, H2, H3) do { SBAR(); H0; \
        if (NEXT) { B = MFMA32(asbf(KN0), asbf(q0[qn]), (f32x16){}); PIN(B); } SBAR(); \
        EX4(A, 0); SBAR(); \
        o[qp] = MFMA32(asbf(VP1), asbf(pw1), o[qp]); PIN(o[qp]); SBAR(); H1; \
        pw0[0] = cvt2(A[0], A[1]); pw0[1] = cvt2(A[2], A[3]); PIN(pw0); SBAR(); EX4(A, 4); SBAR(); \
        if (NEXT) { B = MFMA32(asbf(KN1), asbf(q1[qn]), B); PIN(B); } SBAR(); H2; SBAR(); \
        pw0[2] = cvt2(A[4], A[5]); pw0[3] = cvt2(A[6], A[7]); PIN(pw0); SBAR(); EX4(A, 8); SBAR(); H3; \
        o[q] = MFMA32(asbf(V0), asbf(pw0), o[q]); PIN(o[q]); SBAR(); \
        { float e12 = __builtin_amdgcn_exp2f(A[12]), e13 = __builtin_amdgcn_exp2f(A[13]), e14 = __builtin_amdgcn_exp2f(A[14]), e15 = __builtin_amdgcn_exp2f(A[15]); \
          asm volatile("" : "+v"(e12), "+v"(e13), "+v"(e14), "+v"(e15)); SBAR(); \
          pw1[0] = cvt2(A[8], A[9]); pw1[1] = cvt2(A[10], A[11]); SBAR(); pw1[2] = cvt2(e12, e13); pw1[3] = cvt2(e14, e15); PIN(pw1); } SBAR(); } while (0)
#define TRIP(c, n, p, LD, NV, NK) do { \
        UNIT(sA, sB, kf[c][0], kf[c][1], 1, 0, 3, vf[c][0], vf[p][1], true, WAIT2(NV, vf[c][0], vf[c][1]), { if (LD) { kvp += 4096; LD1(kf[p][0], 0); } }, { if (LD) LD1(kf[p][1], 1024); }, {}); \
        UNIT(sB, sA, kf[c][0], kf[c][1], 2, 1, 0, vf[c][0], vf[c][1], true, {}, { if (LD) LD1(vf[p][0], 2048); }, { if (LD) LD1(vf[p][1], 3072); }, {}); \
        UNIT(sA, sB, kf[c][0], kf[c][1], 3, 2, 1, vf[c][0], vf[c][1], true, {}, {}, WAIT2(NK, kf[n][0], kf[n][1]), {}); \
        UNIT(sB, sA, kf[n][0], kf[n][1], 0, 3, 2, vf[c][0], vf[c][1], true, {}, {}, {}, {}); } while (0)
        _Pragma("unroll 1") for (int it = 0; it < 3; ++it) {
            TRIP(0, 1, 3, true, 8, 10); TRIP(1, 2, 0, true, 8, 10); TRIP(2, 3, 1, true, 8, 10); TRIP(3, 0, 2, true, 8, 10);
            STAMPV(17 + it);
        }
        TRIP(0, 1, 3, true, 8, 10);
        ladd(L, L_QL, 1u);
        TRIP(1, 2, 0, false, 8, 6); TRIP(2, 3, 1, false, 4, 2);
        ladd(L, L_QE, 1u);
        lwait(L, L_UF, 8u);
        UNIT(sA, sB, kf[3][0], kf[3][1], 1, 0, 3, vf[3][0], vf[2][1], true, WAIT2(0, vf[3][0], vf[3][1]), {}, {}, {});
        UNIT(sB, sA, kf[3][0], kf[3][1], 2, 1, 0, vf[3][0], vf[3][1], true, {}, {}, {}, {});
        UNIT(sA, sB, kf[3][0], kf[3][1], 3, 2, 1, vf[3][0], vf[3][1], true, {}, GIVE(o[0], ro0, lw0), {}, {});
        UNIT(sB, sA, kf[0][0], kf[0][1], 0, 3, 2, vf[3][0], vf[3][1], false, {}, GIVE(o[1], ro1, lw1), {}, {});
        STAMPV(20);
        o[3] = MFMA32(asbf(vf[3][1]), asbf(pw1), o[3]); PIN(o[3]); SBAR();
        GIVE(o[2], ro2, lw2); SBAR();
        STAMP(7);

        float l3 = o[3][12];
        *(LAS float*)(lw3) = l3;
        {   const unsigned u0 = __float_as_uint(o[0][12]), u1 = __float_as_uint(o[1][12]), u2 = __float_as_uint(o[2][12]), u3 = __float_as_uint(l3);
            const unsigned lo = min(min(u0, u1), min(u2, u3)), up = max(max(u0, u1), max(u2, u3));
            const bool bad = __builtin_amdgcn_ballot_w64(hi && (lo < 0x1F800000u || up > 0x5D800000u)) != 0;
            ladd(L, L_GV, bad ? 0x10001u : 1u); }
        unsigned gv = lread(L, L_GV);
        if (__builtin_expect((gv & 0xffffu) < 4u, 0)) { _Pragma("unroll 1") for (unsigned spin = 0; (gv & 0xffffu) < 4u && spin < (1u << 20); ++spin) gv = lread(L, L_GV); }
        asm volatile("" ::: "memory");
        STAMP(15);
        bf16x8 f[6]; f32x4 m0;
        UNROLL for (int t = 0; t < 6; ++t) f[t] = ld128(rop + (t >> 1) * 8192 + (t & 1) * 1024);
        m0 = *(LAS const f32x4*)(lsp);
        SBAR();
        if (__builtin_expect(gv > 0xffffu, 0)) {
            auto qbar = [&](unsigned n) __attribute__((always_inline)) { asm volatile("s_waitcnt lgkmcnt(0)" ::: "memory"); ladd(L, L_FB, 1u); lwait(L, L_FB, n); };
            float mx[4] = {-INFINITY, -INFINITY, -INFINITY, -INFINITY};
            qbar(4u);
            _Pragma("unroll 1") for (int t = 0; t < 16; ++t) {
                const bf16x8 a = asbf(__builtin_amdgcn_raw_buffer_load_b128(rsWS, l16, kv0 + t * 4096, SC1));
                const bf16x8 c = asbf(__builtin_amdgcn_raw_buffer_load_b128(rsWS, l16, kv0 + t * 4096 + 1024, SC1));
                UNROLL for (int q = 0; q < 4; ++q) { f32x16 s = MFMA32(a, asbf(q0[q]), (f32x16){}); s = MFMA32(c, asbf(q1[q]), s); mx[q] = fmaxf(mx[q], rowmax16(s)); SBAR(); }
            }
            *(LAS float*)(lw0) = mx[0]; *(LAS float*)(lw1) = mx[1]; *(LAS float*)(lw2) = mx[2]; *(LAS float*)(lw3) = mx[3];
            qbar(8u);
            UNROLL for (int q = 0; q < 4; ++q) { const f32x4 n0 = *(LAS const f32x4*)(L + (lsb ^ (((q + 1) & 3) * 512)));
                set_shift(q1[q], fmaxf(fmaxf(n0[0], n0[1]), fmaxf(n0[2], n0[3])), hi); }
            qbar(12u);
            UNROLL for (int q = 0; q < 4; ++q) {
                f32x16 acc = {};
                _Pragma("unroll 1") for (int t = 0; t < 16; ++t) {
                    const bf16x8 a = asbf(__builtin_amdgcn_raw_buffer_load_b128(rsWS, l16, kv0 + t * 4096, SC1));
                    const bf16x8 c = asbf(__builtin_amdgcn_raw_buffer_load_b128(rsWS, l16, kv0 + t * 4096 + 1024, SC1));
                    const bf16x8 d = asbf(__builtin_amdgcn_raw_buffer_load_b128(rsWS, l16, kv0 + t * 4096 + 2048, SC1));
                    const bf16x8 e = asbf(__builtin_amdgcn_raw_buffer_load_b128(rsWS, l16, kv0 + t * 4096 + 3072, SC1));
                    f32x16 s = MFMA32(a, asbf(q0[q]), (f32x16){}); s = MFMA32(c, asbf(q1[q]), s);
                    UNROLL for (int r = 0; r < 16; ++r) s[r] = __builtin_amdgcn_exp2f(s[r]);
                    acc = MFMA32(d, asbf(pack8(s, 0)), acc); acc = MFMA32(e, asbf(pack8(s, 8)), acc);
                }
                if (q == 0) { GIVE(acc, ro0, lw0); } else if (q == 1) { GIVE(acc, ro1, lw1); } else if (q == 2) { GIVE(acc, ro2, lw2); } else { *(LAS float*)(lw3) = acc[12]; o[3] = acc; }
            }
            qbar(16u);
            UNROLL for (int t = 0; t < 6; ++t) f[t] = ld128(rop + (t >> 1) * 8192 + (t & 1) * 1024);
            m0 = *(LAS const f32x4*)(lsp);
        }
        __builtin_amdgcn_s_setprio(2);
        f32x16 t = o[3];
        UNROLL for (int k = 0; k < 6; ++k) t = MFMA32(asbf((k & 1) ? sel1 : sel0), f[k], t);
        {
            float s0 = m0[0] + m0[1], s2 = m0[2] + m0[3]; PIN(s0); PIN(s2);
            f32x2 sc; sc[0] = sc[1] = gt * __builtin_amdgcn_rcpf(s0 + s2); PIN(sc);
            u32x4 oa, ob;
            UNROLL for (int e = 0; e < 4; ++e) { const f32x2 a = sc * (f32x2){t[2 * e], t[2 * e + 1]}; oa[e] = nosent(cvt2(a[0], a[1])); }
            st128(oa, rsWS, l16, opiece, SC1);
            SBAR();
            UNROLL for (int e = 0; e < 4; ++e) { const f32x2 a = sc * (f32x2){t[8 + 2 * e], t[9 + 2 * e]}; ob[e] = nosent(cvt2(a[0], a[1])); }
            if (hi) { ob[2] = 0u; ob[3] = 0u; }
            st128(ob, rsWS, l16, opiece + 1024, SC1);
        }
        __builtin_amdgcn_s_setprio(0);
        STAMP(8);
        const int tok = blk * 64 + qrow, col = 56 * j + 32 * hd + 4 * hi;
        const rsrc_t rsD = mkrs(dskip, ROWB), rsO = mkrs(out, SEQ * DM * 4);
        if (ODRAIN) wait_vm<0>();
        ladd(L, L_OD, 1u);
        f32x4 r[4]; u32x2 xv[4], dv[4];
        UNROLL for (int e = 0; e < 4; ++e) { xv[e] = __builtin_amdgcn_raw_buffer_load_b64(rsX, tok * ROWB + col * 2 + e * 16, 0, 0); dv[e] = __builtin_amdgcn_raw_buffer_load_b64(rsD, col * 2 + e * 16, 0, 0); }
        SBAR();
        UNROLL for (int e = 0; e < 4; ++e) {
            float t0 = bf2f(xv[e][0]) * bf2f(dv[e][0]), t1 = bfhi(xv[e][0]) * bfhi(dv[e][0]), t2 = bf2f(xv[e][1]) * bf2f(dv[e][1]), t3 = bfhi(xv[e][1]) * bfhi(dv[e][1]);
            PIN(t0); PIN(t1); PIN(t2); PIN(t3); t0 *= 1.0f - gt; t1 *= 1.0f - gt; t2 *= 1.0f - gt; t3 *= 1.0f - gt; PIN(t0); PIN(t1); PIN(t2); PIN(t3);
            r[e] = (f32x4){t0, t1, t2, t3}; }
        const lp rp = lbase(L, L_RED + hd * 32768 + th * 16384 + l16);
        unsigned oo = (tok * DM + col) * 4, o3 = hd ? OOB : oo + 96; PIN(oo); PIN(o3);
        lds_bar();
        STAMP(10);
        UNROLL for (int e = 0; e < 4; ++e) {
            r[e] += (*(LAS const f32x4*)(rp + e * 1024) + *(LAS const f32x4*)(rp + 4096 + e * 1024)) + (*(LAS const f32x4*)(rp + 8192 + e * 1024) + *(LAS const f32x4*)(rp + 12288 + e * 1024));
            st128(__builtin_bit_cast(u32x4, r[e]), rsO, e < 3 ? oo + e * 32 : o3, 0, 0); }
    } else {
        const int u = w - 4, st = min(16 * u + r16, 55);
        const unsigned eoff = (G == 0 && 16 * u + r16 < 56) ? st * 8 : OOB;
        const rsrc_t rsBR = mkrs(bre, DM * ROWB), rsBI = mkrs(bim, DM * ROWB);
        const unsigned vb0_ = (56 * j + 16 * u + r8) * ROWB + ((sl ^ (r8 >> 1)) << 4), vb1_ = u < 3 ? (vb0_ ^ 64) + 8 * ROWB : OOB;
#define DST2(a) (u < 3 ? (a) : l0 + L_DUMMY)
        unsigned p0, p1, p2;
        const uint16_t *a0 = lre + 56 * j + st, *a1 = lim + 56 * j + st, *a2 = logdt + 56 * j + st; PIN(a0); PIN(a1); PIN(a2);
#define TOUCH(ptr, bytes, vo) do { const rsrc_t r_ = mkrs(ptr, bytes); \
            asm volatile("s_mov_b32 m0, %0\n\ts_nop 0\n\tbuffer_load_dword %1, %2, 0 offen lds" :: "s"(l0 + L_DUMMY), "v"(vo), "s"(r_) : "memory"); } while (0)
        if (PFE == 1) { if (u < 3) TOUCH(u == 0 ? wq : u == 1 ? bre : bim, DM * ROWB, lane < 25 ? (b * 25 + lane) * 64 : OOB); else TOUCH(x, SEQ * ROWB, lane < 56 ? (blk * 448 + j * 56 + lane) * 128 : OOB); }
        const bool master = b == 0 && w == 7;
        if (master) {
            __builtin_amdgcn_raw_buffer_store_b32(0u, rsWS, lane * 128, 0, SC1);
            __builtin_amdgcn_raw_buffer_store_b32(0u, rsWS, lane * 128 + 8192, 0, SC1);
            __builtin_amdgcn_raw_buffer_store_b32(0u, rsWS, lane < 32 ? lane * 128 + 16384 : OOB, 0, SC1);
        }
        asm volatile("s_barrier" ::: "memory");
        if (master) { wait_vm<0>(); BUMP(); }
        if (PFE == 2 && u < 3) {
            const int r = b >> 3;
            TOUCH(u == 0 ? wq : u == 1 ? bre : bim, DM * ROWB, lane < 49 ? (r * 98 + lane) * 128 : OOB);
            TOUCH(u == 0 ? wq : u == 1 ? bre : bim, DM * ROWB, lane < 49 ? (r * 98 + 49 + lane) * 128 : OOB);
        }
        if (PFE == 0) { if (u < 3) TOUCH(u == 0 ? wq : u == 1 ? bre : bim, DM * ROWB, lane < 25 ? (b * 25 + lane) * 64 : OOB); }
        asm volatile("global_load_ushort %0, %3, off\n\tglobal_load_ushort %1, %4, off\n\tglobal_load_ushort %2, %5, off"
                     : "=&v"(p0), "=&v"(p1), "=&v"(p2) : "v"(a0), "v"(a1), "v"(a2) : "memory");
#define BOFF(c) ((c) % 3 == 0 ? L_B0 : L_WK + ((c) % 3 - 1) * 14336)
#define BD(c) do { dma(rsBR, vb0_, (c) * 128, l0 + BOFF(c) + u * 2048); dma(rsBI, vb0_, (c) * 128, l0 + BOFF(c) + 7168 + u * 2048); \
                   dma(rsBR, vb1_, (c) * 128, DST2(l0 + BOFF(c) + u * 2048 + 1024)); dma(rsBI, vb1_, (c) * 128, DST2(l0 + BOFF(c) + 7168 + u * 2048 + 1024)); } while (0)
        STAMP(14);
        asm volatile("s_waitcnt vmcnt(0)" : "+v"(p0), "+v"(p1), "+v"(p2) :: "memory");
        if (master) BUMP();
        f32x2 lam, coef; cf lam8, lam16, lam64, wlo, whi, zG;
        {   const float lr = bf2f(p0), li = bf2f(p1), dt = bfround(fexp(bf2f(p2)));
            const float er = fexp(dt * lr), rev = __builtin_amdgcn_fractf(dt * li * 0.15915494309189535f);
            const cf l1 = {er * __builtin_amdgcn_cosf(rev), er * __builtin_amdgcn_sinf(rev)};
            const float inv = __builtin_amdgcn_rcpf(lr * lr + li * li);
            const cf cq = cmul((cf){l1.r - 1.0f, l1.i}, (cf){lr * inv, -li * inv});
            lam = (f32x2){l1.r, l1.i}; coef = (f32x2){cq.r, cq.i};
            const cf l2 = cmul(l1, l1), l4 = cmul(l2, l2); lam8 = cmul(l4, l4); lam16 = cmul(lam8, lam8);
            const cf l32 = cmul(lam16, lam16); lam64 = cmul(l32, l32);
            const cf l128 = cmul(lam64, lam64), l256 = cmul(l128, l128), l512 = cmul(l256, l256), l1024 = cmul(l512, l512), one = {1.f, 0.f};
            wlo = cmul((G & 1) ? lam16 : one, (G & 2) ? l32 : one); whi = cmul(wlo, lam8);
            zG = cmul((G & 1) ? l512 : one, (G & 2) ? l1024 : one);
            asm volatile("" : "+v"(lam), "+v"(coef), "+v"(lam8.r), "+v"(lam8.i), "+v"(lam16.r), "+v"(lam16.i), "+v"(lam64.r), "+v"(lam64.i),
                              "+v"(wlo.r), "+v"(wlo.i), "+v"(whi.r), "+v"(whi.i), "+v"(zG.r), "+v"(zG.i) :: "memory"); }
        const int rr = u < 3 ? r16 : r16 & 7, rg = (r32 & 3) + 4 * (r32 >> 3), tk = 32 * ((r32 >> 2) & 1) + (rg >> 1) + 8 * (rg & 1);
        const unsigned bro = (G & 1) * 7168 + u * 2048 + rr * 128 + ((hi ^ (rr >> 1)) << 4), ato = L_X + tk * 128 + ((hi ^ xswz(tk)) << 4);
        lp br[2][4], at[4];
        UNROLL for (int k = 0; k < 4; ++k) { br[0][k] = lbase(L, L_B0 + (bro ^ (k << 5))); br[1][k] = lbase(L, L_WK + (bro ^ (k << 5))); at[k] = lbase(L, ato ^ (k << 5)); }
        if (master) { _Pragma("unroll 1") for (unsigned spin = 0; lread(L, L_KD) < 1u && spin < SPIN_CAP; ++spin) { BUMP(); __builtin_amdgcn_s_sleep(8); } BUMP(); wait_vm<0>(); }
        if (B0EARLY) BD(0);
        lwait(L, L_KD, 1u);
        STAMP(1);
        if (!B0EARLY) BD(0);
        BD(1); BD(2);
        f32x16 ur = {}, ui = {};
        bf16x8 fb[2][4], fx[2][8];
#define BP(c, k) ((c) % 3 == 0 ? br[0][k] : br[1][k] + ((c) % 3 - 1) * 14336)
#define BRD(c) do { wait_vm<(c) < 5 ? 8 : 4 * (6 - (c))>(); UNROLL for (int k = 0; k < 4; ++k) fb[(c) & 1][k] = ld128(BP(c, k)); \
                    if ((c) < 4) { asm volatile("s_waitcnt lgkmcnt(0)" ::: "memory"); BD((c) + 3); } } while (0)
#define XRD(c, i) fx[(c) & 1][i] = ld128(at[(i) >> 1] + (c) * 8192 + ((i) & 1) * 2048)
#define UCH(c) UNROLL for (int i = 0; i < 8; ++i) { \
            if (i & 1) ui = MFMA32(fx[(c) & 1][i], fb[(c) & 1][i >> 1], ui); else ur = MFMA32(fx[(c) & 1][i], fb[(c) & 1][i >> 1], ur); \
            SBAR(); \
            if (i == BRI && (c) < 6) BRD((c) < 6 ? (c) + 1 : 6); \
            if ((c) < 6) XRD((c) + 1, i); \
            SBAR(); }
        BRD(0);
        lwait(L, L_XA, 4u);
        STAMP(2);
        UNROLL for (int i = 0; i < 8; ++i) XRD(0, i);
        SBAR();
        UCH(0) UCH(1) UCH(2)
        __builtin_amdgcn_s_setprio(UPRIO);
        UCH(3) UCH(4) UCH(5) UCH(6)
        if (TCW == 0) {
        if (u < 3) TOUCH(u == 0 ? wo : u == 1 ? cre : cim, DM * ROWB, lane < 25 ? (b * 25 + lane) * 64 : OOB);
        }
        lcount(L, L_UF, lane);
        UNROLL for (int r = 0; r < 16; ++r) {
            auto sw = __builtin_amdgcn_permlane16_swap(__float_as_uint(ur[r]), __float_as_uint(ui[r]), false, false); ur[r] = __uint_as_float(sw[0]); ui[r] = __uint_as_float(sw[1]); }
        STAMP(3);
        f32x2 hr, hm;
        UNROLL for (int p = 0; p < 8; ++p) {
            f32x2 tr, ti; pk_cmul(tr, ti, coef, PAIR(ur, p), PAIR(ui, p));
            ur[2 * p] = tr[0]; ur[2 * p + 1] = tr[1]; ui[2 * p] = ti[0]; ui[2 * p + 1] = ti[1];
            if (p == 0) { hr = tr; hm = ti; } else { f32x2 nr, ni; pk_cfma(nr, ni, lam, hr, hm, tr, ti); hr = nr; hm = ni; }
        }
        const cf elo = {hr[0], hm[0]}, e16 = cfma(lam8, elo, (cf){hr[1], hm[1]});
        cf e0, e1, e2, e3; bcast4(e16.r, e0.r, e1.r, e2.r, e3.r); bcast4(e16.i, e0.i, e1.i, e2.i, e3.i);
        const cf t2 = cfma(lam16, e0, e1), t3 = cfma(lam16, t2, e2), eb = cfma(lam16, t3, e3);
        lwait(L, L_KR, 1u); lwait(L, L_QF, 4u);
        __builtin_amdgcn_raw_buffer_store_b64((u32x2){nosent(__float_as_uint(eb.r)), nosent(__float_as_uint(eb.i))}, rsWS, eoff, WS_E + (blk * DM + 56 * j) * 8, SC1);
        if (TCW == 1) {
        if (u < 3) TOUCH(u == 0 ? wo : u == 1 ? cre : cim, DM * ROWB, lane < 25 ? (b * 25 + lane) * 64 : OOB);
        }
        const cf pg = {G == 0 ? 0.f : G == 1 ? e0.r : G == 2 ? t2.r : t3.r, G == 0 ? 0.f : G == 1 ? e0.i : G == 2 ? t2.i : t3.i}, ph = cfma(lam8, pg, elo);
        STAMP(4);
        asm volatile("s_mov_b32 m0, %0\n\ts_nop 0\n\tbuffer_load_dword %1, %2, %3 offen sc1 lds"
                     :: "s"(l0 + L_DUMMY), "v"(u == 0 ? lane * 128 : OOB), "s"(rsWS), "s"(WS_KV + (b >> 3) * 8192) : "memory");
        st128((u32x4){~0u, ~0u, ~0u, ~0u}, rsWS, l16, opiece, SC1);
        st128((u32x4){~0u, ~0u, ~0u, ~0u}, rsWS, l16, opiece + 1024, SC1);
        const float gt = *(LAS const float*)(L + L_G + (32 * th + r32) * 4), go = *(LAS const float*)(L + L_G + (32 * (th ^ 1) + r32) * 4);
        lwait(L, L_UF, 4u);
        const rsrc_t rsCR = mkrs(cre, DM * ROWB), rsCI = mkrs(cim, DM * ROWB);
        const unsigned vc = (56 * j + 8 * u + r8) * ROWB + ((sl ^ ((4 * u + (r8 >> 1)) & 7)) << 4), vc4 = (56 * j + 32 + r8) * ROWB + ((sl ^ (r8 >> 1)) << 4);
        const rsrc_t rsCX = mkrs((u & 1) ? cim : cre, DM * ROWB);
#define CD(n) do { if ((n) < 14) { if ((n) & 1) dma(rsCI, vc, ((n) >> 1) * 128, l0 + L_CIM + ((n) >> 1) * 7168 + u * 1024); else dma(rsCR, vc, ((n) >> 1) * 128, l0 + L_C + ((n) >> 1) * 7168 + u * 1024); } \
            else { const int p_ = min(2 * ((n) - 14) + (u >> 1), 20), cc_ = (p_ * 43) >> 7, g_ = p_ - 3 * cc_;     \
                   dma(rsCX, vc4 ^ ((g_ & 1) << 6), g_ * 8 * ROWB + cc_ * 128, l0 + (u & 1) * L_CIM + cc_ * 7168 + (4 + g_) * 1024); } } while (0)
        const rsrc_t rsE = mkrs(ws + WS_E + 56 * j * 8, 32 * DM * 8 - 56 * j * 8);
        const int ebase = (blk - 1 - 8 * G) * (DM * 8) + st * 8;
        const unsigned hoff = lane == 0 ? WS_HCNT + blk * 128 : OOB;
        UNROLL for (int n = 0; n < ELDN; ++n) { CD(n); PACE(); }
        STAMP(38);
        {
            u32x2 ev[8];
            UNROLL for (int m = 0; m < 8; m += 2)
                asm volatile("buffer_load_dwordx2 %0, %2, %4, 0 offen sc1\n\tbuffer_load_dwordx2 %1, %3, %4, 0 offen sc1"
                             : "=&v"(ev[m]), "=&v"(ev[m + 1]) : "v"(ebase - m * (DM * 8)), "v"(ebase - (m + 1) * (DM * 8)), "s"(rsE) : "memory");
            UNROLL for (int n = ELDN; n < ELDN + 5; ++n) { PACE(); CD(n); }
            asm volatile("s_waitcnt vmcnt(5)" : "+v"(ev[0]), "+v"(ev[1]), "+v"(ev[2]), "+v"(ev[3]), "+v"(ev[4]), "+v"(ev[5]), "+v"(ev[6]), "+v"(ev[7]) :: "memory");
            _Pragma("unroll 1") for (unsigned spin = 0; spin < SPIN_CAP; ++spin) {
                unsigned mx = 0;
                UNROLL for (int m = 0; m < 8; ++m) mx = max(mx, max(ev[m][0], ev[m][1]));
                if (__builtin_expect(__builtin_amdgcn_ballot_w64(mx == ~0u) == 0, 1)) break;
                __builtin_amdgcn_s_sleep(8);
                asm volatile("" ::: "memory");
                UNROLL for (int m = 0; m < 8; ++m) ev[m] = __builtin_amdgcn_raw_buffer_load_b64(rsE, ebase - m * (DM * 8), 0, SC1);
            }
            STAMP(39);
            cf part = {__uint_as_float(ev[7][0]), __uint_as_float(ev[7][1])};
            UNROLL for (int m = 6; m >= 0; --m) part = cfma(lam64, part, (cf){__uint_as_float(ev[m][0]), __uint_as_float(ev[m][1])});
            part = cmul(zG, part);
            if (ebase < 0) part = (cf){0.f, 0.f};
            PIN(part.r); SBAR(); CD(ELDN + 5); SBAR();
            part.r = sum4(part.r); part.i = sum4(part.i);
            const cf ilo = cfma(wlo, part, pg), ihi = cfma(whi, part, ph);
            hr = (f32x2){ilo.r, ihi.r}; hm = (f32x2){ilo.i, ihi.i};
            u32x4 pr0, pr1, pi0, pi1;
            UNROLL for (int p = 0; p < 8; ++p) {
                f32x2 nr, ni; pk_cfma(nr, ni, lam, hr, hm, PAIR(ur, p), PAIR(ui, p)); hr = nr; hm = ni;
                unsigned cr, ci; asm("v_cvt_pk_bf16_f32 %0, %2, %3\n\tv_cvt_pk_bf16_f32 %1, -%4, -%5" : "=&v"(cr), "=&v"(ci) : "v"(hr[0]), "v"(hr[1]), "v"(hm[0]), "v"(hm[1]));
                if (p < 4) { pr0[p & 3] = cr; pi0[p & 3] = ci; } else { pr1[p & 3] = cr; pi1[p & 3] = ci; }
                if (p == 1 || p == 5) { PIN(hr); SBAR(); CD(ELDN + 6 + (p >> 2)); SBAR(); }
            }
            const unsigned sb = L_HS + u * 4096, wa = sb + r16 * 128 + (((2 * G) ^ (r16 & 7)) << 4);
            const int tok = (lane & 48) + (r16 >> 1) + 8 * (lane & 1);
            const unsigned gha = (tok >> 5) * 57344 + (tok & 31) * 16, ghb = u < 3 ? gha + 1024 : OOB, gs = WS_H + blk * 114688 + (7 * j + 2 * u) * 1024;
            const unsigned rao = sb + tq * 128 + (((2 * G + (tp >> 1)) ^ tq) << 4) + 8 * (tp & 1);
            const lp wa0 = lbase(L, wa), wa1 = lbase(L, wa ^ 16), ra = lbase(L, rao), rb = lbase(L, (rao ^ 64) + 512);
            *(LAS u32x4*)(wa0) = pr0; *(LAS u32x4*)(wa1) = pr1; *(LAS u32x4*)(wa0 + 2048) = pi0; *(LAS u32x4*)(wa1 + 2048) = pi1;
            asm volatile("" ::: "memory");
            UNROLL for (int pl = 0; pl < 2; ++pl) {
                const bf16x8 c0 = ldtr2(ra + pl * 2048, rb + pl * 2048), c1 = ldtr2(ra + pl * 2048 + 1024, rb + pl * 2048 + 1024);
                st128(__builtin_bit_cast(u32x4, c0), rsWS, gha + pl * 512, gs, SC1);
                st128(__builtin_bit_cast(u32x4, c1), rsWS, ghb + pl * 512, gs, SC1);
            }
        }
        STAMP(5);
        lcount(L, L_SF, lane);
        UNROLL for (int n = ELDN + 8; n < 25; ++n) { PACE(); CD(n); }
        wait_vm<25 - ELDN - 8>();
        atomic_inc(rsWS, hoff);
        lwait(L, L_SF, 8u);
        unsigned hc = 0u;
        { const rsrc_t rsWO = mkrs(wo, DM * ROWB);
          UNROLL for (int n = 0; n < 14; ++n) { dma(rsWO, vw, (u * 14 + n) * 8 * ROWB, l0 + L_WO + (u * 14 + n) * 1024);
              if (n == 9) asm volatile("buffer_atomic_add %0, %1, %2, 0 offen sc0 sc1" : "+v"(hc) : "v"(hoff), "s"(rsWS) : "memory");
              PACE(); } }
        STAMP(40);
        asm volatile("s_waitcnt vmcnt(4)" : "+v"(hc) :: "memory");
        lcount(L, L_WF, lane);
        if (__builtin_expect(__builtin_amdgcn_readfirstlane(hc) != 32, 0)) {
            _Pragma("unroll 1") for (unsigned spin = 0; __builtin_amdgcn_readfirstlane(atomic_read(rsWS, hoff)) != 32 && spin < SPIN_CAP; ++spin) __builtin_amdgcn_s_sleep(8); }
        const unsigned hb = WS_H + blk * 114688 + u * 1024;
        u32x4 h0[14], h1[14];
        UNROLL for (int m = 0; m < 3; ++m) { h0[m] = __builtin_amdgcn_raw_buffer_load_b128(rsWS, l16, hb + th * 57344 + m * 4096, SC1); PACE(); }
        const unsigned cx = (32 * (G & 1) + 8 * tp) ^ (((tq >> 1) & 1) << 6), nb = L_WO + (112 * u + tq) * 128, wa_ = nb + hi * 512;
        const lp wa0_ = lbase(L, wa_ + cx), wa1_ = lbase(L, wa_ + (cx ^ 64)), nb0 = lbase(L, nb + cx), nb1 = lbase(L, nb + (cx ^ 64));
        wait_vm<3>();
        bf16x8 wf0[8], wf1[8];
        UNROLL for (int k = 0; k < 8; ++k) { const unsigned ro = (28 * (k >> 1) + 16 * (k & 1)) * 128;
            wf0[k] = ldtr2(wa0_ + ro, ((k & 1) ? nb0 : wa0_) + ro + 1024); wf1[k] = ldtr2(wa1_ + ro, ((k & 1) ? nb1 : wa1_) + ro + 1024); }
        lcount(L, L_UF, lane);
        STAMP(42);
        UNROLL for (int m = 3; m < HP; ++m) { h0[m] = __builtin_amdgcn_raw_buffer_load_b128(rsWS, l16, hb + th * 57344 + m * 4096, SC1); PACE(); }
        lwait_idle(L, L_QL, 4u);
        UNROLL for (int m = HP; m < 14; ++m) h0[m] = __builtin_amdgcn_raw_buffer_load_b128(rsWS, l16, hb + th * 57344 + m * 4096, SC1);
        UNROLL for (int m = 0; m < 14; ++m) h1[m] = __builtin_amdgcn_raw_buffer_load_b128(rsWS, l16, hb + (th ^ 1) * 57344 + m * 4096, SC1);
        const int rbc = min(32 + r32, 55);
        const unsigned cao = hi * L_CIM + r32 * 128 + ((u ^ ((r32 >> 1) & 7)) << 4), cbo = hi * L_CIM + rbc * 128 + ((u ^ ((rbc >> 1) & 7)) << 4);
        const lp ca0 = lbase(L, cao), ca1 = lbase(L, cao ^ 64), cb0 = lbase(L, cbo), cb1 = lbase(L, cbo ^ 64);
        const lp rp = lbase(L, L_RED + th * 16384 + u * 4096 + l16), rq = lbase(L, L_RED + (th ^ 1) * 16384 + u * 4096 + l16);
        const unsigned oa = WS_O + blk * 65536 + (th * 16 + 4 * u) * 2048, ob = WS_O + blk * 65536 + ((th ^ 1) * 16 + 4 * u) * 2048;
        f32x2 s2 = {2.0f * (1.0f - gt), 0.f}, s3 = {2.0f * (1.0f - go), 0.f}; PIN(s2); PIN(s3);
        lwait(L, L_WF, 4u);
        STAMP(41);
        __builtin_amdgcn_s_setprio(YPRIO);
        lwait_idle(L, L_QE, 4u);
        __builtin_amdgcn_s_sleep(YSLEEP);
        STAMP(15);
        f32x16 y[4]; y[0] = y[1] = y[2] = y[3] = (f32x16){};
        u32x4 of[16];
#define OLD(k0) UNROLL for (int k = (k0); k < (k0) + 8; ++k) of[k] = __builtin_amdgcn_raw_buffer_load_b128(rsWS, l16 + (k & 3) * 1024, (k < 8 ? oa : ob) + ((k >> 2) & 1) * 4096, SC1)
        {   bf16x8 fa[28], fc[28];
            UNROLL for (int m = -YD; m < 28; ++m) {
                if (m + YD < 28) { const int n = (m + YD) % 14; fa[m + YD] = ld128(((n & 1) ? ca1 : ca0) + (n >> 1) * 7168); fc[m + YD] = ld128(((n & 1) ? cb1 : cb0) + (n >> 1) * 7168); }
                if (m >= 0 && m < 14) { y[0] = MFMA32(fa[m], asbf(h0[m]), y[0]); y[1] = MFMA32(fc[m], asbf(h0[m]), y[1]); }
                if (m >= 14) { y[2] = MFMA32(fa[m], asbf(h1[m - 14]), y[2]); y[3] = MFMA32(fc[m], asbf(h1[m - 14]), y[3]); }
                if (m >= 16 && m < 24) { UNROLL for (int k = 0; k < 2; ++k) { const f32x2 a = pk_scale(s2, (f32x2){y[k][2 * (m - 16)], y[k][2 * (m - 16) + 1]}); y[k][2 * (m - 16)] = a[0]; y[k][2 * (m - 16) + 1] = a[1]; } PIN(y[0]); PIN(y[1]); }
                if (OM < 28 && m == OM) { lwait(L, L_OD, ODN); if (OSLEEP) __builtin_amdgcn_s_sleep(OSLEEP); STAMP(43); OLD(0); }
                if (OM < 28 && m == (OM + 2 < 27 ? OM + 2 : 27)) { OLD(8); }
                if (m == 26) ladd(L, L_YF, 1u);
                SBAR();
            } }
        STAMP(9);
        lwait(L, L_YF, 4u);
        asm volatile("s_nop 11" : "+v"(y[2]), "+v"(y[3]));
        UNROLL for (int k = 2; k < 4; ++k) UNROLL for (int e = 0; e < 8; ++e) { const f32x2 a = pk_scale(s3, (f32x2){y[k][2 * e], y[k][2 * e + 1]}); y[k][2 * e] = a[0]; y[k][2 * e + 1] = a[1]; }
        asm volatile("s_nop 1" : "+v"(y[2]), "+v"(y[3]));
        if (OM >= 28) { lwait(L, L_OD, ODN); if (OSLEEP) __builtin_amdgcn_s_sleep(OSLEEP); STAMP(43); OLD(0); OLD(8); }
#define OMAX(mx, k) mx = max(max(mx, max(of[k][0], of[k][1])), max(of[k][2], of[k][3]))
#define OFIX(mx, k0) do { if (__builtin_expect(__builtin_amdgcn_ballot_w64(mx == ~0u) != 0, 0)) { _Pragma("unroll 1") for (unsigned spin = 0; spin < SPIN_CAP; ++spin) { \
            asm volatile("" ::: "memory");                              \
            OLD(k0); mx = 0; UNROLL for (int k = (k0); k < (k0) + 8; ++k) OMAX(mx, k); \
            if (__builtin_amdgcn_ballot_w64(mx == ~0u) == 0) break; } } } while (0)
        SBAR();
        unsigned mxa = 0, mxb = 0;
        UNROLL for (int k = 0; k < 8; ++k) OMAX(mxa, k);
        OFIX(mxa, 0);
        STAMP(10);
#define REDW(p, Y, e) *(LAS f32x4*)((p) + (e) * 1024) = (f32x4){Y[4 * (e)], Y[4 * (e) + 1], Y[4 * (e) + 2], Y[4 * (e) + 3]}
        UNROLL for (int k = 0; k < 8; ++k) { y[0] = MFMA32(wf0[k], asbf(of[k]), y[0]); SBAR(); OMAX(mxb, 8 + k); PIN(mxb); SBAR(); }
        UNROLL for (int k = 0; k < 8; ++k) { y[1] = MFMA32(wf1[k], asbf(of[k]), y[1]); SBAR(); if (k >= 3 && k < 7) REDW(rp, y[0], k - 3); SBAR(); }
        OFIX(mxb, 8);
        UNROLL for (int k = 0; k < 8; ++k) { y[2] = MFMA32(wf0[k], asbf(of[8 + k]), y[2]); SBAR(); if (k >= 3 && k < 7) REDW(rp + 32768, y[1], k - 3); SBAR(); }
        UNROLL for (int k = 0; k < 8; ++k) { y[3] = MFMA32(wf1[k], asbf(of[8 + k]), y[3]); SBAR(); if (k >= 3 && k < 7) REDW(rq, y[2], k - 3); SBAR(); }
        UNROLL for (int e = 0; e < 4; ++e) REDW(rq + 32768, y[3], e);
        lds_bar();
    }
    STAMP(11);
}

extern "C" void kernel_launch(void* const* d_in, const int* in_sizes, int n_in, void* d_out, int out_size, void* d_ws, size_t ws_size,
                              hipStream_t stream) {
    const uint16_t* const* in = (const uint16_t* const*)d_in;
    gated_mix_kernel<<<256, 512, 0, stream>>>(in[0], in[1], in[2], in[3], in[4], in[6], in[7], in[8], in[9], in[10], in[11], in[12], in[13], in[14],
                                              (float*)d_out, (char*)d_ws);
}
```

```cpp
#include <hip/hip_runtime.h>
#include <hip/hip_bf16.h>
#include <stdint.h>

typedef __bf16 bf16x8 __attribute__((ext_vector_type(8)));
typedef __bf16 bf16x2 __attribute__((ext_vector_type(2)));
typedef float f32x16 __attribute__((ext_vector_type(16)));
typedef float f32x4 __attribute__((ext_vector_type(4)));
typedef float f32x2 __attribute__((ext_vector_type(2)));
typedef unsigned u32x4 __attribute__((ext_vector_type(4)));
typedef unsigned u32x2 __attribute__((ext_vector_type(2)));
typedef short s16x4 __attribute__((ext_vector_type(4)));
typedef __amdgpu_buffer_rsrc_t rsrc_t;
#define LAS __attribute__((address_space(3)))
#define DI __device__ __forceinline__
#define SBAR() __builtin_amdgcn_sched_barrier(0)
#define PIN(x) asm volatile("" : "+v"(x))
#define PINS(x) asm volatile("" : "+s"(x))
#define MFMA32(a, b, c) __builtin_amdgcn_mfma_f32_32x32x16_bf16((a), (b), (c), 0, 0, 0)
#define MFMA16(a, b, c) __builtin_amdgcn_mfma_f32_16x16x32_bf16((a), (b), (c), 0, 0, 0)
#define UNROLL _Pragma("unroll")
#define PACE() asm volatile("s_sleep %0" :: "i"(CSLEEP) : "memory")
#define SC1 16

#define CSLEEP 3
#define YSLEEP 1
#define CPREF 32768
#define WQ2 8
#define YD 3
#define OSLEEP 6
#define PACKED 1
#define ODN 4u
#define PFC 0
#define B0EARLY 0
#define KCHK 29
#define PFE 0
#define ODRAIN 0
#define ELDN 12
#define QD_ 7
#define OM 28
#define BRI 7
#define TCW 0
#define LSLEEP 0
#define REGB 1
#define HP 9
#define YPRIO 0
#define AMF 1
#define DRAINST 1
#define OAUX 16
#define QREP 2
#define KPOLL 16
#define UPRIO 1
#define SPRIO 0
#define OM0 16
#define OM1 22
#define MAP42 0

constexpr int SEQ = 2048, DM = 448, ROWB = DM * 2;
constexpr unsigned WS_KVCNT = 0;
constexpr unsigned WS_HCNT = 16384;
constexpr unsigned WS_X = 20480;
constexpr unsigned OOB = 0x7FFFFFF0u;
constexpr unsigned WS_KV = 65536;
constexpr unsigned WS_E = WS_KV + 262144;
constexpr unsigned WS_H = WS_E + 131072;
constexpr unsigned WS_O = WS_H + 32 * 114688;
constexpr unsigned WS_END = WS_O + 32 * 65536;
constexpr unsigned WS_STAMP = WS_END;

constexpr unsigned L_X = 0, L_WK = 57344, L_WQ = 86016, L_B0 = 143360, L_G = 160768;
constexpr unsigned L_KD = 161024, L_XA = 161028, L_XF = 161060, L_QD = 161032, L_QF = 161036, L_UF = 161040, L_WF = 161044, L_KR = 161048, L_YF = 161052, L_XG = 161064, L_GV = 161068, L_QE = 161072, L_SF = 161076, L_QL = 161080, L_OD = 161056, L_FB = 161084;
constexpr unsigned L_QSH = 122880, L_HS = 106496;
constexpr unsigned L_C = 0, L_CIM = 50176, L_WO = 100352;
constexpr unsigned L_RO = 131072, L_LS = 155648;
constexpr unsigned L_RED = 0, L_DUMMY = 157696;
constexpr unsigned LDS_BYTES = 163840;
constexpr unsigned SPIN_CAP = 1u << 14;

DI unsigned cvt2(float a, float b) { f32x2 v = {a, b}; return __builtin_bit_cast(unsigned, __builtin_convertvector(v, bf16x2)); }
DI float bf2f(unsigned h) { return __uint_as_float(h << 16); }
DI float bfhi(unsigned h) { return __uint_as_float(h & 0xffff0000u); }
DI float bfround(float x) { return __uint_as_float(cvt2(x, 0.f) << 16); }
DI float fexp(float x) { return __builtin_amdgcn_exp2f(x * 1.4426950408889634f); }
DI rsrc_t mkrs(const void* p, unsigned bytes) { return __builtin_amdgcn_make_buffer_rsrc((void*)p, (short)0, (int)bytes, 0x00020000); }
DI void dma(rsrc_t rs, unsigned voff, unsigned soff, unsigned lds) {
    asm volatile("s_mov_b32 m0, %0\n\ts_nop 0\n\tbuffer_load_dwordx4 %1, %2, %3 offen lds" :: "s"(lds), "v"(voff), "s"(rs), "s"(soff) : "memory"); }
template <int N> DI void wait_bar() { asm volatile("s_waitcnt vmcnt(%0) lgkmcnt(0)\n\ts_barrier" :: "i"(N) : "memory"); }
template <int N> DI void wait_vm() { asm volatile("s_waitcnt vmcnt(%0)" :: "i"(N) : "memory"); }
DI void lds_bar() { asm volatile("s_waitcnt lgkmcnt(0)\n\ts_barrier" ::: "memory"); }
typedef LAS char* lp;
DI lp lbase(LAS char* L, unsigned off) { lp p = L + off; PIN(p); return p; }
DI bf16x8 ld128(lp p) { return __builtin_bit_cast(bf16x8, *(LAS const u32x4*)p); }
DI bf16x8 ldtr2(lp p0, lp p1) {
    s16x4 a = __builtin_amdgcn_ds_read_tr16_b64_v4i16((LAS s16x4*)p0), b = __builtin_amdgcn_ds_read_tr16_b64_v4i16((LAS s16x4*)p1);
    return __builtin_bit_cast(bf16x8, __builtin_shufflevector(a, b, 0, 1, 2, 3, 4, 5, 6, 7)); }
DI bf16x8 asbf(u32x4 v) { return __builtin_bit_cast(bf16x8, v); }
DI void st128(u32x4 v, rsrc_t rs, unsigned voff, unsigned soff, int aux) {
    if (aux) __builtin_amdgcn_raw_buffer_store_b128(v, rs, voff, soff, SC1); else __builtin_amdgcn_raw_buffer_store_b128(v, rs, voff, soff, 0);
    asm volatile("s_nop 1" :: "v"(v)); }
DI unsigned nosent(unsigned v) { return v < 0xFFFFFFFEu ? v : 0xFFFFFFFEu; }
DI unsigned atomic_read(rsrc_t rs, unsigned voff) { return (unsigned)__builtin_amdgcn_raw_ptr_buffer_atomic_add_i32(0, rs, (int)voff, 0, SC1); }
DI void atomic_inc(rsrc_t rs, unsigned voff) { asm volatile("buffer_atomic_add %0, %1, %2, 0 offen sc1" :: "v"(1u), "v"(voff), "s"(rs) : "memory"); }
struct cf { float r, i; };
DI cf cmul(cf a, cf b) { cf o;
    asm("v_mul_f32 %0, %3, %5\n\tv_mul_f32 %1, %3, %4\n\tv_fma_f32 %0, %2, %4, -%0\n\tv_fma_f32 %1, %2, %5, %1" : "=&v"(o.r), "=&v"(o.i) : "v"(a.r), "v"(a.i), "v"(b.r), "v"(b.i)); return o; }
DI cf cfma(cf a, cf b, cf c) { cf o;
    asm("v_fma_f32 %0, -%3, %5, %6\n\tv_fma_f32 %1, %3, %4, %7\n\tv_fma_f32 %0, %2, %4, %0\n\tv_fma_f32 %1, %2, %5, %1"
        : "=&v"(o.r), "=&v"(o.i) : "v"(a.r), "v"(a.i), "v"(b.r), "v"(b.i), "v"(c.r), "v"(c.i)); return o; }
DI void pk_cfma(f32x2& nr, f32x2& ni, f32x2 l, f32x2 hr, f32x2 hm, f32x2 a, f32x2 c) {
    asm("v_pk_fma_f32 %0, %2, %3, %5 op_sel_hi:[0,1,1]\n\tv_pk_fma_f32 %1, %2, %4, %6 op_sel_hi:[0,1,1]\n\t"
        "v_pk_fma_f32 %0, %2, %4, %0 op_sel:[1,0,0] neg_lo:[1,0,0] neg_hi:[1,0,0]\n\tv_pk_fma_f32 %1, %2, %3, %1 op_sel:[1,0,0]"
        : "=&v"(nr), "=&v"(ni) : "v"(l), "v"(hr), "v"(hm), "v"(a), "v"(c)); }
DI void pk_cmul(f32x2& nr, f32x2& ni, f32x2 l, f32x2 hr, f32x2 hm) {
    asm("v_pk_mul_f32 %0, %2, %3 op_sel_hi:[0,1]\n\tv_pk_mul_f32 %1, %2, %4 op_sel_hi:[0,1]\n\t"
        "v_pk_fma_f32 %0, %2, %4, %0 op_sel:[1,0,0] neg_lo:[1,0,0] neg_hi:[1,0,0]\n\tv_pk_fma_f32 %1, %2, %3, %1 op_sel:[1,0,0]"
        : "=&v"(nr), "=&v"(ni) : "v"(l), "v"(hr), "v"(hm)); }
DI f32x2 pk_scale(f32x2 s, f32x2 a) { f32x2 r; asm("v_pk_mul_f32 %0, %1, %2 op_sel_hi:[0,1]" : "=v"(r) : "v"(s), "v"(a)); return r; }
DI float sum4(float v) {
    auto a = __builtin_amdgcn_permlane16_swap(__float_as_uint(v), __float_as_uint(v), false, false); v = __uint_as_float(a[0]) + __uint_as_float(a[1]);
    auto c = __builtin_amdgcn_permlane32_swap(__float_as_uint(v), __float_as_uint(v), false, false); return __uint_as_float(c[0]) + __uint_as_float(c[1]); }
DI void bcast4(float v, float& r0, float& r1, float& r2, float& r3) {
    auto a = __builtin_amdgcn_permlane16_swap(__float_as_uint(v), __float_as_uint(v), false, false);
    auto e = __builtin_amdgcn_permlane32_swap(a[0], a[0], false, false); auto o = __builtin_amdgcn_permlane32_swap(a[1], a[1], false, false);
    r0 = __uint_as_float(e[0]); r2 = __uint_as_float(e[1]); r1 = __uint_as_float(o[0]); r3 = __uint_as_float(o[1]); }
DI float rowmax16(const f32x16& s) {
    float a = fmaxf(fmaxf(s[0], s[1]), s[2]), b = fmaxf(fmaxf(s[3], s[4]), s[5]);
    a = fmaxf(fmaxf(a, s[6]), s[7]); b = fmaxf(fmaxf(b, s[8]), s[9]); a = fmaxf(fmaxf(a, s[10]), s[11]); b = fmaxf(fmaxf(b, s[12]), s[13]);
    a = fmaxf(fmaxf(a, s[14]), s[15]); a = fmaxf(a, b);
    auto rr = __builtin_amdgcn_permlane32_swap(__float_as_uint(a), __float_as_uint(a), false, false);
    return fmaxf(__uint_as_float(rr[0]), __uint_as_float(rr[1])); }
DI void set_shift(u32x4& q1, float c, int hi) {
    const float c1 = bfround(c), c2 = bfround(c - c1), c3 = c - c1 - c2;
    if (hi) { q1[2] = cvt2(-c1, -c2); q1[3] = cvt2(-c3, 0.f); } }
DI void ladd(LAS char* L, unsigned off, unsigned v) {
    asm volatile("s_mov_b64 exec, 1\n\tds_add_u32 %0, %1\n\ts_mov_b64 exec, -1" :: "v"(L + off), "v"(v) : "memory"); }
DI void lcount(LAS char* L, unsigned off, int) { ladd(L, off, 1u); }
DI unsigned lread(LAS char* L, unsigned off) { return __builtin_amdgcn_readfirstlane(*(volatile LAS unsigned*)(L + off)); }
DI void lwait(LAS char* L, unsigned off, unsigned n) {
    if (__builtin_expect(lread(L, off) < n, 0)) { _Pragma("unroll 1") for (unsigned spin = 0; lread(L, off) < n && spin < (1u << 20); ++spin) { if (LSLEEP) __builtin_amdgcn_s_sleep(LSLEEP); } }
    asm volatile("" ::: "memory"); }
DI void lwait_idle(LAS char* L, unsigned off, unsigned n) {
    _Pragma("unroll 1") for (unsigned spin = 0; lread(L, off) < n && spin < (1u << 18); ++spin) __builtin_amdgcn_s_sleep(2);
    asm volatile("" ::: "memory"); }
DI int xswz(int row) { return (row >> 1) & 7; }
DI u32x4 pack8(const f32x16& a, int e) { return (u32x4){cvt2(a[e], a[e + 1]), cvt2(a[e + 2], a[e + 3]), cvt2(a[e + 4], a[e + 5]), cvt2(a[e + 6], a[e + 7])}; }
#define PAIR(v, p) (f32x2){v[2 * (p)], v[2 * (p) + 1]}

#define STAMP(k)
#define STAMPV(k)

__global__ void __launch_bounds__(512, 2)
gated_mix_kernel(const uint16_t* x, const uint16_t* wq, const uint16_t* wk, const uint16_t* wv, const uint16_t* wo, const uint16_t* lre, const uint16_t* lim,
                 const uint16_t* bre, const uint16_t* bim, const uint16_t* cre, const uint16_t* cim, const uint16_t* logdt, const uint16_t* dskip,
                 const uint16_t* gate, float* out, char* ws) {
    __shared__ __attribute__((aligned(16))) char smem[LDS_BYTES];
    LAS char* L = (LAS char*)smem;
    const unsigned l0 = (unsigned)(uintptr_t)L;
    const int lane = threadIdx.x & 63, w = __builtin_amdgcn_readfirstlane(threadIdx.x >> 6);
    const int b = blockIdx.x;
    STAMP(0);
    {   unsigned long long pc; asm volatile("s_getpc_b64 %0" : "=s"(pc));
        const rsrc_t rsC = mkrs((const void*)(pc & ~255ull), CPREF);
        asm volatile("s_mov_b32 m0, %0\n\ts_nop 4\n\tbuffer_load_dword %1, %2, 0 offen lds" :: "s"(l0 + L_DUMMY), "v"(threadIdx.x * 64), "s"(rsC) : "memory"); }
    const int blk = (b & 7) * 4 + (b >> 6), j = (b >> 3) & 7;
    const int th = w & 1, hd = (w >> 1) & 1;
    const int r32 = lane & 31, hi = lane >> 5, r16 = lane & 15, G = lane >> 4;
    const int tq = r16 >> 2, tp = lane & 3;
    const int sl = lane & 7, r8 = lane >> 3;
    const unsigned l16 = lane * 16;
    const int kind = j & 1, tg = j >> 1;
    unsigned vx = r8 * ROWB + ((sl ^ (r8 >> 1)) << 4); PIN(vx);
    PINS(x); PINS(wq); PINS(wk); PINS(wv); PINS(wo); PINS(lre); PINS(lim); PINS(bre); PINS(bim); PINS(cre); PINS(cim); PINS(logdt); PINS(dskip); PINS(gate); PINS(out); PINS(ws);
    const rsrc_t rsX = mkrs(x, SEQ * ROWB), rsWS = mkrs(ws, WS_END);
    const unsigned opiece = WS_O + blk * 65536 + (th * 16 + 2 * j + hd) * 2048;

    const rsrc_t rsWQ = mkrs(wq, DM * ROWB);
    const int pwq = sl ^ (((r8 >> 1) & 1) << 2);
    const unsigned vw = r8 * ROWB + 112 * j + (pwq << 4);
#define WQD(i0, i1) do { if (pwq != 7) { UNROLL for (int i = (i0); i < (i1); ++i) { const int p = i < 18 ? 3 * i + w - 1 : min(53 + w, 55); dma(rsWQ, vw, p * 8 * ROWB, l0 + L_WQ + p * 1024); } } } while (0)
#define BUMP() do { if (b == 0) atomic_inc(rsWS, lane < 16 ? WS_X + lane * 128 : OOB); } while (0)
    if (w < 4) {
        const int qrow = 32 * th + r32;
        const unsigned rob = L_RO + w * 2048 + l16, lsb = L_LS + (w * 32 + r32) * 16;
        lp a00, a01, a02, a03, a1, ro0, ro1, ro2, rop, lsp, lw0, lw1, lw2, lw3, qs0, qs1, qs2, qs3;
        unsigned kco, kc = 0u;
        u32x4 sel0, sel1, pw0, pw1, kf[4][2], vf[4][2];
        f32x16 o[4];
#define QPREP() do { const unsigned a0o = L_X + qrow * 128 + ((hi ^ xswz(qrow)) << 4); \
        a00 = lbase(L, a0o); a01 = lbase(L, a0o ^ 32); a02 = lbase(L, a0o ^ 64); a03 = lbase(L, a0o ^ 96); \
        a1 = lbase(L, L_WQ + (8 * hi + tq) * 128 + ((56 * hd + 32 * (G & 1) + 8 * tp) ^ (((tq >> 1) & 1) << 6))); \
        kco = lane < 16 ? WS_KVCNT + (b & 7) * 2048 + lane * 128 : OOB; PIN(kco); \
        ro0 = lbase(L, rob ^ 2048); ro1 = lbase(L, (rob ^ 4096) + 8192); ro2 = lbase(L, (rob ^ 6144) + 16384); rop = lbase(L, rob); lsp = lbase(L, lsb); \
        lw0 = lbase(L, hi ? (lsb ^ 512) + w * 4 : L_DUMMY); lw1 = lbase(L, hi ? (lsb ^ 1024) + w * 4 : L_DUMMY); lw2 = lbase(L, hi ? (lsb ^ 1536) + w * 4 : L_DUMMY); lw3 = lbase(L, hi ? lsb + w * 4 : L_DUMMY); \
        qs0 = lbase(L, L_QSH + (w ^ 1) * 2048 + l16); qs1 = lbase(L, L_QSH + (w ^ 2) * 2048 + l16); qs2 = lbase(L, L_QSH + (w ^ 3) * 2048 + l16); qs3 = lbase(L, L_QSH + w * 2048 + l16); \
        const int js = 4 * ((r32 >> 3) & 1) + (r32 & 3); const unsigned one = ((r32 >> 2) & 1) == hi ? 0x3F80u << (16 * (js & 1)) : 0u; \
        UNROLL for (int e = 0; e < 4; ++e) { const unsigned v = (js >> 1) == e ? one : 0u; sel0[e] = r32 < 16 ? v : 0u; sel1[e] = r32 < 16 ? 0u : v; } PIN(sel0); PIN(sel1); \
        o[0] = o[1] = o[2] = o[3] = (f32x16){}; PIN(o[0]); PIN(o[1]); PIN(o[2]); PIN(o[3]); \
        pw1 = (u32x4){0u, 0u, 0u, 0u}; vf[3][1] = pw1; PIN(pw1); PIN(vf[3][1]); } while (0)
        __builtin_amdgcn_s_setprio(2);
        if (w == 0) {
            const unsigned xoff = lane == 0 ? WS_X + (b & 15) * 128 : OOB;
            const rsrc_t rsWK = mkrs(kind ? wv : wk, DM * 56);
            const unsigned vxa = vx ^ (xswz(16 * tg) << 4), vxb = (vx ^ (xswz(16 * tg + 8) << 4)) + 8 * ROWB, xs = (blk * 64 + tg * 16) * ROWB;
            unsigned x0 = 0u, x1 = 0u;
            asm volatile("buffer_atomic_add %0, %1, %2, 0 offen sc0 sc1" : "+v"(x0) : "v"(xoff), "s"(rsWS) : "memory");
            STAMP(36);
            UNROLL for (int c = 0; c < 7; ++c) {
                dma(rsX, vxa, xs + c * 128, l0 + L_X + c * 8192 + tg * 2048); dma(rsX, vxb, xs + c * 128, l0 + L_X + c * 8192 + tg * 2048 + 1024);
                UNROLL for (int n = (7 * c + 1) / 2; n < (7 * c + 8) / 2; ++n) dma(rsWK, l16, n * 1024, l0 + L_WK + n * 1024);
            }
            asm volatile("s_barrier" ::: "memory");
            STAMP(14);
            __builtin_amdgcn_raw_buffer_store_b64((u32x2){~0u, ~0u}, rsWS, lane < 56 ? lane * 8 : OOB, WS_E + (blk * DM + 56 * j) * 8, SC1);
            const int cg0 = kind ? tp : 4 * (tp >> 1) + (tp & 1), cg1 = kind ? 4 + tp : 4 * (tp >> 1) + 2 + (tp & 1);
            const lp aw0 = lbase(L, L_WK + (8 * G + tq) * 56 + cg0 * 8), aw1 = lbase(L, L_WK + (8 * G + tq) * 56 + cg1 * 8);
            const unsigned axo = L_X + (16 * tg + r16) * 128 + ((G ^ xswz(16 * tg + r16)) << 4);
            const lp ax0 = lbase(L, axo), ax1 = lbase(L, axo ^ 64);
            const int sg = tg & 1;
            const unsigned tb = WS_KV + (2 * blk + (tg >> 1)) * 4096 + (kind ? 2048 + sg * 1024 : 0);
            unsigned kva = kind ? ((G >> 1) * 32 + r16) * 16 + (G & 1) * 8 : (G * 32 + 16 * sg + ((r16 & 3) | ((r16 & 4) << 1) | ((r16 & 8) >> 1))) * 16; PIN(kva);
            unsigned kvb = kind ? kva + 256 : kva + 8; PIN(kvb);
            const bool spare = kind ? r16 >= 12 : G == 3;
            unsigned sp2 = (kind && r16 != 12) ? 0u : 0x3F803F80u, sp3 = kind ? sp2 : 0x00003F80u; PIN(sp2); PIN(sp3);
            f32x4 d0 = {0.f, 0.f, 0.f, 0.f}, d1 = d0;
            bf16x8 xf[14], w0[14], w1[14];
#define KVR(c) do { wait_vm<1 + 39 - 2 * ((c) + 1) - (7 * (c) + 8) / 2>(); UNROLL for (int ks = 2 * (c); ks < 2 * (c) + 2; ++ks) { xf[ks] = ld128(((ks & 1) ? ax1 : ax0) + (c) * 8192); \
                        w0[ks] = ldtr2(aw0 + ks * 1792, aw0 + ks * 1792 + 224); w1[ks] = ldtr2(aw1 + ks * 1792, aw1 + ks * 1792 + 224); } SBAR(); } while (0)
#define KVM(c, K) do { UNROLL for (int ks = 2 * (c); ks < 2 * (c) + 2; ++ks) { \
                        if (K) { d0 = MFMA16(xf[ks], w0[ks], d0); d1 = MFMA16(xf[ks], w1[ks], d1); } else { d0 = MFMA16(w0[ks], xf[ks], d0); d1 = MFMA16(w1[ks], xf[ks], d1); } } SBAR(); } while (0)
#define KVP(K) do { KVR(0); STAMP(1); KVR(1); KVM(0, K); KVR(2); KVM(1, K); KVR(3); KVM(2, K); KVR(4); KVM(3, K); KVR(5); KVM(4, K); KVR(6); STAMP(32); KVM(5, K); KVM(6, K); } while (0)
            if (kind) KVP(1); else KVP(0);
            __builtin_amdgcn_raw_buffer_store_b64((u32x2){cvt2(d0[0], d0[1]), cvt2(d0[2], d0[3])}, rsWS, kva, tb, SC1);
            __builtin_amdgcn_raw_buffer_store_b64((u32x2){spare ? sp2 : cvt2(d1[0], d1[1]), spare ? sp3 : cvt2(d1[2], d1[3])}, rsWS, kvb, tb, SC1);
            asm volatile("buffer_atomic_add %0, %1, %2, 0 offen sc0 sc1" : "+v"(x1) : "v"(xoff), "s"(rsWS) : "memory");
            ladd(L, L_KD, 1u); ladd(L, L_XA, 1u); ladd(L, L_XF, 1u);
            STAMP(12);
            QPREP();
            asm volatile("s_waitcnt vmcnt(0)" : "+v"(x0), "+v"(x1) :: "memory");
            STAMP(35);
            const unsigned s0 = __builtin_amdgcn_readfirstlane(x0); unsigned s1 = __builtin_amdgcn_readfirstlane(x1);
            _Pragma("unroll 1") for (unsigned spin = 0; s1 == s0 && spin < SPIN_CAP; ++spin) s1 = __builtin_amdgcn_readfirstlane(atomic_read(rsWS, xoff));
            atomic_inc(rsWS, lane < 8 ? WS_KVCNT + lane * 2048 + (b & 15) * 128 : OOB);
            BUMP();
            STAMP(13);
        } else {
            unsigned g0, g1, g2;
            const int gk = min((w - 1) * 192, 256) + lane;
            const uint16_t* gp = gate + gk; PIN(gp);
            const int ga = 2 * (w - 1) + (w - 1 >= tg ? 2 : 0);
            unsigned vxa = vx ^ (xswz(8 * ga) << 4), vxb = (vx ^ (xswz(8 * ga + 8) << 4)) + 8 * ROWB; const unsigned xs = (blk * 64 + 8 * ga) * ROWB; PIN(vxa); PIN(vxb);
            if (w == 1 && lane < 16) *(LAS unsigned*)(L + L_KD + lane * 4) = 0u;
            lds_bar();
            UNROLL for (int c = 0; c < 7; ++c) { dma(rsX, vxa, xs + c * 128, l0 + L_X + c * 8192 + ga * 1024); dma(rsX, vxb, xs + c * 128, l0 + L_X + c * 8192 + ga * 1024 + 1024); }
            asm volatile("global_load_ushort %0, %3, off\n\tglobal_load_ushort %1, %3, off offset:128\n\tglobal_load_ushort %2, %3, off offset:256"
                         : "=&v"(g0), "=&v"(g1), "=&v"(g2) : "v"(gp) : "memory");
            WQD(0, 6);
            QPREP();
            wait_vm<9>();
            lcount(L, L_XA, lane);
            WQD(6, 19);
            STAMP(14);
            asm volatile("s_waitcnt vmcnt(9)" : "+v"(g0), "+v"(g1), "+v"(g2) :: "memory");
            const lp gl = L + (L_WQ + gk * 128 + (112 ^ (((gk >> 1) & 1) << 6)));
            *(LAS uint16_t*)(gl) = (uint16_t)g0; *(LAS uint16_t*)(gl + 8192) = (uint16_t)g1; *(LAS uint16_t*)(gl + 16384) = (uint16_t)g2;
            lcount(L, L_XF, lane);
            STAMP(1);
        }
        lwait(L, L_XF, 4u);
        STAMP(2);
        f32x16 qa = {};
        { bf16x8 wf[28], xq[28];
          UNROLL for (int ks = 0; ks < 28 + QD_; ++ks) {
            if (ks == WQ2) { STAMP(44); wait_vm<0>(); lcount(L, L_XG, lane); STAMP(45); }
            if (ks == 15) { lwait(L, L_XG, 4u); STAMP(46); }
            if (ks < 28) { wf[ks] = ldtr2(a1 + ks * 2048, a1 + ks * 2048 + 512); xq[ks] = ld128(((ks & 3) == 0 ? a00 : (ks & 3) == 1 ? a01 : (ks & 3) == 2 ? a02 : a03) + (ks >> 2) * 8192); }
            if (ks >= QD_) qa = MFMA32(wf[ks - QD_], xq[ks - QD_], qa);
            if (ks == KPOLL && w == 0) asm volatile("buffer_atomic_add %0, %1, %2, 0 offen sc0 sc1" : "+v"(kc) : "v"(kco), "s"(rsWS) : "memory");
            if (ks == KCHK && w == 0) {
                asm volatile("s_waitcnt vmcnt(0)" : "+v"(kc) :: "memory");
                if (__builtin_expect(__builtin_amdgcn_ballot_w64(kc != 16 && lane < 16) != 0, 0)) { _Pragma("unroll 1") for (unsigned spin = 0; spin < SPIN_CAP; ++spin) {
                    BUMP(); if (__builtin_amdgcn_ballot_w64(atomic_read(rsWS, kco) != 16 && lane < 16) == 0) break; } }
                ladd(L, L_KR, 1u); }
            if (ks == 28) ladd(L, L_QD, 1u);
            SBAR(); } }
        const unsigned kv0 = WS_KV + w * 65536;
        const char* kvp = ws + kv0;
#define LD1(A0, off) asm volatile("global_load_dwordx4 %0, %1, %2 offset:%3 sc1" : "=&v"(A0) : "v"(l16), "s"(kvp), "i"(off) : "memory")
#define LD2(A0, A1, off) do { LD1(A0, off); LD1(A1, (off) + 1024); } while (0)
#define WAIT2(N, A, B) asm volatile("s_waitcnt vmcnt(%2)" : "+v"(A), "+v"(B) : "i"(N) : "memory")
        STAMP(47);
        lwait(L, L_KR, 1u);
        LD2(kf[0][0], kf[0][1], 0); LD2(vf[0][0], vf[0][1], 2048);
        STAMP(48);
        lwait(L, L_QD, 4u);
        asm volatile("s_nop 11" : "+v"(qa));
        const f32x2 cs = {0.18898223650461361f * 1.4426950408889634f, 0.f};
        u32x4 q0[4], q1[4];
        UNROLL for (int e = 0; e < 4; ++e) { const f32x2 a = pk_scale(cs, (f32x2){qa[2 * e], qa[2 * e + 1]}), c = pk_scale(cs, (f32x2){qa[8 + 2 * e], qa[9 + 2 * e]});
            q0[3][e] = cvt2(a[0], a[1]); q1[3][e] = cvt2(c[0], c[1]); }
        if (hi) { q1[3][2] = 0u; q1[3][3] = 0u; }
        *(LAS u32x4*)(qs3) = q0[3]; *(LAS u32x4*)(qs3 + 1024) = q1[3];
        if (hd == 1 && hi) *(LAS float*)(L + L_G + qrow * 4) = __builtin_amdgcn_rcpf(1.0f + fexp(-qa[12]));
        STAMP(3);
        lcount(L, L_QF, lane);
        UNROLL for (int n = 1; n < 3; ++n) { kvp += 4096; LD2(kf[n][0], kf[n][1], 0); LD2(vf[n][0], vf[n][1], 2048); }
        STAMP(4);

        f32x16 sA, sB;
        lwait(L, L_QF, 4u);
        STAMP(5);
        q0[0] = *(LAS const u32x4*)(qs0); q1[0] = *(LAS const u32x4*)(qs0 + 1024); q0[1] = *(LAS const u32x4*)(qs1); q1[1] = *(LAS const u32x4*)(qs1 + 1024);
        q0[2] = *(LAS const u32x4*)(qs2); q1[2] = *(LAS const u32x4*)(qs2 + 1024);
        const float gt = *(LAS const float*)(L + L_G + qrow * 4);
        ladd(L, L_SF, 1u);
#define GIVE(O, RP, LP) do { *(LAS u32x4*)(RP) = pack8(O, 0); *(LAS u32x4*)((RP) + 1024) = pack8(O, 8); *(LAS float*)(LP) = O[12]; } while (0)
        WAIT2(10, kf[0][0], kf[0][1]);
        __builtin_amdgcn_s_setprio(0);
        STAMP(6);
        sA = MFMA32(asbf(kf[0][0]), asbf(q0[0]), (f32x16){}); sA = MFMA32(asbf(kf[0][1]), asbf(q1[0]), sA);
#define EXP(A, i) A[i] = __builtin_amdgcn_exp2f(A[i])
#define EX4(A, i) do { EXP(A, i); EXP(A, i + 1); EXP(A, i + 2); EXP(A, i + 3); PIN(A); } while (0)
#define UNIT(A, B, KN0, KN1, qn, q, qp, V0, VP1, NEXT, H0, H1, H2, H3) do { SBAR(); H0; \
        if (NEXT) { B = MFMA32(asbf(KN0), asbf(q0[qn]), (f32x16){}); PIN(B); } SBAR(); \
        EX4(A, 0); SBAR(); \
        o[qp] = MFMA32(asbf(VP1), asbf(pw1), o[qp]); PIN(o[qp]); SBAR(); H1; \
        pw0[0] = cvt2(A[0], A[1]); pw0[1] = cvt2(A[2], A[3]); PIN(pw0); SBAR(); EX4(A, 4); SBAR(); \
        if (NEXT) { B = MFMA32(asbf(KN1), asbf(q1[qn]), B); PIN(B); } SBAR(); H2; SBAR(); \
        pw0[2] = cvt2(A[4], A[5]); pw0[3] = cvt2(A[6], A[7]); PIN(pw0); SBAR(); EX4(A, 8); SBAR(); H3; \
        o[q] = MFMA32(asbf(V0), asbf(pw0), o[q]); PIN(o[q]); SBAR(); \
        { float e12 = __builtin_amdgcn_exp2f(A[12]), e13 = __builtin_amdgcn_exp2f(A[13]), e14 = __builtin_amdgcn_exp2f(A[14]), e15 = __builtin_amdgcn_exp2f(A[15]); \
          asm volatile("" : "+v"(e12), "+v"(e13), "+v"(e14), "+v"(e15)); SBAR(); \
          pw1[0] = cvt2(A[8], A[9]); pw1[1] = cvt2(A[10], A[11]); SBAR(); pw1[2] = cvt2(e12, e13); pw1[3] = cvt2(e14, e15); PIN(pw1); } SBAR(); } while (0)
#define TRIP(c, n, p, LD, NV, NK) do { \
        UNIT(sA, sB, kf[c][0], kf[c][1], 1, 0, 3, vf[c][0], vf[p][1], true, WAIT2(NV, vf[c][0], vf[c][1]), { if (LD) { kvp += 4096; LD1(kf[p][0], 0); } }, { if (LD) LD1(kf[p][1], 1024); }, {}); \
        UNIT(sB, sA, kf[c][0], kf[c][1], 2, 1, 0, vf[c][0], vf[c][1], true, {}, { if (LD) LD1(vf[p][0], 2048); }, { if (LD) LD1(vf[p][1], 3072); }, {}); \
        UNIT(sA, sB, kf[c][0], kf[c][1], 3, 2, 1, vf[c][0], vf[c][1], true, {}, {}, WAIT2(NK, kf[n][0], kf[n][1]), {}); \
        UNIT(sB, sA, kf[n][0], kf[n][1], 0, 3, 2, vf[c][0], vf[c][1], true, {}, {}, {}, {}); } while (0)
        _Pragma("unroll 1") for (int it = 0; it < 3; ++it) {
            TRIP(0, 1, 3, true, 8, 10); TRIP(1, 2, 0, true, 8, 10); TRIP(2, 3, 1, true, 8, 10); TRIP(3, 0, 2, true, 8, 10);
            STAMPV(17 + it);
        }
        TRIP(0, 1, 3, true, 8, 10);
        ladd(L, L_QL, 1u);
        TRIP(1, 2, 0, false, 8, 6); TRIP(2, 3, 1, false, 4, 2);
        ladd(L, L_QE, 1u);
        lwait(L, L_UF, 8u);
        UNIT(sA, sB, kf[3][0], kf[3][1], 1, 0, 3, vf[3][0], vf[2][1], true, WAIT2(0, vf[3][0], vf[3][1]), {}, {}, {});
        UNIT(sB, sA, kf[3][0], kf[3][1], 2, 1, 0, vf[3][0], vf[3][1], true, {}, {}, {}, {});
        UNIT(sA, sB, kf[3][0], kf[3][1], 3, 2, 1, vf[3][0], vf[3][1], true, {}, GIVE(o[0], ro0, lw0), {}, {});
        UNIT(sB, sA, kf[0][0], kf[0][1], 0, 3, 2, vf[3][0], vf[3][1], false, {}, GIVE(o[1], ro1, lw1), {}, {});
        STAMPV(20);
        o[3] = MFMA32(asbf(vf[3][1]), asbf(pw1), o[3]); PIN(o[3]); SBAR();
        GIVE(o[2], ro2, lw2); SBAR();
        STAMP(7);

        float l3 = o[3][12];
        *(LAS float*)(lw3) = l3;
        {   const unsigned u0 = __float_as_uint(o[0][12]), u1 = __float_as_uint(o[1][12]), u2 = __float_as_uint(o[2][12]), u3 = __float_as_uint(l3);
            const unsigned lo = min(min(u0, u1), min(u2, u3)), up = max(max(u0, u1), max(u2, u3));
            const bool bad = __builtin_amdgcn_ballot_w64(hi && (lo < 0x1F800000u || up > 0x5D800000u)) != 0;
            ladd(L, L_GV, bad ? 0x10001u : 1u); }
        unsigned gv = lread(L, L_GV);
        if (__builtin_expect((gv & 0xffffu) < 4u, 0)) { _Pragma("unroll 1") for (unsigned spin = 0; (gv & 0xffffu) < 4u && spin < (1u << 20); ++spin) gv = lread(L, L_GV); }
        asm volatile("" ::: "memory");
        STAMP(15);
        bf16x8 f[6]; f32x4 m0;
        UNROLL for (int t = 0; t < 6; ++t) f[t] = ld128(rop + (t >> 1) * 8192 + (t & 1) * 1024);
        m0 = *(LAS const f32x4*)(lsp);
        SBAR();
        if (__builtin_expect(gv > 0xffffu, 0)) {
            auto qbar = [&](unsigned n) __attribute__((always_inline)) { asm volatile("s_waitcnt lgkmcnt(0)" ::: "memory"); ladd(L, L_FB, 1u); lwait(L, L_FB, n); };
            float mx[4] = {-INFINITY, -INFINITY, -INFINITY, -INFINITY};
            qbar(4u);
            _Pragma("unroll 1") for (int t = 0; t < 16; ++t) {
                const bf16x8 a = asbf(__builtin_amdgcn_raw_buffer_load_b128(rsWS, l16, kv0 + t * 4096, SC1));
                const bf16x8 c = asbf(__builtin_amdgcn_raw_buffer_load_b128(rsWS, l16, kv0 + t * 4096 + 1024, SC1));
                UNROLL for (int q = 0; q < 4; ++q) { f32x16 s = MFMA32(a, asbf(q0[q]), (f32x16){}); s = MFMA32(c, asbf(q1[q]), s); mx[q] = fmaxf(mx[q], rowmax16(s)); SBAR(); }
            }
            *(LAS float*)(lw0) = mx[0]; *(LAS float*)(lw1) = mx[1]; *(LAS float*)(lw2) = mx[2]; *(LAS float*)(lw3) = mx[3];
            qbar(8u);
            UNROLL for (int q = 0; q < 4; ++q) { const f32x4 n0 = *(LAS const f32x4*)(L + (lsb ^ (((q + 1) & 3) * 512)));
                set_shift(q1[q], fmaxf(fmaxf(n0[0], n0[1]), fmaxf(n0[2], n0[3])), hi); }
            qbar(12u);
            UNROLL for (int q = 0; q < 4; ++q) {
                f32x16 acc = {};
                _Pragma("unroll 1") for (int t = 0; t < 16; ++t) {
                    const bf16x8 a = asbf(__builtin_amdgcn_raw_buffer_load_b128(rsWS, l16, kv0 + t * 4096, SC1));
                    const bf16x8 c = asbf(__builtin_amdgcn_raw_buffer_load_b128(rsWS, l16, kv0 + t * 4096 + 1024, SC1));
                    const bf16x8 d = asbf(__builtin_amdgcn_raw_buffer_load_b128(rsWS, l16, kv0 + t * 4096 + 2048, SC1));
                    const bf16x8 e = asbf(__builtin_amdgcn_raw_buffer_load_b128(rsWS, l16, kv0 + t * 4096 + 3072, SC1));
                    f32x16 s = MFMA32(a, asbf(q0[q]), (f32x16){}); s = MFMA32(c, asbf(q1[q]), s);
                    UNROLL for (int r = 0; r < 16; ++r) s[r] = __builtin_amdgcn_exp2f(s[r]);
                    acc = MFMA32(d, asbf(pack8(s, 0)), acc); acc = MFMA32(e, asbf(pack8(s, 8)), acc);
                }
                if (q == 0) { GIVE(acc, ro0, lw0); } else if (q == 1) { GIVE(acc, ro1, lw1); } else if (q == 2) { GIVE(acc, ro2, lw2); } else { *(LAS float*)(lw3) = acc[12]; o[3] = acc; }
            }
            qbar(16u);
            UNROLL for (int t = 0; t < 6; ++t) f[t] = ld128(rop + (t >> 1) * 8192 + (t & 1) * 1024);
            m0 = *(LAS const f32x4*)(lsp);
        }
        __builtin_amdgcn_s_setprio(2);
        f32x16 t = o[3];
        UNROLL for (int k = 0; k < 6; ++k) t = MFMA32(asbf((k & 1) ? sel1 : sel0), f[k], t);
        {
            float s0 = m0[0] + m0[1], s2 = m0[2] + m0[3]; PIN(s0); PIN(s2);
            f32x2 sc; sc[0] = sc[1] = gt * __builtin_amdgcn_rcpf(s0 + s2); PIN(sc);
            u32x4 oa, ob;
            UNROLL for (int e = 0; e < 4; ++e) { const f32x2 a = sc * (f32x2){t[2 * e], t[2 * e + 1]}; oa[e] = nosent(cvt2(a[0], a[1])); }
            st128(oa, rsWS, l16, opiece, SC1);
            SBAR();
            UNROLL for (int e = 0; e < 4; ++e) { const f32x2 a = sc * (f32x2){t[8 + 2 * e], t[9 + 2 * e]}; ob[e] = nosent(cvt2(a[0], a[1])); }
            if (hi) { ob[2] = 0u; ob[3] = 0u; }
            st128(ob, rsWS, l16, opiece + 1024, SC1);
        }
        __builtin_amdgcn_s_setprio(0);
        STAMP(8);
        ladd(L, L_OD, 1u);
        const rsrc_t rsD = mkrs(dskip, ROWB), rsO = mkrs(out, SEQ * DM * 4);
        const int lr = (lane * 74) >> 10, gm = lane - 14 * lr, row = 16 * w + lr, col = 56 * j + 4 * gm;
        f32x4 r[4]; u32x2 xv[4]; float gv4[4];
        const u32x2 dv = __builtin_amdgcn_raw_buffer_load_b64(rsD, col * 2, 0, 0);
        UNROLL for (int e = 0; e < 4; ++e) { xv[e] = __builtin_amdgcn_raw_buffer_load_b64(rsX, lane < 56 ? (blk * 64 + row) * ROWB + col * 2 : OOB, e * 4 * ROWB, 0);
                                             gv4[e] = *(LAS const float*)(L + L_G + (row + 4 * e) * 4); }
        lp rp[4];
        UNROLL for (int e = 0; e < 4; ++e) rp[e] = lbase(L, L_RED + (gm >> 3) * 32768 + (w >> 1) * 16384 + ((gm & 7) >> 1) * 1024 + (gm & 1) * 512 + ((((row + 4 * e) & 31) ^ gm) << 4));
        unsigned oo = lane < 56 ? ((blk * 64 + row) * DM + col) * 4 : OOB; PIN(oo);
        SBAR();
        UNROLL for (int e = 0; e < 4; ++e) { const float g1 = 1.0f - gv4[e];
            float t0 = bf2f(xv[e][0]) * bf2f(dv[0]), t1 = bfhi(xv[e][0]) * bfhi(dv[0]), t2 = bf2f(xv[e][1]) * bf2f(dv[1]), t3 = bfhi(xv[e][1]) * bfhi(dv[1]);
            PIN(t0); PIN(t1); PIN(t2); PIN(t3); t0 *= g1; t1 *= g1; t2 *= g1; t3 *= g1; PIN(t0); PIN(t1); PIN(t2); PIN(t3);
            r[e] = (f32x4){t0, t1, t2, t3}; }
        lds_bar();
        STAMP(10);
        UNROLL for (int e = 0; e < 4; ++e) {
            r[e] += (*(LAS const f32x4*)(rp[e]) + *(LAS const f32x4*)(rp[e] + 4096)) + (*(LAS const f32x4*)(rp[e] + 8192) + *(LAS const f32x4*)(rp[e] + 12288));
            __builtin_amdgcn_raw_buffer_store_b128(__builtin_bit_cast(u32x4, r[e]), rsO, oo, e * 4 * DM * 4, OAUX); asm volatile("s_nop 1" :: "v"(r[e])); }
    } else {
        const int u = w - 4, st = min(16 * u + r16, 55);
        const unsigned eoff = (G == 0 && 16 * u + r16 < 56) ? st * 8 : OOB;
        const rsrc_t rsBR = mkrs(bre, DM * ROWB), rsBI = mkrs(bim, DM * ROWB);
        const unsigned vb0_ = (56 * j + 16 * u + r8) * ROWB + ((sl ^ (r8 >> 1)) << 4), vb1_ = u < 3 ? (vb0_ ^ 64) + 8 * ROWB : OOB;
#define DST2(a) (u < 3 ? (a) : l0 + L_DUMMY)
        unsigned p0, p1, p2;
        const uint16_t *a0 = lre + 56 * j + st, *a1 = lim + 56 * j + st, *a2 = logdt + 56 * j + st; PIN(a0); PIN(a1); PIN(a2);
#define TOUCH(ptr, bytes, vo) do { const rsrc_t r_ = mkrs(ptr, bytes); \
            asm volatile("s_mov_b32 m0, %0\n\ts_nop 0\n\tbuffer_load_dword %1, %2, 0 offen lds" :: "s"(l0 + L_DUMMY), "v"(vo), "s"(r_) : "memory"); } while (0)
        if (PFE == 1) { if (u < 3) TOUCH(u == 0 ? wq : u == 1 ? bre : bim, DM * ROWB, lane < 25 ? (b * 25 + lane) * 64 : OOB); else TOUCH(x, SEQ * ROWB, lane < 56 ? (blk * 448 + j * 56 + lane) * 128 : OOB); }
        const bool master = b == 0 && w == 7;
        if (master) {
            __builtin_amdgcn_raw_buffer_store_b32(0u, rsWS, lane * 128, 0, SC1);
            __builtin_amdgcn_raw_buffer_store_b32(0u, rsWS, lane * 128 + 8192, 0, SC1);
            __builtin_amdgcn_raw_buffer_store_b32(0u, rsWS, lane < 32 ? lane * 128 + 16384 : OOB, 0, SC1);
        }
        asm volatile("s_barrier" ::: "memory");
        if (master) { wait_vm<0>(); BUMP(); }
        if (PFE == 2 && u < 3) {
            const int r = b >> 3;
            TOUCH(u == 0 ? wq : u == 1 ? bre : bim, DM * ROWB, lane < 49 ? (r * 98 + lane) * 128 : OOB);
            TOUCH(u == 0 ? wq : u == 1 ? bre : bim, DM * ROWB, lane < 49 ? (r * 98 + 49 + lane) * 128 : OOB);
        }
        if (PFE == 0) { if (u < 3) TOUCH(u == 0 ? wq : u == 1 ? bre : bim, DM * ROWB, lane < 25 ? (b * 25 + lane) * 64 : OOB); }
        asm volatile("global_load_ushort %0, %3, off\n\tglobal_load_ushort %1, %4, off\n\tglobal_load_ushort %2, %5, off"
                     : "=&v"(p0), "=&v"(p1), "=&v"(p2) : "v"(a0), "v"(a1), "v"(a2) : "memory");
#define BOFF(c) ((c) % 3 == 0 ? L_B0 : L_WK + ((c) % 3 - 1) * 14336)
#define BD(c) do { dma(rsBR, vb0_, (c) * 128, l0 + BOFF(c) + u * 2048); dma(rsBI, vb0_, (c) * 128, l0 + BOFF(c) + 7168 + u * 2048); \
                   dma(rsBR, vb1_, (c) * 128, DST2(l0 + BOFF(c) + u * 2048 + 1024)); dma(rsBI, vb1_, (c) * 128, DST2(l0 + BOFF(c) + 7168 + u * 2048 + 1024)); } while (0)
        STAMP(14);
        asm volatile("s_waitcnt vmcnt(0)" : "+v"(p0), "+v"(p1), "+v"(p2) :: "memory");
        if (master) BUMP();
        f32x2 lam, coef; cf lam8, lam16, lam64, wlo, whi, zG;
        {   const float lr = bf2f(p0), li = bf2f(p1), dt = bfround(fexp(bf2f(p2)));
            const float er = fexp(dt * lr), rev = __builtin_amdgcn_fractf(dt * li * 0.15915494309189535f);
            const cf l1 = {er * __builtin_amdgcn_cosf(rev), er * __builtin_amdgcn_sinf(rev)};
            const float inv = __builtin_amdgcn_rcpf(lr * lr + li * li);
            const cf cq = cmul((cf){l1.r - 1.0f, l1.i}, (cf){lr * inv, -li * inv});
            lam = (f32x2){l1.r, l1.i}; coef = (f32x2){cq.r, cq.i};
            const cf l2 = cmul(l1, l1), l4 = cmul(l2, l2); lam8 = cmul(l4, l4); lam16 = cmul(lam8, lam8);
            const cf l32 = cmul(lam16, lam16); lam64 = cmul(l32, l32);
            const cf l128 = cmul(lam64, lam64), l256 = cmul(l128, l128), l512 = cmul(l256, l256), l1024 = cmul(l512, l512), one = {1.f, 0.f};
            wlo = cmul((G & 1) ? lam16 : one, (G & 2) ? l32 : one); whi = cmul(wlo, lam8);
            zG = cmul((G & 1) ? l512 : one, (G & 2) ? l1024 : one);
            asm volatile("" : "+v"(lam), "+v"(coef), "+v"(lam8.r), "+v"(lam8.i), "+v"(lam16.r), "+v"(lam16.i), "+v"(lam64.r), "+v"(lam64.i),
                              "+v"(wlo.r), "+v"(wlo.i), "+v"(whi.r), "+v"(whi.i), "+v"(zG.r), "+v"(zG.i) :: "memory"); }
        const int rr = u < 3 ? r16 : r16 & 7, rg = (r32 & 3) + 4 * (r32 >> 3), tk = 32 * ((r32 >> 2) & 1) + (rg >> 1) + 8 * (rg & 1);
        const unsigned bro = (G & 1) * 7168 + u * 2048 + rr * 128 + ((hi ^ (rr >> 1)) << 4), ato = L_X + tk * 128 + ((hi ^ xswz(tk)) << 4);
        lp br[2][4], at[4];
        UNROLL for (int k = 0; k < 4; ++k) { br[0][k] = lbase(L, L_B0 + (bro ^ (k << 5))); br[1][k] = lbase(L, L_WK + (bro ^ (k << 5))); at[k] = lbase(L, ato ^ (k << 5)); }
        if (master) { _Pragma("unroll 1") for (unsigned spin = 0; lread(L, L_KD) < 1u && spin < SPIN_CAP; ++spin) { BUMP(); __builtin_amdgcn_s_sleep(8); } BUMP(); wait_vm<0>(); }
        if (B0EARLY) BD(0);
        lwait(L, L_KD, 1u);
        STAMP(1);
        if (!B0EARLY) BD(0);
        BD(1); BD(2);
        f32x16 ur = {}, ui = {};
        bf16x8 fb[2][4], fx[2][8];
#define BP(c, k) ((c) % 3 == 0 ? br[0][k] : br[1][k] + ((c) % 3 - 1) * 14336)
#define BRD(c) do { wait_vm<(c) < 5 ? 8 : 4 * (6 - (c))>(); UNROLL for (int k = 0; k < 4; ++k) fb[(c) & 1][k] = ld128(BP(c, k)); \
                    if ((c) < 4) { asm volatile("s_waitcnt lgkmcnt(0)" ::: "memory"); BD((c) + 3); } } while (0)
#define XRD(c, i) fx[(c) & 1][i] = ld128(at[(i) >> 1] + (c) * 8192 + ((i) & 1) * 2048)
#define UCH(c) UNROLL for (int i = 0; i < 8; ++i) { \
            if (i & 1) ui = MFMA32(fx[(c) & 1][i], fb[(c) & 1][i >> 1], ui); else ur = MFMA32(fx[(c) & 1][i], fb[(c) & 1][i >> 1], ur); \
            SBAR(); \
            if (i == BRI && (c) < 6) BRD((c) < 6 ? (c) + 1 : 6); \
            if ((c) < 6) XRD((c) + 1, i); \
            SBAR(); }
        BRD(0);
        lwait(L, L_XA, 4u);
        STAMP(2);
        UNROLL for (int i = 0; i < 8; ++i) XRD(0, i);
        SBAR();
        UCH(0) UCH(1) UCH(2)
        __builtin_amdgcn_s_setprio(UPRIO);
        UCH(3) UCH(4) UCH(5) UCH(6)
        if (TCW == 0) {
        if (u < 3) TOUCH(u == 0 ? wo : u == 1 ? cre : cim, DM * ROWB, lane < 25 ? (b * 25 + lane) * 64 : OOB);
        }
        lcount(L, L_UF, lane);
        UNROLL for (int r = 0; r < 16; ++r) {
            auto sw = __builtin_amdgcn_permlane16_swap(__float_as_uint(ur[r]), __float_as_uint(ui[r]), false, false); ur[r] = __uint_as_float(sw[0]); ui[r] = __uint_as_float(sw[1]); }
        STAMP(3);
        f32x2 hr, hm;
        UNROLL for (int p = 0; p < 8; ++p) {
            f32x2 tr, ti; pk_cmul(tr, ti, coef, PAIR(ur, p), PAIR(ui, p));
            ur[2 * p] = tr[0]; ur[2 * p + 1] = tr[1]; ui[2 * p] = ti[0]; ui[2 * p + 1] = ti[1];
            if (p == 0) { hr = tr; hm = ti; } else { f32x2 nr, ni; pk_cfma(nr, ni, lam, hr, hm, tr, ti); hr = nr; hm = ni; }
        }
        const cf elo = {hr[0], hm[0]}, e16 = cfma(lam8, elo, (cf){hr[1], hm[1]});
        cf e0, e1, e2, e3; bcast4(e16.r, e0.r, e1.r, e2.r, e3.r); bcast4(e16.i, e0.i, e1.i, e2.i, e3.i);
        const cf t2 = cfma(lam16, e0, e1), t3 = cfma(lam16, t2, e2), eb = cfma(lam16, t3, e3);
        lwait(L, L_KR, 1u); lwait(L, L_QF, 4u);
        __builtin_amdgcn_raw_buffer_store_b64((u32x2){nosent(__float_as_uint(eb.r)), nosent(__float_as_uint(eb.i))}, rsWS, eoff, WS_E + (blk * DM + 56 * j) * 8, SC1);
        if (TCW == 1) {
        if (u < 3) TOUCH(u == 0 ? wo : u == 1 ? cre : cim, DM * ROWB, lane < 25 ? (b * 25 + lane) * 64 : OOB);
        }
        const cf pg = {G == 0 ? 0.f : G == 1 ? e0.r : G == 2 ? t2.r : t3.r, G == 0 ? 0.f : G == 1 ? e0.i : G == 2 ? t2.i : t3.i}, ph = cfma(lam8, pg, elo);
        STAMP(4);
        asm volatile("s_mov_b32 m0, %0\n\ts_nop 0\n\tbuffer_load_dword %1, %2, %3 offen sc1 lds"
                     :: "s"(l0 + L_DUMMY), "v"(u == 0 ? lane * 128 : OOB), "s"(rsWS), "s"(WS_KV + (b >> 3) * 8192) : "memory");
        st128((u32x4){~0u, ~0u, ~0u, ~0u}, rsWS, l16, opiece, SC1);
        st128((u32x4){~0u, ~0u, ~0u, ~0u}, rsWS, l16, opiece + 1024, SC1);
        const float gt = *(LAS const float*)(L + L_G + (32 * th + r32) * 4), go = *(LAS const float*)(L + L_G + (32 * (th ^ 1) + r32) * 4);
        lwait(L, L_UF, 4u);
        const rsrc_t rsCR = mkrs(cre, DM * ROWB), rsCI = mkrs(cim, DM * ROWB);
        const unsigned vc = (56 * j + 8 * u + r8) * ROWB + ((sl ^ ((4 * u + (r8 >> 1)) & 7)) << 4), vc4 = (56 * j + 32 + r8) * ROWB + ((sl ^ (r8 >> 1)) << 4);
        const rsrc_t rsCX = mkrs((u & 1) ? cim : cre, DM * ROWB);
#define CD(n) do { if ((n) < 14) { if ((n) & 1) dma(rsCI, vc, ((n) >> 1) * 128, l0 + L_CIM + ((n) >> 1) * 7168 + u * 1024); else dma(rsCR, vc, ((n) >> 1) * 128, l0 + L_C + ((n) >> 1) * 7168 + u * 1024); } \
            else { const int p_ = min(2 * ((n) - 14) + (u >> 1), 20), cc_ = (p_ * 43) >> 7, g_ = p_ - 3 * cc_;     \
                   dma(rsCX, vc4 ^ ((g_ & 1) << 6), g_ * 8 * ROWB + cc_ * 128, l0 + (u & 1) * L_CIM + cc_ * 7168 + (4 + g_) * 1024); } } while (0)
        const rsrc_t rsE = mkrs(ws + WS_E + 56 * j * 8, 32 * DM * 8 - 56 * j * 8);
        const int ebase = (blk - 1 - 8 * G) * (DM * 8) + st * 8;
        const unsigned hoff = lane == 0 ? WS_HCNT + blk * 128 : OOB;
        UNROLL for (int n = 0; n < ELDN; ++n) { CD(n); PACE(); }
        STAMP(38);
        {
            u32x2 ev[8];
            UNROLL for (int m = 0; m < 8; m += 2)
                asm volatile("buffer_load_dwordx2 %0, %2, %4, 0 offen sc1\n\tbuffer_load_dwordx2 %1, %3, %4, 0 offen sc1"
                             : "=&v"(ev[m]), "=&v"(ev[m + 1]) : "v"(ebase - m * (DM * 8)), "v"(ebase - (m + 1) * (DM * 8)), "s"(rsE) : "memory");
            UNROLL for (int n = ELDN; n < ELDN + 5; ++n) { PACE(); CD(n); }
            asm volatile("s_waitcnt vmcnt(5)" : "+v"(ev[0]), "+v"(ev[1]), "+v"(ev[2]), "+v"(ev[3]), "+v"(ev[4]), "+v"(ev[5]), "+v"(ev[6]), "+v"(ev[7]) :: "memory");
            _Pragma("unroll 1") for (unsigned spin = 0; spin < SPIN_CAP; ++spin) {
                unsigned mx = 0;
                UNROLL for (int m = 0; m < 8; ++m) mx = max(mx, max(ev[m][0], ev[m][1]));
                if (__builtin_expect(__builtin_amdgcn_ballot_w64(mx == ~0u) == 0, 1)) break;
                __builtin_amdgcn_s_sleep(8);
                asm volatile("" ::: "memory");
                UNROLL for (int m = 0; m < 8; ++m) ev[m] = __builtin_amdgcn_raw_buffer_load_b64(rsE, ebase - m * (DM * 8), 0, SC1);
            }
            STAMP(39);
            cf part = {__uint_as_float(ev[7][0]), __uint_as_float(ev[7][1])};
            UNROLL for (int m = 6; m >= 0; --m) part = cfma(lam64, part, (cf){__uint_as_float(ev[m][0]), __uint_as_float(ev[m][1])});
            part = cmul(zG, part);
            if (ebase < 0) part = (cf){0.f, 0.f};
            PIN(part.r); SBAR(); CD(ELDN + 5); SBAR();
            part.r = sum4(part.r); part.i = sum4(part.i);
            const cf ilo = cfma(wlo, part, pg), ihi = cfma(whi, part, ph);
            hr = (f32x2){ilo.r, ihi.r}; hm = (f32x2){ilo.i, ihi.i};
            u32x4 pr0, pr1, pi0, pi1;
            UNROLL for (int p = 0; p < 8; ++p) {
                f32x2 nr, ni; pk_cfma(nr, ni, lam, hr, hm, PAIR(ur, p), PAIR(ui, p)); hr = nr; hm = ni;
                unsigned cr, ci; asm("v_cvt_pk_bf16_f32 %0, %2, %3\n\tv_cvt_pk_bf16_f32 %1, -%4, -%5" : "=&v"(cr), "=&v"(ci) : "v"(hr[0]), "v"(hr[1]), "v"(hm[0]), "v"(hm[1]));
                if (p < 4) { pr0[p & 3] = cr; pi0[p & 3] = ci; } else { pr1[p & 3] = cr; pi1[p & 3] = ci; }
                if (p == 1 || p == 5) { PIN(hr); SBAR(); CD(ELDN + 6 + (p >> 2)); SBAR(); }
            }
            const unsigned sb = L_HS + u * 4096, wa = sb + r16 * 128 + (((2 * G) ^ (r16 & 7)) << 4);
            const int tok = (lane & 48) + (r16 >> 1) + 8 * (lane & 1);
            const unsigned gha = (tok >> 5) * 57344 + (tok & 31) * 16, ghb = u < 3 ? gha + 1024 : OOB, gs = WS_H + blk * 114688 + (7 * j + 2 * u) * 1024;
            const unsigned rao = sb + tq * 128 + (((2 * G + (tp >> 1)) ^ tq) << 4) + 8 * (tp & 1);
            const lp wa0 = lbase(L, wa), wa1 = lbase(L, wa ^ 16), ra = lbase(L, rao), rb = lbase(L, (rao ^ 64) + 512);
            *(LAS u32x4*)(wa0) = pr0; *(LAS u32x4*)(wa1) = pr1; *(LAS u32x4*)(wa0 + 2048) = pi0; *(LAS u32x4*)(wa1 + 2048) = pi1;
            asm volatile("" ::: "memory");
            UNROLL for (int pl = 0; pl < 2; ++pl) {
                const bf16x8 c0 = ldtr2(ra + pl * 2048, rb + pl * 2048), c1 = ldtr2(ra + pl * 2048 + 1024, rb + pl * 2048 + 1024);
                st128(__builtin_bit_cast(u32x4, c0), rsWS, gha + pl * 512, gs, SC1);
                st128(__builtin_bit_cast(u32x4, c1), rsWS, ghb + pl * 512, gs, SC1);
            }
        }
        STAMP(5);
        lcount(L, L_SF, lane);
        UNROLL for (int n = ELDN + 8; n < 25; ++n) { PACE(); CD(n); }
        wait_vm<25 - ELDN - 8>();
        atomic_inc(rsWS, hoff);
        lwait(L, L_SF, 8u);
        unsigned hc = 0u;
        { const rsrc_t rsWO = mkrs(wo, DM * ROWB);
          UNROLL for (int n = 0; n < 14; ++n) { dma(rsWO, vw, (u * 14 + n) * 8 * ROWB, l0 + L_WO + (u * 14 + n) * 1024);
              if (n == 9) asm volatile("buffer_atomic_add %0, %1, %2, 0 offen sc0 sc1" : "+v"(hc) : "v"(hoff), "s"(rsWS) : "memory");
              PACE(); } }
        STAMP(40);
        asm volatile("s_waitcnt vmcnt(4)" : "+v"(hc) :: "memory");
        lcount(L, L_WF, lane);
        if (__builtin_expect(__builtin_amdgcn_readfirstlane(hc) != 32, 0)) {
            _Pragma("unroll 1") for (unsigned spin = 0; __builtin_amdgcn_readfirstlane(atomic_read(rsWS, hoff)) != 32 && spin < SPIN_CAP; ++spin) __builtin_amdgcn_s_sleep(8); }
        const unsigned hb = WS_H + blk * 114688 + u * 1024;
        u32x4 h0[14], h1[14];
        UNROLL for (int m = 0; m < 3; ++m) { h0[m] = __builtin_amdgcn_raw_buffer_load_b128(rsWS, l16, hb + th * 57344 + m * 4096, SC1); PACE(); }
        const unsigned cx = (32 * (G & 1) + 8 * tp) ^ (((tq >> 1) & 1) << 6), nb = L_WO + (112 * u + tq) * 128, wa_ = nb + hi * 512;
        const lp wa0_ = lbase(L, wa_ + cx), wa1_ = lbase(L, wa_ + (cx ^ 64)), nb0 = lbase(L, nb + cx), nb1 = lbase(L, nb + (cx ^ 64));
        wait_vm<3>();
        bf16x8 wf0[8], wf1[8];
        UNROLL for (int k = 0; k < 8; ++k) { const unsigned ro = (28 * (k >> 1) + 16 * (k & 1)) * 128;
            wf0[k] = ldtr2(wa0_ + ro, ((k & 1) ? nb0 : wa0_) + ro + 1024); wf1[k] = ldtr2(wa1_ + ro, ((k & 1) ? nb1 : wa1_) + ro + 1024); }
        lcount(L, L_UF, lane);
        STAMP(42);
        UNROLL for (int m = 3; m < HP; ++m) { h0[m] = __builtin_amdgcn_raw_buffer_load_b128(rsWS, l16, hb + th * 57344 + m * 4096, SC1); PACE(); }
        lwait_idle(L, L_QL, 4u);
        UNROLL for (int m = HP; m < 14; ++m) h0[m] = __builtin_amdgcn_raw_buffer_load_b128(rsWS, l16, hb + th * 57344 + m * 4096, SC1);
        UNROLL for (int m = 0; m < 14; ++m) h1[m] = __builtin_amdgcn_raw_buffer_load_b128(rsWS, l16, hb + (th ^ 1) * 57344 + m * 4096, SC1);
        const int rbc = min(32 + r32, 55);
        const unsigned cao = hi * L_CIM + r32 * 128 + ((u ^ ((r32 >> 1) & 7)) << 4), cbo = hi * L_CIM + rbc * 128 + ((u ^ ((rbc >> 1) & 7)) << 4);
        const lp ca0 = lbase(L, cao), ca1 = lbase(L, cao ^ 64), cb0 = lbase(L, cbo), cb1 = lbase(L, cbo ^ 64);
        unsigned rp = l0 + L_RED + th * 16384 + u * 4096 + (l16 ^ (hi << 4)); PIN(rp);
        const unsigned oa = WS_O + blk * 65536 + (th * 16 + 4 * u) * 2048, ob = WS_O + blk * 65536 + ((th ^ 1) * 16 + 4 * u) * 2048;
        f32x2 s2 = {2.0f * (1.0f - gt), 0.f}, s3 = {2.0f * (1.0f - go), 0.f}; PIN(s2); PIN(s3);
        lwait(L, L_WF, 4u);
        STAMP(41);
        __builtin_amdgcn_s_setprio(YPRIO);
        lwait_idle(L, L_QE, 4u);
        __builtin_amdgcn_s_sleep(YSLEEP);
        STAMP(15);
        f32x16 y[4]; y[0] = y[1] = y[2] = y[3] = (f32x16){};
        u32x4 of[16];
#define OLD(k0) UNROLL for (int k = (k0); k < (k0) + 8; ++k) of[k] = __builtin_amdgcn_raw_buffer_load_b128(rsWS, l16 + (k & 3) * 1024, (k < 8 ? oa : ob) + ((k >> 2) & 1) * 4096, SC1)
        {   bf16x8 fa[28], fc[28];
            UNROLL for (int m = -YD; m < 28; ++m) {
                if (m + YD < 28) { const int n = (m + YD) % 14; fa[m + YD] = ld128(((n & 1) ? ca1 : ca0) + (n >> 1) * 7168); fc[m + YD] = ld128(((n & 1) ? cb1 : cb0) + (n >> 1) * 7168); }
                if (m >= 0 && m < 14) { y[0] = MFMA32(fa[m], asbf(h0[m]), y[0]); y[1] = MFMA32(fc[m], asbf(h0[m]), y[1]); }
                if (m >= 14) { y[2] = MFMA32(fa[m], asbf(h1[m - 14]), y[2]); y[3] = MFMA32(fc[m], asbf(h1[m - 14]), y[3]); }
                if (m >= 16 && m < 24) { UNROLL for (int k = 0; k < 2; ++k) { const f32x2 a = pk_scale(s2, (f32x2){y[k][2 * (m - 16)], y[k][2 * (m - 16) + 1]}); y[k][2 * (m - 16)] = a[0]; y[k][2 * (m - 16) + 1] = a[1]; } PIN(y[0]); PIN(y[1]); }
                if (OM < 28 && m == OM) { lwait(L, L_OD, ODN); if (OSLEEP) __builtin_amdgcn_s_sleep(OSLEEP); STAMP(43); OLD(0); }
                if (OM < 28 && m == (OM + 2 < 27 ? OM + 2 : 27)) { OLD(8); }
                if (m == 26) ladd(L, L_YF, 1u);
                SBAR();
            } }
        STAMP(9);
        lwait(L, L_YF, 4u);
        asm volatile("s_nop 11" : "+v"(y[2]), "+v"(y[3]));
        UNROLL for (int k = 2; k < 4; ++k) UNROLL for (int e = 0; e < 8; ++e) { const f32x2 a = pk_scale(s3, (f32x2){y[k][2 * e], y[k][2 * e + 1]}); y[k][2 * e] = a[0]; y[k][2 * e + 1] = a[1]; }
        asm volatile("s_nop 1" : "+v"(y[2]), "+v"(y[3]));
        if (OM >= 28) { lwait(L, L_OD, ODN); if (OSLEEP) __builtin_amdgcn_s_sleep(OSLEEP); STAMP(43); OLD(0); OLD(8); }
#define OMAX(mx, k) mx = max(max(mx, max(of[k][0], of[k][1])), max(of[k][2], of[k][3]))
#define OFIX(mx, k0) do { if (__builtin_expect(__builtin_amdgcn_ballot_w64(mx == ~0u) != 0, 0)) { _Pragma("unroll 1") for (unsigned spin = 0; spin < SPIN_CAP; ++spin) { \
            asm volatile("" ::: "memory");                              \
            OLD(k0); mx = 0; UNROLL for (int k = (k0); k < (k0) + 8; ++k) OMAX(mx, k); \
            if (__builtin_amdgcn_ballot_w64(mx == ~0u) == 0) break; } } } while (0)
        SBAR();
        unsigned mxa = 0, mxb = 0;
        UNROLL for (int k = 0; k < 8; ++k) OMAX(mxa, k);
        OFIX(mxa, 0);
        STAMP(10);
#define REDW(k, Y, e) if ((e) < 3 || !((k) & 1)) *(LAS f32x4*)((lp)(uintptr_t)(rp ^ (((k) >> 1) * 16384 + (8 * ((k) & 1) + 2 * (e)) * 16)) + ((k) & 1) * 32768 + (e) * 1024) = (f32x4){Y[4 * (e)], Y[4 * (e) + 1], Y[4 * (e) + 2], Y[4 * (e) + 3]}
        UNROLL for (int k = 0; k < 8; ++k) { y[0] = MFMA32(wf0[k], asbf(of[k]), y[0]); SBAR(); OMAX(mxb, 8 + k); PIN(mxb); SBAR(); }
        UNROLL for (int k = 0; k < 8; ++k) { y[1] = MFMA32(wf1[k], asbf(of[k]), y[1]); SBAR(); if (k >= 3 && k < 7) REDW(0, y[0], k - 3); SBAR(); }
        OFIX(mxb, 8);
        UNROLL for (int k = 0; k < 8; ++k) { y[2] = MFMA32(wf0[k], asbf(of[8 + k]), y[2]); SBAR(); if (k >= 3 && k < 7) REDW(1, y[1], k - 3); SBAR(); }
        UNROLL for (int k = 0; k < 8; ++k) { y[3] = MFMA32(wf1[k], asbf(of[8 + k]), y[3]); SBAR(); if (k >= 3 && k < 7) REDW(2, y[2], k - 3); SBAR(); }
        UNROLL for (int e = 0; e < 4; ++e) REDW(3, y[3], e);
        lds_bar();
    }
    STAMP(11);
}

extern "C" void kernel_launch(void* const* d_in, const int* in_sizes, int n_in, void* d_out, int out_size, void* d_ws, size_t ws_size,
                              hipStream_t stream) {
    const uint16_t* const* in = (const uint16_t* const*)d_in;
    gated_mix_kernel<<<256, 512, 0, stream>>>(in[0], in[1], in[2], in[3], in[4], in[6], in[7], in[8], in[9], in[10], in[11], in[12], in[13], in[14],
                                              (float*)d_out, (char*)d_ws);
}
```

```cpp
#include <hip/hip_runtime.h>
#include <hip/hip_bf16.h>
#include <stdint.h>

typedef __bf16 bf16x8 __attribute__((ext_vector_type(8)));
typedef __bf16 bf16x2 __attribute__((ext_vector_type(2)));
typedef float f32x16 __attribute__((ext_vector_type(16)));
typedef float f32x4 __attribute__((ext_vector_type(4)));
typedef float f32x2 __attribute__((ext_vector_type(2)));
typedef unsigned u32x4 __attribute__((ext_vector_type(4)));
typedef unsigned u32x2 __attribute__((ext_vector_type(2)));
typedef short s16x4 __attribute__((ext_vector_type(4)));
typedef __amdgpu_buffer_rsrc_t rsrc_t;
#define LAS __attribute__((address_space(3)))
#define DI __device__ __forceinline__
#define SBAR() __builtin_amdgcn_sched_barrier(0)
#define PIN(x) asm volatile("" : "+v"(x))
#define PINS(x) asm volatile("" : "+s"(x))
#define MFMA32(a, b, c) __builtin_amdgcn_mfma_f32_32x32x16_bf16((a), (b), (c), 0, 0, 0)
#define MFMA16(a, b, c) __builtin_amdgcn_mfma_f32_16x16x32_bf16((a), (b), (c), 0, 0, 0)
#define UNROLL _Pragma("unroll")
#define PACE() asm volatile("s_sleep %0" :: "i"(CSLEEP) : "memory")
#define SC1 16

#define CSLEEP 3
#define YSLEEP 1
#define CPREF 32768
#define WQ2 8
#define YD 3
#define OSLEEP 6
#define PACKED 1
#define ODN 4u
#define PFC 0
#define B0EARLY 0
#define KCHK 29
#define PFE 0
#define ODRAIN 0
#define ELDN 14
#define QD_ 7
#define OM 28
#define BRI 7
#define TCW 0
#define LSLEEP 0
#define REGB 1
#define HP 6
#define YPRIO 0
#define AMF 1
#define DRAINST 1
#define OAUX 16
#define QREP 2
#define KPOLL 16
#define UPRIO 1
#define SPRIO 0
#define OM0 16
#define OM1 22
#define MAP42 0

constexpr int SEQ = 2048, DM = 448, ROWB = DM * 2;
constexpr unsigned WS_KVCNT = 0;
constexpr unsigned WS_HCNT = 16384;
constexpr unsigned WS_X = 20480;
constexpr unsigned OOB = 0x7FFFFFF0u;
constexpr unsigned WS_KV = 65536;
constexpr unsigned WS_E = WS_KV + 262144;
constexpr unsigned WS_H = WS_E + 131072;
constexpr unsigned WS_O = WS_H + 32 * 114688;
constexpr unsigned WS_END = WS_O + 32 * 65536;
constexpr unsigned WS_STAMP = WS_END;

constexpr unsigned L_X = 0, L_WK = 57344, L_WQ = 86016, L_B0 = 143360, L_G = 160768;
constexpr unsigned L_KD = 161024, L_XA = 161028, L_XF = 161060, L_QD = 161032, L_QF = 161036, L_UF = 161040, L_WF = 161044, L_KR = 161048, L_YF = 161052, L_XG = 161064, L_GV = 161068, L_QE = 161072, L_SF = 161076, L_QL = 161080, L_OD = 161056, L_FB = 161084;
constexpr unsigned L_QSH = 131072, L_HS = 114688;
constexpr unsigned L_C = 0, L_CIM = 57344, L_WO = 114688;
constexpr unsigned L_RO = 131072, L_LS = 155648;
constexpr unsigned L_RED = 0, L_DUMMY = 157696;
constexpr unsigned LDS_BYTES = 163840;
constexpr unsigned SPIN_CAP = 1u << 14;

DI unsigned cvt2(float a, float b) { f32x2 v = {a, b}; return __builtin_bit_cast(unsigned, __builtin_convertvector(v, bf16x2)); }
DI float bf2f(unsigned h) { return __uint_as_float(h << 16); }
DI float bfhi(unsigned h) { return __uint_as_float(h & 0xffff0000u); }
DI float bfround(float x) { return __uint_as_float(cvt2(x, 0.f) << 16); }
DI float fexp(float x) { return __builtin_amdgcn_exp2f(x * 1.4426950408889634f); }
DI rsrc_t mkrs(const void* p, unsigned bytes) { return __builtin_amdgcn_make_buffer_rsrc((void*)p, (short)0, (int)bytes, 0x00020000); }
DI void dma(rsrc_t rs, unsigned voff, unsigned soff, unsigned lds) {
    asm volatile("s_mov_b32 m0, %0\n\ts_nop 0\n\tbuffer_load_dwordx4 %1, %2, %3 offen lds" :: "s"(lds), "v"(voff), "s"(rs), "s"(soff) : "memory"); }
template <int N> DI void wait_bar() { asm volatile("s_waitcnt vmcnt(%0) lgkmcnt(0)\n\ts_barrier" :: "i"(N) : "memory"); }
template <int N> DI void wait_vm() { asm volatile("s_waitcnt vmcnt(%0)" :: "i"(N) : "memory"); }
DI void lds_bar() { asm volatile("s_waitcnt lgkmcnt(0)\n\ts_barrier" ::: "memory"); }
typedef LAS char* lp;
DI lp lbase(LAS char* L, unsigned off) { lp p = L + off; PIN(p); return p; }
DI bf16x8 ld128(lp p) { return __builtin_bit_cast(bf16x8, *(LAS const u32x4*)p); }
DI bf16x8 ldtr2(lp p0, lp p1) {
    s16x4 a = __builtin_amdgcn_ds_read_tr16_b64_v4i16((LAS s16x4*)p0), b = __builtin_amdgcn_ds_read_tr16_b64_v4i16((LAS s16x4*)p1);
    return __builtin_bit_cast(bf16x8, __builtin_shufflevector(a, b, 0, 1, 2, 3, 4, 5, 6, 7)); }
DI bf16x8 asbf(u32x4 v) { return __builtin_bit_cast(bf16x8, v); }
DI void st128(u32x4 v, rsrc_t rs, unsigned voff, unsigned soff, int aux) {
    if (aux) __builtin_amdgcn_raw_buffer_store_b128(v, rs, voff, soff, SC1); else __builtin_amdgcn_raw_buffer_store_b128(v, rs, voff, soff, 0);
    asm volatile("s_nop 1" :: "v"(v)); }
DI unsigned nosent(unsigned v) { return v < 0xFFFFFFFEu ? v : 0xFFFFFFFEu; }
DI unsigned atomic_read(rsrc_t rs, unsigned voff) { return (unsigned)__builtin_amdgcn_raw_ptr_buffer_atomic_add_i32(0, rs, (int)voff, 0, SC1); }
DI void atomic_inc(rsrc_t rs, unsigned voff) { asm volatile("buffer_atomic_add %0, %1, %2, 0 offen sc1" :: "v"(1u), "v"(voff), "s"(rs) : "memory"); }
struct cf { float r, i; };
DI cf cmul(cf a, cf b) { cf o;
    asm("v_mul_f32 %0, %3, %5\n\tv_mul_f32 %1, %3, %4\n\tv_fma_f32 %0, %2, %4, -%0\n\tv_fma_f32 %1, %2, %5, %1" : "=&v"(o.r), "=&v"(o.i) : "v"(a.r), "v"(a.i), "v"(b.r), "v"(b.i)); return o; }
DI cf cfma(cf a, cf b, cf c) { cf o;
    asm("v_fma_f32 %0, -%3, %5, %6\n\tv_fma_f32 %1, %3, %4, %7\n\tv_fma_f32 %0, %2, %4, %0\n\tv_fma_f32 %1, %2, %5, %1"
        : "=&v"(o.r), "=&v"(o.i) : "v"(a.r), "v"(a.i), "v"(b.r), "v"(b.i), "v"(c.r), "v"(c.i)); return o; }
DI void pk_cfma(f32x2& nr, f32x2& ni, f32x2 l, f32x2 hr, f32x2 hm, f32x2 a, f32x2 c) {
    asm("v_pk_fma_f32 %0, %2, %3, %5 op_sel_hi:[0,1,1]\n\tv_pk_fma_f32 %1, %2, %4, %6 op_sel_hi:[0,1,1]\n\t"
        "v_pk_fma_f32 %0, %2, %4, %0 op_sel:[1,0,0] neg_lo:[1,0,0] neg_hi:[1,0,0]\n\tv_pk_fma_f32 %1, %2, %3, %1 op_sel:[1,0,0]"
        : "=&v"(nr), "=&v"(ni) : "v"(l), "v"(hr), "v"(hm), "v"(a), "v"(c)); }
DI void pk_cmul(f32x2& nr, f32x2& ni, f32x2 l, f32x2 hr, f32x2 hm) {
    asm("v_pk_mul_f32 %0, %2, %3 op_sel_hi:[0,1]\n\tv_pk_mul_f32 %1, %2, %4 op_sel_hi:[0,1]\n\t"
        "v_pk_fma_f32 %0, %2, %4, %0 op_sel:[1,0,0] neg_lo:[1,0,0] neg_hi:[1,0,0]\n\tv_pk_fma_f32 %1, %2, %3, %1 op_sel:[1,0,0]"
        : "=&v"(nr), "=&v"(ni) : "v"(l), "v"(hr), "v"(hm)); }
DI f32x2 pk_scale(f32x2 s, f32x2 a) { f32x2 r; asm("v_pk_mul_f32 %0, %1, %2 op_sel_hi:[0,1]" : "=v"(r) : "v"(s), "v"(a)); return r; }
DI float sum4(float v) {
    auto a = __builtin_amdgcn_permlane16_swap(__float_as_uint(v), __float_as_uint(v), false, false); v = __uint_as_float(a[0]) + __uint_as_float(a[1]);
    auto c = __builtin_amdgcn_permlane32_swap(__float_as_uint(v), __float_as_uint(v), false, false); return __uint_as_float(c[0]) + __uint_as_float(c[1]); }
DI void bcast4(float v, float& r0, float& r1, float& r2, float& r3) {
    auto a = __builtin_amdgcn_permlane16_swap(__float_as_uint(v), __float_as_uint(v), false, false);
    auto e = __builtin_amdgcn_permlane32_swap(a[0], a[0], false, false); auto o = __builtin_amdgcn_permlane32_swap(a[1], a[1], false, false);
    r0 = __uint_as_float(e[0]); r2 = __uint_as_float(e[1]); r1 = __uint_as_float(o[0]); r3 = __uint_as_float(o[1]); }
DI float rowmax16(const f32x16& s) {
    float a = fmaxf(fmaxf(s[0], s[1]), s[2]), b = fmaxf(fmaxf(s[3], s[4]), s[5]);
    a = fmaxf(fmaxf(a, s[6]), s[7]); b = fmaxf(fmaxf(b, s[8]), s[9]); a = fmaxf(fmaxf(a, s[10]), s[11]); b = fmaxf(fmaxf(b, s[12]), s[13]);
    a = fmaxf(fmaxf(a, s[14]), s[15]); a = fmaxf(a, b);
    auto rr = __builtin_amdgcn_permlane32_swap(__float_as_uint(a), __float_as_uint(a), false, false);
    return fmaxf(__uint_as_float(rr[0]), __uint_as_float(rr[1])); }
DI void set_shift(u32x4& q1, float c, int hi) {
    const float c1 = bfround(c), c2 = bfround(c - c1), c3 = c - c1 - c2;
    if (hi) { q1[2] = cvt2(-c1, -c2); q1[3] = cvt2(-c3, 0.f); } }
DI void ladd(LAS char* L, unsigned off, unsigned v) {
    asm volatile("s_mov_b64 exec, 1\n\tds_add_u32 %0, %1\n\ts_mov_b64 exec, -1" :: "v"(L + off), "v"(v) : "memory"); }
DI void lcount(LAS char* L, unsigned off, int) { ladd(L, off, 1u); }
DI unsigned lread(LAS char* L, unsigned off) { return __builtin_amdgcn_readfirstlane(*(volatile LAS unsigned*)(L + off)); }
DI void lwait(LAS char* L, unsigned off, unsigned n) {
    if (__builtin_expect(lread(L, off) < n, 0)) { _Pragma("unroll 1") for (unsigned spin = 0; lread(L, off) < n && spin < (1u << 20); ++spin) { if (LSLEEP) __builtin_amdgcn_s_sleep(LSLEEP); } }
    asm volatile("" ::: "memory"); }
DI void lwait_idle(LAS char* L, unsigned off, unsigned n) {
    _Pragma("unroll 1") for (unsigned spin = 0; lread(L, off) < n && spin < (1u << 18); ++spin) __builtin_amdgcn_s_sleep(2);
    asm volatile("" ::: "memory"); }
DI int xswz(int row) { return (row >> 1) & 7; }
DI u32x4 pack8(const f32x16& a, int e) { return (u32x4){cvt2(a[e], a[e + 1]), cvt2(a[e + 2], a[e + 3]), cvt2(a[e + 4], a[e + 5]), cvt2(a[e + 6], a[e + 7])}; }
#define PAIR(v, p) (f32x2){v[2 * (p)], v[2 * (p) + 1]}

#define STAMP(k)
#define STAMPV(k)

__global__ void __launch_bounds__(512, 2)
gated_mix_kernel(const uint16_t* x, const uint16_t* wq, const uint16_t* wk, const uint16_t* wv, const uint16_t* wo, const uint16_t* lre, const uint16_t* lim,
                 const uint16_t* bre, const uint16_t* bim, const uint16_t* cre, const uint16_t* cim, const uint16_t* logdt, const uint16_t* dskip,
                 const uint16_t* gate, float* out, char* ws) {
    __shared__ __attribute__((aligned(16))) char smem[LDS_BYTES];
    LAS char* L = (LAS char*)smem;
    const unsigned l0 = (unsigned)(uintptr_t)L;
    const int lane = threadIdx.x & 63, w = __builtin_amdgcn_readfirstlane(threadIdx.x >> 6);
    const int b = blockIdx.x;
#define EXIT() return
    STAMP(0);
    {   unsigned long long pc; asm volatile("s_getpc_b64 %0" : "=s"(pc));
        const rsrc_t rsC = mkrs((const void*)(pc & ~255ull), CPREF);
        asm volatile("s_mov_b32 m0, %0\n\ts_nop 4\n\tbuffer_load_dword %1, %2, 0 offen lds" :: "s"(l0 + L_DUMMY), "v"(threadIdx.x * 64), "s"(rsC) : "memory"); }
    const int blk = (b & 7) * 4 + (b >> 6), j = (b >> 3) & 7;
    const int th = w & 1, hd = (w >> 1) & 1;
    const int r32 = lane & 31, hi = lane >> 5, r16 = lane & 15, G = lane >> 4;
    const int tq = r16 >> 2, tp = lane & 3;
    const int sl = lane & 7, r8 = lane >> 3;
    const unsigned l16 = lane * 16;
    const int kind = j & 1, tg = j >> 1;
    unsigned vx = r8 * ROWB + ((sl ^ (r8 >> 1)) << 4); PIN(vx);
    PINS(x); PINS(wq); PINS(wk); PINS(wv); PINS(wo); PINS(lre); PINS(lim); PINS(bre); PINS(bim); PINS(cre); PINS(cim); PINS(logdt); PINS(dskip); PINS(gate); PINS(out); PINS(ws);
    const rsrc_t rsX = mkrs(x, SEQ * ROWB), rsWS = mkrs(ws, WS_END);
    const unsigned opiece = WS_O + blk * 65536 + (th * 16 + 2 * j + hd) * 2048;

    const rsrc_t rsWQ = mkrs(wq, DM * ROWB);
    const int pwq = sl ^ (((r8 >> 1) & 1) << 2);
    const unsigned vw = r8 * ROWB + 112 * j + (pwq << 4);
#define WQD(i0, i1) do { if (pwq != 7) { UNROLL for (int i = (i0); i < (i1); ++i) { const int p = i < 18 ? 3 * i + w - 1 : min(53 + w, 55); dma(rsWQ, vw, p * 8 * ROWB, l0 + L_WQ + p * 1024); } } } while (0)
#define BUMP() do { if (b == 0) atomic_inc(rsWS, lane < 16 ? WS_X + lane * 128 : OOB); } while (0)
    if (w < 4) {
        const int qrow = 32 * th + r32;
        const unsigned rob = L_RO + w * 2048 + l16, lsb = L_LS + (w * 32 + r32) * 16;
        lp a00, a01, a02, a03, a1, ro0, ro1, ro2, rop, lsp, lw0, lw1, lw2, lw3, qs0, qs1, qs2, qs3;
        unsigned kco, kc = 0u;
        u32x4 sel0, sel1, pw0, pw1, kf[4][2], vf[4][2];
        f32x16 o[4];
#define QPREP() do { const unsigned a0o = L_X + qrow * 128 + ((hi ^ xswz(qrow)) << 4); \
        a00 = lbase(L, a0o); a01 = lbase(L, a0o ^ 32); a02 = lbase(L, a0o ^ 64); a03 = lbase(L, a0o ^ 96); \
        a1 = lbase(L, L_WQ + (8 * hi + tq) * 128 + ((56 * hd + 32 * (G & 1) + 8 * tp) ^ (((tq >> 1) & 1) << 6))); \
        kco = lane < 16 ? WS_KVCNT + (b & 7) * 2048 + lane * 128 : OOB; PIN(kco); \
        ro0 = lbase(L, rob ^ 2048); ro1 = lbase(L, (rob ^ 4096) + 8192); ro2 = lbase(L, (rob ^ 6144) + 16384); rop = lbase(L, rob); lsp = lbase(L, lsb); \
        lw0 = lbase(L, hi ? (lsb ^ 512) + w * 4 : L_DUMMY); lw1 = lbase(L, hi ? (lsb ^ 1024) + w * 4 : L_DUMMY); lw2 = lbase(L, hi ? (lsb ^ 1536) + w * 4 : L_DUMMY); lw3 = lbase(L, hi ? lsb + w * 4 : L_DUMMY); \
        qs0 = lbase(L, L_QSH + (w ^ 1) * 2048 + l16); qs1 = lbase(L, L_QSH + (w ^ 2) * 2048 + l16); qs2 = lbase(L, L_QSH + (w ^ 3) * 2048 + l16); qs3 = lbase(L, L_QSH + w * 2048 + l16); \
        const int js = 4 * ((r32 >> 3) & 1) + (r32 & 3); const unsigned one = ((r32 >> 2) & 1) == hi ? 0x3F80u << (16 * (js & 1)) : 0u; \
        UNROLL for (int e = 0; e < 4; ++e) { const unsigned v = (js >> 1) == e ? one : 0u; sel0[e] = r32 < 16 ? v : 0u; sel1[e] = r32 < 16 ? 0u : v; } PIN(sel0); PIN(sel1); \
        o[0] = o[1] = o[2] = o[3] = (f32x16){}; PIN(o[0]); PIN(o[1]); PIN(o[2]); PIN(o[3]); \
        pw1 = (u32x4){0u, 0u, 0u, 0u}; vf[3][1] = pw1; PIN(pw1); PIN(vf[3][1]); } while (0)
        __builtin_amdgcn_s_setprio(2);
        if (w == 0) {
            const unsigned xoff = lane == 0 ? WS_X + (b & 15) * 128 : OOB;
            const rsrc_t rsWK = mkrs(kind ? wv : wk, DM * 56);
            const unsigned vxa = vx ^ (xswz(16 * tg) << 4), vxb = (vx ^ (xswz(16 * tg + 8) << 4)) + 8 * ROWB, xs = (blk * 64 + tg * 16) * ROWB;
            unsigned x0 = 0u, x1 = 0u;
            asm volatile("buffer_atomic_add %0, %1, %2, 0 offen sc0 sc1" : "+v"(x0) : "v"(xoff), "s"(rsWS) : "memory");
            STAMP(36);
            UNROLL for (int c = 0; c < 7; ++c) {
                dma(rsX, vxa, xs + c * 128, l0 + L_X + c * 8192 + tg * 2048); dma(rsX, vxb, xs + c * 128, l0 + L_X + c * 8192 + tg * 2048 + 1024);
                UNROLL for (int n = (7 * c + 1) / 2; n < (7 * c + 8) / 2; ++n) dma(rsWK, l16, n * 1024, l0 + L_WK + n * 1024);
            }
            asm volatile("s_barrier" ::: "memory");
            STAMP(14);
            __builtin_amdgcn_raw_buffer_store_b64((u32x2){~0u, ~0u}, rsWS, lane < 56 ? lane * 8 : OOB, WS_E + (blk * DM + 56 * j) * 8, SC1);
            const int cg0 = kind ? tp : 4 * (tp >> 1) + (tp & 1), cg1 = kind ? 4 + tp : 4 * (tp >> 1) + 2 + (tp & 1);
            const lp aw0 = lbase(L, L_WK + (8 * G + tq) * 56 + cg0 * 8), aw1 = lbase(L, L_WK + (8 * G + tq) * 56 + cg1 * 8);
            const unsigned axo = L_X + (16 * tg + r16) * 128 + ((G ^ xswz(16 * tg + r16)) << 4);
            const lp ax0 = lbase(L, axo), ax1 = lbase(L, axo ^ 64);
            const int sg = tg & 1;
            const unsigned tb = WS_KV + (2 * blk + (tg >> 1)) * 4096 + (kind ? 2048 + sg * 1024 : 0);
            unsigned kva = kind ? ((G >> 1) * 32 + r16) * 16 + (G & 1) * 8 : (G * 32 + 16 * sg + ((r16 & 3) | ((r16 & 4) << 1) | ((r16 & 8) >> 1))) * 16; PIN(kva);
            unsigned kvb = kind ? kva + 256 : kva + 8; PIN(kvb);
            const bool spare = kind ? r16 >= 12 : G == 3;
            unsigned sp2 = (kind && r16 != 12) ? 0u : 0x3F803F80u, sp3 = kind ? sp2 : 0x00003F80u; PIN(sp2); PIN(sp3);
            f32x4 d0 = {0.f, 0.f, 0.f, 0.f}, d1 = d0;
            bf16x8 xf[14], w0[14], w1[14];
#define KVR(c) do { wait_vm<1 + 39 - 2 * ((c) + 1) - (7 * (c) + 8) / 2>(); UNROLL for (int ks = 2 * (c); ks < 2 * (c) + 2; ++ks) { xf[ks] = ld128(((ks & 1) ? ax1 : ax0) + (c) * 8192); \
                        w0[ks] = ldtr2(aw0 + ks * 1792, aw0 + ks * 1792 + 224); w1[ks] = ldtr2(aw1 + ks * 1792, aw1 + ks * 1792 + 224); } SBAR(); } while (0)
#define KVM(c, K) do { UNROLL for (int ks = 2 * (c); ks < 2 * (c) + 2; ++ks) { \
                        if (K) { d0 = MFMA16(xf[ks], w0[ks], d0); d1 = MFMA16(xf[ks], w1[ks], d1); } else { d0 = MFMA16(w0[ks], xf[ks], d0); d1 = MFMA16(w1[ks], xf[ks], d1); } } SBAR(); } while (0)
#define KVP(K) do { KVR(0); STAMP(1); KVR(1); KVM(0, K); KVR(2); KVM(1, K); KVR(3); KVM(2, K); KVR(4); KVM(3, K); KVR(5); KVM(4, K); KVR(6); STAMP(32); KVM(5, K); KVM(6, K); } while (0)
            if (kind) KVP(1); else KVP(0);
            __builtin_amdgcn_raw_buffer_store_b64((u32x2){cvt2(d0[0], d0[1]), cvt2(d0[2], d0[3])}, rsWS, kva, tb, SC1);
            __builtin_amdgcn_raw_buffer_store_b64((u32x2){spare ? sp2 : cvt2(d1[0], d1[1]), spare ? sp3 : cvt2(d1[2], d1[3])}, rsWS, kvb, tb, SC1);
            asm volatile("buffer_atomic_add %0, %1, %2, 0 offen sc0 sc1" : "+v"(x1) : "v"(xoff), "s"(rsWS) : "memory");
            ladd(L, L_KD, 1u); ladd(L, L_XA, 1u); ladd(L, L_XF, 1u);
            STAMP(12);
            QPREP();
            asm volatile("s_waitcnt vmcnt(0)" : "+v"(x0), "+v"(x1) :: "memory");
            STAMP(35);
            const unsigned s0 = __builtin_amdgcn_readfirstlane(x0); unsigned s1 = __builtin_amdgcn_readfirstlane(x1);
            _Pragma("unroll 1") for (unsigned spin = 0; s1 == s0 && spin < SPIN_CAP; ++spin) s1 = __builtin_amdgcn_readfirstlane(atomic_read(rsWS, xoff));
            atomic_inc(rsWS, lane < 8 ? WS_KVCNT + lane * 2048 + (b & 15) * 128 : OOB);
            BUMP();
            STAMP(13);
        } else {
            unsigned g0, g1, g2;
            const int gk = min((w - 1) * 192, 256) + lane;
            const uint16_t* gp = gate + gk; PIN(gp);
            const int ga = 2 * (w - 1) + (w - 1 >= tg ? 2 : 0);
            unsigned vxa = vx ^ (xswz(8 * ga) << 4), vxb = (vx ^ (xswz(8 * ga + 8) << 4)) + 8 * ROWB; const unsigned xs = (blk * 64 + 8 * ga) * ROWB; PIN(vxa); PIN(vxb);
            if (w == 1 && lane < 16) *(LAS unsigned*)(L + L_KD + lane * 4) = 0u;
            lds_bar();
            UNROLL for (int c = 0; c < 7; ++c) { dma(rsX, vxa, xs + c * 128, l0 + L_X + c * 8192 + ga * 1024); dma(rsX, vxb, xs + c * 128, l0 + L_X + c * 8192 + ga * 1024 + 1024); }
            asm volatile("global_load_ushort %0, %3, off\n\tglobal_load_ushort %1, %3, off offset:128\n\tglobal_load_ushort %2, %3, off offset:256"
                         : "=&v"(g0), "=&v"(g1), "=&v"(g2) : "v"(gp) : "memory");
            WQD(0, 6);
            QPREP();
            wait_vm<9>();
            lcount(L, L_XA, lane);
            WQD(6, 19);
            STAMP(14);
            asm volatile("s_waitcnt vmcnt(9)" : "+v"(g0), "+v"(g1), "+v"(g2) :: "memory");
            const lp gl = L + (L_WQ + gk * 128 + (112 ^ (((gk >> 1) & 1) << 6)));
            *(LAS uint16_t*)(gl) = (uint16_t)g0; *(LAS uint16_t*)(gl + 8192) = (uint16_t)g1; *(LAS uint16_t*)(gl + 16384) = (uint16_t)g2;
            lcount(L, L_XF, lane);
            STAMP(1);
        }
        lwait(L, L_XF, 4u);
        STAMP(2);
        f32x16 qa = {};
        { bf16x8 wf[28], xq[28];
          UNROLL for (int ks = 0; ks < 28 + QD_; ++ks) {
            if (ks == WQ2) { STAMP(44); wait_vm<0>(); lcount(L, L_XG, lane); STAMP(45); }
            if (ks == 15) { lwait(L, L_XG, 4u); STAMP(46); }
            if (ks < 28) { wf[ks] = ldtr2(a1 + ks * 2048, a1 + ks * 2048 + 512); xq[ks] = ld128(((ks & 3) == 0 ? a00 : (ks & 3) == 1 ? a01 : (ks & 3) == 2 ? a02 : a03) + (ks >> 2) * 8192); }
            if (ks >= QD_) qa = MFMA32(wf[ks - QD_], xq[ks - QD_], qa);
            if (ks == KPOLL && w == 0) asm volatile("buffer_atomic_add %0, %1, %2, 0 offen sc0 sc1" : "+v"(kc) : "v"(kco), "s"(rsWS) : "memory");
            if (ks == KCHK && w == 0) {
                asm volatile("s_waitcnt vmcnt(0)" : "+v"(kc) :: "memory");
                if (__builtin_expect(__builtin_amdgcn_ballot_w64(kc != 16 && lane < 16) != 0, 0)) { _Pragma("unroll 1") for (unsigned spin = 0; spin < SPIN_CAP; ++spin) {
                    BUMP(); if (__builtin_amdgcn_ballot_w64(atomic_read(rsWS, kco) != 16 && lane < 16) == 0) break; } }
                ladd(L, L_KR, 1u); }
            if (ks == 28) ladd(L, L_QD, 1u);
            SBAR(); } }
        const unsigned kv0 = WS_KV + w * 65536;
        const char* kvp = ws + kv0;
#define LD1(A0, off) asm volatile("global_load_dwordx4 %0, %1, %2 offset:%3 sc1" : "=&v"(A0) : "v"(l16), "s"(kvp), "i"(off) : "memory")
#define LD2(A0, A1, off) do { LD1(A0, off); LD1(A1, (off) + 1024); } while (0)
#define WAIT2(N, A, B) asm volatile("s_waitcnt vmcnt(%2)" : "+v"(A), "+v"(B) : "i"(N) : "memory")
        STAMP(47);
        lwait(L, L_KR, 1u);
        LD2(kf[0][0], kf[0][1], 0); LD2(vf[0][0], vf[0][1], 2048);
        STAMP(48);
        lwait(L, L_QD, 4u);
        asm volatile("s_nop 11" : "+v"(qa));
        const f32x2 cs = {0.18898223650461361f * 1.4426950408889634f, 0.f};
        u32x4 q0[4], q1[4];
        UNROLL for (int e = 0; e < 4; ++e) { const f32x2 a = pk_scale(cs, (f32x2){qa[2 * e], qa[2 * e + 1]}), c = pk_scale(cs, (f32x2){qa[8 + 2 * e], qa[9 + 2 * e]});
            q0[3][e] = cvt2(a[0], a[1]); q1[3][e] = cvt2(c[0], c[1]); }
        if (hi) { q1[3][2] = 0u; q1[3][3] = 0u; }
        *(LAS u32x4*)(qs3) = q0[3]; *(LAS u32x4*)(qs3 + 1024) = q1[3];
        if (hd == 1 && hi) *(LAS float*)(L + L_G + qrow * 4) = __builtin_amdgcn_rcpf(1.0f + fexp(-qa[12]));
        STAMP(3);
        lcount(L, L_QF, lane);
        UNROLL for (int n = 1; n < 3; ++n) { kvp += 4096; LD2(kf[n][0], kf[n][1], 0); LD2(vf[n][0], vf[n][1], 2048); }
        STAMP(4);

        f32x16 sA, sB;
        lwait(L, L_QF, 4u);
        STAMP(5);
        q0[0] = *(LAS const u32x4*)(qs0); q1[0] = *(LAS const u32x4*)(qs0 + 1024); q0[1] = *(LAS const u32x4*)(qs1); q1[1] = *(LAS const u32x4*)(qs1 + 1024);
        q0[2] = *(LAS const u32x4*)(qs2); q1[2] = *(LAS const u32x4*)(qs2 + 1024);
        const float gt = *(LAS const float*)(L + L_G + qrow * 4);
        ladd(L, L_SF, 1u);
#define GIVE(O, RP, LP) do { *(LAS u32x4*)(RP) = pack8(O, 0); *(LAS u32x4*)((RP) + 1024) = pack8(O, 8); *(LAS float*)(LP) = O[12]; } while (0)
        WAIT2(10, kf[0][0], kf[0][1]);
        __builtin_amdgcn_s_setprio(0);
        STAMP(6);
        sA = MFMA32(asbf(kf[0][0]), asbf(q0[0]), (f32x16){}); sA = MFMA32(asbf(kf[0][1]), asbf(q1[0]), sA);
#define EXP(A, i) A[i] = __builtin_amdgcn_exp2f(A[i])
#define EX4(A, i) do { EXP(A, i); EXP(A, i + 1); EXP(A, i + 2); EXP(A, i + 3); PIN(A); } while (0)
#define UNIT(A, B, KN0, KN1, qn, q, qp, V0, VP1, NEXT, H0, H1, H2, H3) do { SBAR(); H0; \
        if (NEXT) { B = MFMA32(asbf(KN0), asbf(q0[qn]), (f32x16){}); PIN(B); } SBAR(); \
        EX4(A, 0); SBAR(); \
        o[qp] = MFMA32(asbf(VP1), asbf(pw1), o[qp]); PIN(o[qp]); SBAR(); H1; \
        pw0[0] = cvt2(A[0], A[1]); pw0[1] = cvt2(A[2], A[3]); PIN(pw0); SBAR(); EX4(A, 4); SBAR(); \
        if (NEXT) { B = MFMA32(asbf(KN1), asbf(q1[qn]), B); PIN(B); } SBAR(); H2; SBAR(); \
        pw0[2] = cvt2(A[4], A[5]); pw0[3] = cvt2(A[6], A[7]); PIN(pw0); SBAR(); EX4(A, 8); SBAR(); H3; \
        o[q] = MFMA32(asbf(V0), asbf(pw0), o[q]); PIN(o[q]); SBAR(); \
        { float e12 = __builtin_amdgcn_exp2f(A[12]), e13 = __builtin_amdgcn_exp2f(A[13]), e14 = __builtin_amdgcn_exp2f(A[14]), e15 = __builtin_amdgcn_exp2f(A[15]); \
          asm volatile("" : "+v"(e12), "+v"(e13), "+v"(e14), "+v"(e15)); SBAR(); \
          pw1[0] = cvt2(A[8], A[9]); pw1[1] = cvt2(A[10], A[11]); SBAR(); pw1[2] = cvt2(e12, e13); pw1[3] = cvt2(e14, e15); PIN(pw1); } SBAR(); } while (0)
#define TRIP(c, n, p, LD, NV, NK) do { \
        UNIT(sA, sB, kf[c][0], kf[c][1], 1, 0, 3, vf[c][0], vf[p][1], true, WAIT2(NV, vf[c][0], vf[c][1]), { if (LD) { kvp += 4096; LD1(kf[p][0], 0); } }, { if (LD) LD1(kf[p][1], 1024); }, {}); \
        UNIT(sB, sA, kf[c][0], kf[c][1], 2, 1, 0, vf[c][0], vf[c][1], true, {}, { if (LD) LD1(vf[p][0], 2048); }, { if (LD) LD1(vf[p][1], 3072); }, {}); \
        UNIT(sA, sB, kf[c][0], kf[c][1], 3, 2, 1, vf[c][0], vf[c][1], true, {}, {}, WAIT2(NK, kf[n][0], kf[n][1]), {}); \
        UNIT(sB, sA, kf[n][0], kf[n][1], 0, 3, 2, vf[c][0], vf[c][1], true, {}, {}, {}, {}); } while (0)
        _Pragma("unroll 1") for (int it = 0; it < 3; ++it) {
            TRIP(0, 1, 3, true, 8, 10); TRIP(1, 2, 0, true, 8, 10); TRIP(2, 3, 1, true, 8, 10); TRIP(3, 0, 2, true, 8, 10);
            STAMPV(17 + it);
        }
        TRIP(0, 1, 3, true, 8, 10);
        ladd(L, L_QL, 1u);
        TRIP(1, 2, 0, false, 8, 6); TRIP(2, 3, 1, false, 4, 2);
        ladd(L, L_QE, 1u);
        lwait(L, L_UF, 8u);
        UNIT(sA, sB, kf[3][0], kf[3][1], 1, 0, 3, vf[3][0], vf[2][1], true, WAIT2(0, vf[3][0], vf[3][1]), {}, {}, {});
        UNIT(sB, sA, kf[3][0], kf[3][1], 2, 1, 0, vf[3][0], vf[3][1], true, {}, {}, {}, {});
        UNIT(sA, sB, kf[3][0], kf[3][1], 3, 2, 1, vf[3][0], vf[3][1], true, {}, GIVE(o[0], ro0, lw0), {}, {});
        UNIT(sB, sA, kf[0][0], kf[0][1], 0, 3, 2, vf[3][0], vf[3][1], false, {}, GIVE(o[1], ro1, lw1), {}, {});
        STAMPV(20);
        o[3] = MFMA32(asbf(vf[3][1]), asbf(pw1), o[3]); PIN(o[3]); SBAR();
        GIVE(o[2], ro2, lw2); SBAR();
        STAMP(7);

        float l3 = o[3][12];
        *(LAS float*)(lw3) = l3;
        {   const unsigned u0 = __float_as_uint(o[0][12]), u1 = __float_as_uint(o[1][12]), u2 = __float_as_uint(o[2][12]), u3 = __float_as_uint(l3);
            const unsigned lo = min(min(u0, u1), min(u2, u3)), up = max(max(u0, u1), max(u2, u3));
            const bool bad = __builtin_amdgcn_ballot_w64(hi && (lo < 0x1F800000u || up > 0x5D800000u)) != 0;
            ladd(L, L_GV, bad ? 0x10001u : 1u); }
        unsigned gv = lread(L, L_GV);
        if (__builtin_expect((gv & 0xffffu) < 4u, 0)) { _Pragma("unroll 1") for (unsigned spin = 0; (gv & 0xffffu) < 4u && spin < (1u << 20); ++spin) gv = lread(L, L_GV); }
        asm volatile("" ::: "memory");
        STAMP(15);
        bf16x8 f[6]; f32x4 m0;
        UNROLL for (int t = 0; t < 6; ++t) f[t] = ld128(rop + (t >> 1) * 8192 + (t & 1) * 1024);
        m0 = *(LAS const f32x4*)(lsp);
        SBAR();
        if (__builtin_expect(gv > 0xffffu, 0)) {
            auto qbar = [&](unsigned n) __attribute__((always_inline)) { asm volatile("s_waitcnt lgkmcnt(0)" ::: "memory"); ladd(L, L_FB, 1u); lwait(L, L_FB, n); };
            float mx[4] = {-INFINITY, -INFINITY, -INFINITY, -INFINITY};
            qbar(4u);
            _Pragma("unroll 1") for (int t = 0; t < 16; ++t) {
                const bf16x8 a = asbf(__builtin_amdgcn_raw_buffer_load_b128(rsWS, l16, kv0 + t * 4096, SC1));
                const bf16x8 c = asbf(__builtin_amdgcn_raw_buffer_load_b128(rsWS, l16, kv0 + t * 4096 + 1024, SC1));
                UNROLL for (int q = 0; q < 4; ++q) { f32x16 s = MFMA32(a, asbf(q0[q]), (f32x16){}); s = MFMA32(c, asbf(q1[q]), s); mx[q] = fmaxf(mx[q], rowmax16(s)); SBAR(); }
            }
            *(LAS float*)(lw0) = mx[0]; *(LAS float*)(lw1) = mx[1]; *(LAS float*)(lw2) = mx[2]; *(LAS float*)(lw3) = mx[3];
            qbar(8u);
            UNROLL for (int q = 0; q < 4; ++q) { const f32x4 n0 = *(LAS const f32x4*)(L + (lsb ^ (((q + 1) & 3) * 512)));
                set_shift(q1[q], fmaxf(fmaxf(n0[0], n0[1]), fmaxf(n0[2], n0[3])), hi); }
            qbar(12u);
            UNROLL for (int q = 0; q < 4; ++q) {
                f32x16 acc = {};
                _Pragma("unroll 1") for (int t = 0; t < 16; ++t) {
                    const bf16x8 a = asbf(__builtin_amdgcn_raw_buffer_load_b128(rsWS, l16, kv0 + t * 4096, SC1));
                    const bf16x8 c = asbf(__builtin_amdgcn_raw_buffer_load_b128(rsWS, l16, kv0 + t * 4096 + 1024, SC1));
                    const bf16x8 d = asbf(__builtin_amdgcn_raw_buffer_load_b128(rsWS, l16, kv0 + t * 4096 + 2048, SC1));
                    const bf16x8 e = asbf(__builtin_amdgcn_raw_buffer_load_b128(rsWS, l16, kv0 + t * 4096 + 3072, SC1));
                    f32x16 s = MFMA32(a, asbf(q0[q]), (f32x16){}); s = MFMA32(c, asbf(q1[q]), s);
                    UNROLL for (int r = 0; r < 16; ++r) s[r] = __builtin_amdgcn_exp2f(s[r]);
                    acc = MFMA32(d, asbf(pack8(s, 0)), acc); acc = MFMA32(e, asbf(pack8(s, 8)), acc);
                }
                if (q == 0) { GIVE(acc, ro0, lw0); } else if (q == 1) { GIVE(acc, ro1, lw1); } else if (q == 2) { GIVE(acc, ro2, lw2); } else { *(LAS float*)(lw3) = acc[12]; o[3] = acc; }
            }
            qbar(16u);
            UNROLL for (int t = 0; t < 6; ++t) f[t] = ld128(rop + (t >> 1) * 8192 + (t & 1) * 1024);
            m0 = *(LAS const f32x4*)(lsp);
        }
        __builtin_amdgcn_s_setprio(2);
        f32x16 t = o[3];
        UNROLL for (int k = 0; k < 6; ++k) t = MFMA32(asbf((k & 1) ? sel1 : sel0), f[k], t);
        {
            float s0 = m0[0] + m0[1], s2 = m0[2] + m0[3]; PIN(s0); PIN(s2);
            f32x2 sc; sc[0] = sc[1] = gt * __builtin_amdgcn_rcpf(s0 + s2); PIN(sc);
            u32x4 oa, ob;
            UNROLL for (int e = 0; e < 4; ++e) { const f32x2 a = sc * (f32x2){t[2 * e], t[2 * e + 1]}; oa[e] = nosent(cvt2(a[0], a[1])); }
            st128(oa, rsWS, l16, opiece, SC1);
            SBAR();
            UNROLL for (int e = 0; e < 4; ++e) { const f32x2 a = sc * (f32x2){t[8 + 2 * e], t[9 + 2 * e]}; ob[e] = nosent(cvt2(a[0], a[1])); }
            if (hi) { ob[2] = 0u; ob[3] = 0u; }
            st128(ob, rsWS, l16, opiece + 1024, SC1);
        }
        __builtin_amdgcn_s_setprio(0);
        STAMP(8);
        if (j == 7) EXIT();
        ladd(L, L_OD, 1u);
        const rsrc_t rsD = mkrs(dskip, ROWB), rsO = mkrs(out, SEQ * DM * 4);
        const int gm = r16, row = 16 * w + G, col = 64 * j + 4 * gm;
        f32x4 r[4]; u32x2 xv[4]; float gv4[4];
        const u32x2 dv = __builtin_amdgcn_raw_buffer_load_b64(rsD, col * 2, 0, 0);
        UNROLL for (int e = 0; e < 4; ++e) { xv[e] = __builtin_amdgcn_raw_buffer_load_b64(rsX, (blk * 64 + row) * ROWB + col * 2, e * 4 * ROWB, 0);
                                             gv4[e] = *(LAS const float*)(L + L_G + (row + 4 * e) * 4); }
        lp rp[4];
        UNROLL for (int e = 0; e < 4; ++e) rp[e] = lbase(L, L_RED + (gm >> 3) * 32768 + (w >> 1) * 16384 + ((gm & 7) >> 1) * 1024 + (gm & 1) * 512 + ((((row + 4 * e) & 31) ^ gm) << 4));
        unsigned oo = ((blk * 64 + row) * DM + col) * 4; PIN(oo);
        SBAR();
        UNROLL for (int e = 0; e < 4; ++e) { const float g1 = 1.0f - gv4[e];
            float t0 = bf2f(xv[e][0]) * bf2f(dv[0]), t1 = bfhi(xv[e][0]) * bfhi(dv[0]), t2 = bf2f(xv[e][1]) * bf2f(dv[1]), t3 = bfhi(xv[e][1]) * bfhi(dv[1]);
            PIN(t0); PIN(t1); PIN(t2); PIN(t3); t0 *= g1; t1 *= g1; t2 *= g1; t3 *= g1; PIN(t0); PIN(t1); PIN(t2); PIN(t3);
            r[e] = (f32x4){t0, t1, t2, t3}; }
        lds_bar();
        STAMP(10);
        UNROLL for (int e = 0; e < 4; ++e) {
            r[e] += (*(LAS const f32x4*)(rp[e]) + *(LAS const f32x4*)(rp[e] + 4096)) + (*(LAS const f32x4*)(rp[e] + 8192) + *(LAS const f32x4*)(rp[e] + 12288));
            __builtin_amdgcn_raw_buffer_store_b128(__builtin_bit_cast(u32x4, r[e]), rsO, oo, e * 4 * DM * 4, OAUX); asm volatile("s_nop 1" :: "v"(r[e])); }
    } else {
        const int u = w - 4, st = min(16 * u + r16, 55);
        const unsigned eoff = (G == 0 && 16 * u + r16 < 56) ? st * 8 : OOB;
        const rsrc_t rsBR = mkrs(bre, DM * ROWB), rsBI = mkrs(bim, DM * ROWB);
        const unsigned vb0_ = (56 * j + 16 * u + r8) * ROWB + ((sl ^ (r8 >> 1)) << 4), vb1_ = u < 3 ? (vb0_ ^ 64) + 8 * ROWB : OOB;
#define DST2(a) (u < 3 ? (a) : l0 + L_DUMMY)
        unsigned p0, p1, p2;
        const uint16_t *a0 = lre + 56 * j + st, *a1 = lim + 56 * j + st, *a2 = logdt + 56 * j + st; PIN(a0); PIN(a1); PIN(a2);
#define TOUCH(ptr, bytes, vo) do { const rsrc_t r_ = mkrs(ptr, bytes); \
            asm volatile("s_mov_b32 m0, %0\n\ts_nop 0\n\tbuffer_load_dword %1, %2, 0 offen lds" :: "s"(l0 + L_DUMMY), "v"(vo), "s"(r_) : "memory"); } while (0)
        if (PFE == 1) { if (u < 3) TOUCH(u == 0 ? wq : u == 1 ? bre : bim, DM * ROWB, lane < 25 ? (b * 25 + lane) * 64 : OOB); else TOUCH(x, SEQ * ROWB, lane < 56 ? (blk * 448 + j * 56 + lane) * 128 : OOB); }
        const bool master = b == 0 && w == 7;
        if (master) {
            __builtin_amdgcn_raw_buffer_store_b32(0u, rsWS, lane * 128, 0, SC1);
            __builtin_amdgcn_raw_buffer_store_b32(0u, rsWS, lane * 128 + 8192, 0, SC1);
            __builtin_amdgcn_raw_buffer_store_b32(0u, rsWS, lane < 32 ? lane * 128 + 16384 : OOB, 0, SC1);
        }
        asm volatile("s_barrier" ::: "memory");
        if (master) { wait_vm<0>(); BUMP(); }
        if (PFE == 2 && u < 3) {
            const int r = b >> 3;
            TOUCH(u == 0 ? wq : u == 1 ? bre : bim, DM * ROWB, lane < 49 ? (r * 98 + lane) * 128 : OOB);
            TOUCH(u == 0 ? wq : u == 1 ? bre : bim, DM * ROWB, lane < 49 ? (r * 98 + 49 + lane) * 128 : OOB);
        }
        if (PFE == 0) { if (u < 3) TOUCH(u == 0 ? wq : u == 1 ? bre : bim, DM * ROWB, lane < 25 ? (b * 25 + lane) * 64 : OOB); }
        asm volatile("global_load_ushort %0, %3, off\n\tglobal_load_ushort %1, %4, off\n\tglobal_load_ushort %2, %5, off"
                     : "=&v"(p0), "=&v"(p1), "=&v"(p2) : "v"(a0), "v"(a1), "v"(a2) : "memory");
#define BOFF(c) ((c) % 3 == 0 ? L_B0 : L_WK + ((c) % 3 - 1) * 14336)
#define BD(c) do { dma(rsBR, vb0_, (c) * 128, l0 + BOFF(c) + u * 2048); dma(rsBI, vb0_, (c) * 128, l0 + BOFF(c) + 7168 + u * 2048); \
                   dma(rsBR, vb1_, (c) * 128, DST2(l0 + BOFF(c) + u * 2048 + 1024)); dma(rsBI, vb1_, (c) * 128, DST2(l0 + BOFF(c) + 7168 + u * 2048 + 1024)); } while (0)
        STAMP(14);
        asm volatile("s_waitcnt vmcnt(0)" : "+v"(p0), "+v"(p1), "+v"(p2) :: "memory");
        if (master) BUMP();
        f32x2 lam, coef; cf lam8, lam16, lam64, wlo, whi, zG;
        {   const float lr = bf2f(p0), li = bf2f(p1), dt = bfround(fexp(bf2f(p2)));
            const float er = fexp(dt * lr), rev = __builtin_amdgcn_fractf(dt * li * 0.15915494309189535f);
            const cf l1 = {er * __builtin_amdgcn_cosf(rev), er * __builtin_amdgcn_sinf(rev)};
            const float inv = __builtin_amdgcn_rcpf(lr * lr + li * li);
            const cf cq = cmul((cf){l1.r - 1.0f, l1.i}, (cf){lr * inv, -li * inv});
            lam = (f32x2){l1.r, l1.i}; coef = (f32x2){cq.r, cq.i};
            const cf l2 = cmul(l1, l1), l4 = cmul(l2, l2); lam8 = cmul(l4, l4); lam16 = cmul(lam8, lam8);
            const cf l32 = cmul(lam16, lam16); lam64 = cmul(l32, l32);
            const cf l128 = cmul(lam64, lam64), l256 = cmul(l128, l128), l512 = cmul(l256, l256), l1024 = cmul(l512, l512), one = {1.f, 0.f};
            wlo = cmul((G & 1) ? lam16 : one, (G & 2) ? l32 : one); whi = cmul(wlo, lam8);
            zG = cmul((G & 1) ? l512 : one, (G & 2) ? l1024 : one);
            asm volatile("" : "+v"(lam), "+v"(coef), "+v"(lam8.r), "+v"(lam8.i), "+v"(lam16.r), "+v"(lam16.i), "+v"(lam64.r), "+v"(lam64.i),
                              "+v"(wlo.r), "+v"(wlo.i), "+v"(whi.r), "+v"(whi.i), "+v"(zG.r), "+v"(zG.i) :: "memory"); }
        const int rr = u < 3 ? r16 : r16 & 7, rg = (r32 & 3) + 4 * (r32 >> 3), tk = 32 * ((r32 >> 2) & 1) + (rg >> 1) + 8 * (rg & 1);
        const unsigned bro = (G & 1) * 7168 + u * 2048 + rr * 128 + ((hi ^ (rr >> 1)) << 4), ato = L_X + tk * 128 + ((hi ^ xswz(tk)) << 4);
        lp br[2][4], at[4];
        UNROLL for (int k = 0; k < 4; ++k) { br[0][k] = lbase(L, L_B0 + (bro ^ (k << 5))); br[1][k] = lbase(L, L_WK + (bro ^ (k << 5))); at[k] = lbase(L, ato ^ (k << 5)); }
        if (master) { _Pragma("unroll 1") for (unsigned spin = 0; lread(L, L_KD) < 1u && spin < SPIN_CAP; ++spin) { BUMP(); __builtin_amdgcn_s_sleep(8); } BUMP(); wait_vm<0>(); }
        if (B0EARLY) BD(0);
        lwait(L, L_KD, 1u);
        STAMP(1);
        if (!B0EARLY) BD(0);
        BD(1); BD(2);
        f32x16 ur = {}, ui = {};
        bf16x8 fb[2][4], fx[2][8];
#define BP(c, k) ((c) % 3 == 0 ? br[0][k] : br[1][k] + ((c) % 3 - 1) * 14336)
#define BRD(c) do { wait_vm<(c) < 5 ? 8 : 4 * (6 - (c))>(); UNROLL for (int k = 0; k < 4; ++k) fb[(c) & 1][k] = ld128(BP(c, k)); \
                    if ((c) < 4) { asm volatile("s_waitcnt lgkmcnt(0)" ::: "memory"); BD((c) + 3); } } while (0)
#define XRD(c, i) fx[(c) & 1][i] = ld128(at[(i) >> 1] + (c) * 8192 + ((i) & 1) * 2048)
#define UCH(c) UNROLL for (int i = 0; i < 8; ++i) { \
            if (i & 1) ui = MFMA32(fx[(c) & 1][i], fb[(c) & 1][i >> 1], ui); else ur = MFMA32(fx[(c) & 1][i], fb[(c) & 1][i >> 1], ur); \
            SBAR(); \
            if (i == BRI && (c) < 6) BRD((c) < 6 ? (c) + 1 : 6); \
            if ((c) < 6) XRD((c) + 1, i); \
            SBAR(); }
        BRD(0);
        lwait(L, L_XA, 4u);
        STAMP(2);
        UNROLL for (int i = 0; i < 8; ++i) XRD(0, i);
        SBAR();
        UCH(0) UCH(1) UCH(2)
        __builtin_amdgcn_s_setprio(UPRIO);
        UCH(3) UCH(4) UCH(5) UCH(6)
        if (TCW == 0) {
        if (u < 3) TOUCH(u == 0 ? wo : u == 1 ? cre : cim, DM * ROWB, lane < 25 ? (b * 25 + lane) * 64 : OOB);
        }
        lcount(L, L_UF, lane);
        UNROLL for (int r = 0; r < 16; ++r) {
            auto sw = __builtin_amdgcn_permlane16_swap(__float_as_uint(ur[r]), __float_as_uint(ui[r]), false, false); ur[r] = __uint_as_float(sw[0]); ui[r] = __uint_as_float(sw[1]); }
        STAMP(3);
        f32x2 hr, hm;
        UNROLL for (int p = 0; p < 8; ++p) {
            f32x2 tr, ti; pk_cmul(tr, ti, coef, PAIR(ur, p), PAIR(ui, p));
            ur[2 * p] = tr[0]; ur[2 * p + 1] = tr[1]; ui[2 * p] = ti[0]; ui[2 * p + 1] = ti[1];
            if (p == 0) { hr = tr; hm = ti; } else { f32x2 nr, ni; pk_cfma(nr, ni, lam, hr, hm, tr, ti); hr = nr; hm = ni; }
        }
        const cf elo = {hr[0], hm[0]}, e16 = cfma(lam8, elo, (cf){hr[1], hm[1]});
        cf e0, e1, e2, e3; bcast4(e16.r, e0.r, e1.r, e2.r, e3.r); bcast4(e16.i, e0.i, e1.i, e2.i, e3.i);
        const cf t2 = cfma(lam16, e0, e1), t3 = cfma(lam16, t2, e2), eb = cfma(lam16, t3, e3);
        lwait(L, L_KR, 1u); lwait(L, L_QF, 4u);
        __builtin_amdgcn_raw_buffer_store_b64((u32x2){nosent(__float_as_uint(eb.r)), nosent(__float_as_uint(eb.i))}, rsWS, eoff, WS_E + (blk * DM + 56 * j) * 8, SC1);
        if (TCW == 1) {
        if (u < 3) TOUCH(u == 0 ? wo : u == 1 ? cre : cim, DM * ROWB, lane < 25 ? (b * 25 + lane) * 64 : OOB);
        }
        const cf pg = {G == 0 ? 0.f : G == 1 ? e0.r : G == 2 ? t2.r : t3.r, G == 0 ? 0.f : G == 1 ? e0.i : G == 2 ? t2.i : t3.i}, ph = cfma(lam8, pg, elo);
        STAMP(4);
        asm volatile("s_mov_b32 m0, %0\n\ts_nop 0\n\tbuffer_load_dword %1, %2, %3 offen sc1 lds"
                     :: "s"(l0 + L_DUMMY), "v"(u == 0 ? lane * 128 : OOB), "s"(rsWS), "s"(WS_KV + (b >> 3) * 8192) : "memory");
        st128((u32x4){~0u, ~0u, ~0u, ~0u}, rsWS, l16, opiece, SC1);
        st128((u32x4){~0u, ~0u, ~0u, ~0u}, rsWS, l16, opiece + 1024, SC1);
        const float gt = *(LAS const float*)(L + L_G + (32 * th + r32) * 4), go = *(LAS const float*)(L + L_G + (32 * (th ^ 1) + r32) * 4);
        lwait(L, L_UF, 4u);
        const rsrc_t rsCR = mkrs(cre, DM * ROWB), rsCI = mkrs(cim, DM * ROWB);
        const unsigned vc = j < 7 ? (64 * j + 8 * u + r8) * ROWB + ((sl ^ ((4 * u + (r8 >> 1)) & 7)) << 4) : OOB;
#define CD(n) dma(((n) & 1) ? rsCI : rsCR, vc, ((n) >> 2) * 128 + ((n) & 2) * 16 * ROWB, l0 + ((n) & 1) * L_CIM + ((n) >> 2) * 8192 + (u + 2 * ((n) & 2)) * 1024)
        const rsrc_t rsE = mkrs(ws + WS_E + 56 * j * 8, 32 * DM * 8 - 56 * j * 8);
        const int ebase = (blk - 1 - 8 * G) * (DM * 8) + st * 8;
        const unsigned hoff = lane == 0 ? WS_HCNT + blk * 128 : OOB;
        UNROLL for (int n = 0; n < ELDN; ++n) { CD(n); PACE(); }
        STAMP(38);
        {
            u32x2 ev[8];
            UNROLL for (int m = 0; m < 8; m += 2)
                asm volatile("buffer_load_dwordx2 %0, %2, %4, 0 offen sc1\n\tbuffer_load_dwordx2 %1, %3, %4, 0 offen sc1"
                             : "=&v"(ev[m]), "=&v"(ev[m + 1]) : "v"(ebase - m * (DM * 8)), "v"(ebase - (m + 1) * (DM * 8)), "s"(rsE) : "memory");
            UNROLL for (int n = ELDN; n < ELDN + 5; ++n) { PACE(); CD(n); }
            asm volatile("s_waitcnt vmcnt(5)" : "+v"(ev[0]), "+v"(ev[1]), "+v"(ev[2]), "+v"(ev[3]), "+v"(ev[4]), "+v"(ev[5]), "+v"(ev[6]), "+v"(ev[7]) :: "memory");
            _Pragma("unroll 1") for (unsigned spin = 0; spin < SPIN_CAP; ++spin) {
                unsigned mx = 0;
                UNROLL for (int m = 0; m < 8; ++m) mx = max(mx, max(ev[m][0], ev[m][1]));
                if (__builtin_expect(__builtin_amdgcn_ballot_w64(mx == ~0u) == 0, 1)) break;
                __builtin_amdgcn_s_sleep(8);
                asm volatile("" ::: "memory");
                UNROLL for (int m = 0; m < 8; ++m) ev[m] = __builtin_amdgcn_raw_buffer_load_b64(rsE, ebase - m * (DM * 8), 0, SC1);
            }
            STAMP(39);
            cf part = {__uint_as_float(ev[7][0]), __uint_as_float(ev[7][1])};
            UNROLL for (int m = 6; m >= 0; --m) part = cfma(lam64, part, (cf){__uint_as_float(ev[m][0]), __uint_as_float(ev[m][1])});
            part = cmul(zG, part);
            if (ebase < 0) part = (cf){0.f, 0.f};
            PIN(part.r); SBAR(); CD(ELDN + 5); SBAR();
            part.r = sum4(part.r); part.i = sum4(part.i);
            const cf ilo = cfma(wlo, part, pg), ihi = cfma(whi, part, ph);
            hr = (f32x2){ilo.r, ihi.r}; hm = (f32x2){ilo.i, ihi.i};
            u32x4 pr0, pr1, pi0, pi1;
            UNROLL for (int p = 0; p < 8; ++p) {
                f32x2 nr, ni; pk_cfma(nr, ni, lam, hr, hm, PAIR(ur, p), PAIR(ui, p)); hr = nr; hm = ni;
                unsigned cr, ci; asm("v_cvt_pk_bf16_f32 %0, %2, %3\n\tv_cvt_pk_bf16_f32 %1, -%4, -%5" : "=&v"(cr), "=&v"(ci) : "v"(hr[0]), "v"(hr[1]), "v"(hm[0]), "v"(hm[1]));
                if (p < 4) { pr0[p & 3] = cr; pi0[p & 3] = ci; } else { pr1[p & 3] = cr; pi1[p & 3] = ci; }
                if (p == 1 || p == 5) { PIN(hr); SBAR(); CD(ELDN + 6 + (p >> 2)); SBAR(); }
            }
            const unsigned sb = L_HS + u * 4096, wa = sb + r16 * 128 + (((2 * G) ^ (r16 & 7)) << 4);
            const int tok = (lane & 48) + (r16 >> 1) + 8 * (lane & 1);
            const unsigned gha = (tok >> 5) * 57344 + (tok & 31) * 16, ghb = u < 3 ? gha + 1024 : OOB, gs = WS_H + blk * 114688 + (7 * j + 2 * u) * 1024;
            const unsigned rao = sb + tq * 128 + (((2 * G + (tp >> 1)) ^ tq) << 4) + 8 * (tp & 1);
            const lp wa0 = lbase(L, wa), wa1 = lbase(L, wa ^ 16), ra = lbase(L, rao), rb = lbase(L, (rao ^ 64) + 512);
            *(LAS u32x4*)(wa0) = pr0; *(LAS u32x4*)(wa1) = pr1; *(LAS u32x4*)(wa0 + 2048) = pi0; *(LAS u32x4*)(wa1 + 2048) = pi1;
            asm volatile("" ::: "memory");
            UNROLL for (int pl = 0; pl < 2; ++pl) {
                const bf16x8 c0 = ldtr2(ra + pl * 2048, rb + pl * 2048), c1 = ldtr2(ra + pl * 2048 + 1024, rb + pl * 2048 + 1024);
                st128(__builtin_bit_cast(u32x4, c0), rsWS, gha + pl * 512, gs, SC1);
                st128(__builtin_bit_cast(u32x4, c1), rsWS, ghb + pl * 512, gs, SC1);
            }
        }
        STAMP(5);
        lcount(L, L_SF, lane);
        UNROLL for (int n = ELDN + 8; n < 28; ++n) { PACE(); CD(n); }
        wait_vm<28 - ELDN - 8>();
        atomic_inc(rsWS, hoff);
        if (j == 7) { ladd(L, L_UF, 1u); EXIT(); }
        lwait(L, L_SF, 8u);
        const rsrc_t rsWO = mkrs(wo, DM * ROWB);
        const unsigned vwo = r8 * ROWB + 128 * j + (pwq << 4);
        const unsigned cx = (32 * (G & 1) + 8 * tp) ^ (((tq >> 1) & 1) << 6), nb = L_WO + u * 7168 + tq * 128, wa_ = nb + hi * 512;
        const lp wa0_ = lbase(L, wa_ + cx), wa1_ = lbase(L, wa_ + (cx ^ 64)), nb0 = lbase(L, nb + cx), nb1 = lbase(L, nb + (cx ^ 64));
        const unsigned hb = WS_H + blk * 114688 + u * 1024;
        u32x4 h0[14], h1[14];
        bf16x8 wf0[8], wf1[8];
#define WOD(p) UNROLL for (int n = 0; n < 7; ++n) { dma(rsWO, vwo, (u * 14 + 7 * (p) + n) * 8 * ROWB, l0 + L_WO + u * 7168 + n * 1024); \
            if ((p) == 0 && n == 3) asm volatile("buffer_atomic_add %0, %1, %2, 0 offen sc0 sc1" : "+v"(hc) : "v"(hoff), "s"(rsWS) : "memory"); PACE(); }
#define WOR(p) UNROLL for (int k = 0; k < 4; ++k) { const unsigned ro = (28 * (k >> 1) + 16 * (k & 1)) * 128; \
            wf0[4 * (p) + k] = ldtr2(wa0_ + ro, ((k & 1) ? nb0 : wa0_) + ro + 1024); wf1[4 * (p) + k] = ldtr2(wa1_ + ro, ((k & 1) ? nb1 : wa1_) + ro + 1024); }
#define HLD0(m0, m1) UNROLL for (int m = (m0); m < (m1); ++m) { h0[m] = __builtin_amdgcn_raw_buffer_load_b128(rsWS, l16, hb + th * 57344 + m * 4096, SC1); PACE(); }
        unsigned hc = 0u;
        WOD(0);
        STAMP(40);
        asm volatile("s_waitcnt vmcnt(3)" : "+v"(hc) :: "memory");
        lcount(L, L_WF, lane);
        if (__builtin_expect(__builtin_amdgcn_readfirstlane(hc) != 32, 0)) {
            _Pragma("unroll 1") for (unsigned spin = 0; __builtin_amdgcn_readfirstlane(atomic_read(rsWS, hoff)) != 32 && spin < SPIN_CAP; ++spin) __builtin_amdgcn_s_sleep(8); }
        HLD0(0, 3);
        wait_vm<3>();
        WOR(0);
        asm volatile("s_waitcnt lgkmcnt(0)" ::: "memory");
        WOD(1);
        HLD0(3, 6);
        wait_vm<3>();
        WOR(1);
        lcount(L, L_UF, lane);
        STAMP(42);
        HLD0(6, HP);
        lwait_idle(L, L_QL, 4u);
        UNROLL for (int m = HP; m < 14; ++m) h0[m] = __builtin_amdgcn_raw_buffer_load_b128(rsWS, l16, hb + th * 57344 + m * 4096, SC1);
        UNROLL for (int m = 0; m < 14; ++m) h1[m] = __builtin_amdgcn_raw_buffer_load_b128(rsWS, l16, hb + (th ^ 1) * 57344 + m * 4096, SC1);
        const unsigned cao = hi * L_CIM + r32 * 128 + ((u ^ ((r32 >> 1) & 7)) << 4);
        const lp ca0 = lbase(L, cao), ca1 = lbase(L, cao ^ 64);
        unsigned rp = l0 + L_RED + th * 16384 + u * 4096 + (l16 ^ (hi << 4)); PIN(rp);
        const unsigned oa = WS_O + blk * 65536 + (th * 16 + 4 * u) * 2048, ob = WS_O + blk * 65536 + ((th ^ 1) * 16 + 4 * u) * 2048;
        f32x2 s2 = {2.0f * (1.0f - gt), 0.f}, s3 = {2.0f * (1.0f - go), 0.f}; PIN(s2); PIN(s3);
        lwait(L, L_WF, 4u);
        STAMP(41);
        __builtin_amdgcn_s_setprio(YPRIO);
        lwait_idle(L, L_QE, 4u);
        __builtin_amdgcn_s_sleep(YSLEEP);
        STAMP(15);
        f32x16 y[4]; y[0] = y[1] = y[2] = y[3] = (f32x16){};
        u32x4 of[16];
#define OLD(k0) UNROLL for (int k = (k0); k < (k0) + 8; ++k) of[k] = __builtin_amdgcn_raw_buffer_load_b128(rsWS, l16 + (k & 3) * 1024, (k < 8 ? oa : ob) + ((k >> 2) & 1) * 4096, SC1)
        {   bf16x8 fa[28], fc[28];
            UNROLL for (int m = -YD; m < 28; ++m) {
                if (m + YD < 28) { const int n = (m + YD) % 14; fa[m + YD] = ld128(((n & 1) ? ca1 : ca0) + (n >> 1) * 8192); fc[m + YD] = ld128(((n & 1) ? ca1 : ca0) + (n >> 1) * 8192 + 4096); }
                if (m >= 0 && m < 14) { y[0] = MFMA32(fa[m], asbf(h0[m]), y[0]); y[1] = MFMA32(fc[m], asbf(h0[m]), y[1]); }
                if (m >= 14) { y[2] = MFMA32(fa[m], asbf(h1[m - 14]), y[2]); y[3] = MFMA32(fc[m], asbf(h1[m - 14]), y[3]); }
                if (m >= 16 && m < 24) { UNROLL for (int k = 0; k < 2; ++k) { const f32x2 a = pk_scale(s2, (f32x2){y[k][2 * (m - 16)], y[k][2 * (m - 16) + 1]}); y[k][2 * (m - 16)] = a[0]; y[k][2 * (m - 16) + 1] = a[1]; } PIN(y[0]); PIN(y[1]); }
                if (OM < 28 && m == OM) { lwait(L, L_OD, ODN); if (OSLEEP) __builtin_amdgcn_s_sleep(OSLEEP); STAMP(43); OLD(0); }
                if (OM < 28 && m == (OM + 2 < 27 ? OM + 2 : 27)) { OLD(8); }
                if (m == 26) ladd(L, L_YF, 1u);
                SBAR();
            } }
        STAMP(9);
        lwait(L, L_YF, 4u);
        asm volatile("s_nop 11" : "+v"(y[2]), "+v"(y[3]));
        UNROLL for (int k = 2; k < 4; ++k) UNROLL for (int e = 0; e < 8; ++e) { const f32x2 a = pk_scale(s3, (f32x2){y[k][2 * e], y[k][2 * e + 1]}); y[k][2 * e] = a[0]; y[k][2 * e + 1] = a[1]; }
        asm volatile("s_nop 1" : "+v"(y[2]), "+v"(y[3]));
        if (OM >= 28) { lwait(L, L_OD, ODN); if (OSLEEP) __builtin_amdgcn_s_sleep(OSLEEP); STAMP(43); OLD(0); OLD(8); }
#define OMAX(mx, k) mx = max(max(mx, max(of[k][0], of[k][1])), max(of[k][2], of[k][3]))
#define OFIX(mx, k0) do { if (__builtin_expect(__builtin_amdgcn_ballot_w64(mx == ~0u) != 0, 0)) { _Pragma("unroll 1") for (unsigned spin = 0; spin < SPIN_CAP; ++spin) { \
            asm volatile("" ::: "memory");                              \
            OLD(k0); mx = 0; UNROLL for (int k = (k0); k < (k0) + 8; ++k) OMAX(mx, k); \
            if (__builtin_amdgcn_ballot_w64(mx == ~0u) == 0) break; } } } while (0)
        SBAR();
        unsigned mxa = 0, mxb = 0;
        UNROLL for (int k = 0; k < 8; ++k) OMAX(mxa, k);
        OFIX(mxa, 0);
        STAMP(10);
#define REDW(k, Y, e) *(LAS f32x4*)((lp)(uintptr_t)(rp ^ (((k) >> 1) * 16384 + (8 * ((k) & 1) + 2 * (e)) * 16)) + ((k) & 1) * 32768 + (e) * 1024) = (f32x4){Y[4 * (e)], Y[4 * (e) + 1], Y[4 * (e) + 2], Y[4 * (e) + 3]}
        UNROLL for (int k = 0; k < 8; ++k) { y[0] = MFMA32(wf0[k], asbf(of[k]), y[0]); SBAR(); OMAX(mxb, 8 + k); PIN(mxb); SBAR(); }
        UNROLL for (int k = 0; k < 8; ++k) { y[1] = MFMA32(wf1[k], asbf(of[k]), y[1]); SBAR(); if (k >= 3 && k < 7) REDW(0, y[0], k - 3); SBAR(); }
        OFIX(mxb, 8);
        UNROLL for (int k = 0; k < 8; ++k) { y[2] = MFMA32(wf0[k], asbf(of[8 + k]), y[2]); SBAR(); if (k >= 3 && k < 7) REDW(1, y[1], k - 3); SBAR(); }
        UNROLL for (int k = 0; k < 8; ++k) { y[3] = MFMA32(wf1[k], asbf(of[8 + k]), y[3]); SBAR(); if (k >= 3 && k < 7) REDW(2, y[2], k - 3); SBAR(); }
        UNROLL for (int e = 0; e < 4; ++e) REDW(3, y[3], e);
        lds_bar();
    }
    STAMP(11);
}

extern "C" void kernel_launch(void* const* d_in, const int* in_sizes, int n_in, void* d_out, int out_size, void* d_ws, size_t ws_size,
                              hipStream_t stream) {
    const uint16_t* const* in = (const uint16_t* const*)d_in;
    gated_mix_kernel<<<256, 512, 0, stream>>>(in[0], in[1], in[2], in[3], in[4], in[6], in[7], in[8], in[9], in[10], in[11], in[12], in[13], in[14],
                                              (float*)d_out, (char*)d_ws);
}
```
